```python
import torch

def _softmax(x, axis=-1):
    return torch.softmax(x, dim=axis)
def _gelu(x):
    return torch.nn.functional.gelu(x, approximate="tanh")
def _silu(x):
    return torch.nn.functional.silu(x)
def _sigmoid(x):
    return torch.sigmoid(x)
def _layernorm(x, axis=-1, eps=1e-5):
    m = x.mean(dim=axis, keepdim=True)
    v = x.var(dim=axis, keepdim=True, unbiased=False)
    return (x - m) / torch.sqrt(v + eps)
def _scatter_add(tgt, idx, src, axis=0):
    return tgt.clone().index_add_(axis, idx, src)
def _segment_sum(src, seg, n):
    out = torch.zeros((n,) + src.shape[1:], dtype=src.dtype)
    return out.index_add_(0, seg, src)
def _pad(x, pw):
    flat = tuple(v for pair in reversed(pw) for v in pair)
    return torch.nn.functional.pad(x, flat)

kernel_name = 'recurrent_scan__sliding_local_dilated__glu_gated_geglu__prompt'

def setup_inputs():

    _w = (lambda a: (lambda t: t.to(torch.bfloat16) if t.is_floating_point() else t)(torch.as_tensor(a)))
    def _A_setup():
        B, S, D = 1, 2048, 1024
        return {
            "x": _w((lambda *s: torch.randn(*s, dtype=torch.bfloat16) * 0.05)(B, S, D)),
            "wqkv": _w((lambda *s: torch.randn(*s, dtype=torch.bfloat16) * 0.2)(D, 3 * D)),
            "wo": _w((lambda *s: torch.randn(*s, dtype=torch.bfloat16) * 0.2)(D, D)),
        }
    _A_in = _A_setup()

    def _B_setup():
        B, S, Dm, Df = 1, 2048, 1024, 3584
        return {
            "x": _w((lambda *s: torch.randn(*s, dtype=torch.bfloat16) * 0.05)(B, S, Dm)),
            "w_gate": _w((lambda *s: torch.randn(*s, dtype=torch.bfloat16) * 0.1)(Dm, Df) * (Dm ** -0.5)),
            "w_up": _w((lambda *s: torch.randn(*s, dtype=torch.bfloat16) * 0.1)(Dm, Df) * (Dm ** -0.5)),
            "w_down": _w((lambda *s: torch.randn(*s, dtype=torch.bfloat16) * 0.1)(Df, Dm) * (Df ** -0.5)),
        }
    _B_in = _B_setup()

    return {
        "A_x": _A_in["x"],
        "A_wqkv": _A_in["wqkv"],
        "A_wo": _A_in["wo"],
        "B_x": _B_in["x"],
        "B_w_gate": _B_in["w_gate"],
        "B_w_up": _B_in["w_up"],
        "B_w_down": _B_in["w_down"],
    }

def reference(A_x, A_wqkv, A_wo, B_x, B_w_gate, B_w_up, B_w_down):
    def _A_ref(x, wqkv, wo):
        B, S, D = x.shape
        H = 32; Dh = D // H
        W, dil = 8, 2
        qkv = (lambda x, *d: x.permute(*d))((x @ wqkv).reshape(B, S, 3, H, Dh), 2, 0, 3, 1, 4)
        q, k, v = qkv[0], qkv[1], qkv[2]
        scale = Dh ** -0.5
        pos = torch.arange(S)
        off = torch.arange(W) * dil
        idx = pos[:, None] - off[None, :]
        mask = idx >= 0
        idx_c = torch.where(mask, idx, 0).reshape(-1)
        kg = k[:, :, idx_c, :].reshape(B, H, S, W, Dh)
        vg = v[:, :, idx_c, :].reshape(B, H, S, W, Dh)
        logits = torch.einsum("bhsd,bhswd->bhsw", q, kg) * scale
        logits = torch.where(mask[None, None, :, :], logits, -1e9)
        attn = _softmax(logits, axis=-1)
        o = torch.einsum("bhsw,bhswd->bhsd", attn, vg)
        return (lambda x, *d: x.permute(*d))(o, 0, 2, 1, 3).reshape(B, S, D) @ wo

    def _B_ref(x, w_gate, w_up, w_down):
        g = torch.einsum("bsd,df->bsf", x, w_gate)
        u = torch.einsum("bsd,df->bsf", x, w_up)
        h = _gelu(g) * u
        return torch.einsum("bsf,fd->bsd", h, w_down)

    x = A_x
    xn = _layernorm(x, axis=-1)
    s = _A_ref(xn, A_wqkv, A_wo)
    g = _silu(_B_ref(xn, B_w_gate, B_w_up, B_w_down))
    return x + s * g
```

```cpp
#include <hip/hip_runtime.h>
#include <hip/hip_bf16.h>
#include <stdint.h>
#include <type_traits>

typedef unsigned short u16;
using bf16x8 = __attribute__((ext_vector_type(8))) short;
using bf16x4 = __attribute__((ext_vector_type(4))) short;
using f32x4 = __attribute__((ext_vector_type(4))) float;
using u32x2 = __attribute__((ext_vector_type(2))) unsigned;
using u32x4 = __attribute__((ext_vector_type(4))) unsigned;
typedef __attribute__((address_space(3))) const char* lds_cptr;

constexpr int S_ = 2048, D_ = 1024, F_ = 3584;

__device__ __forceinline__ float bf2f(u16 h) { return __uint_as_float((unsigned)h << 16); }
__device__ __forceinline__ u16 f2bf(float f) {
  unsigned u = __float_as_uint(f);
  u += 0x7fffu + ((u >> 16) & 1u);
  return (u16)(u >> 16);
}
__device__ __forceinline__ float rbf(float f) { return bf2f(f2bf(f)); }
typedef __attribute__((ext_vector_type(2))) __bf16 bf16x2_t;
typedef __attribute__((ext_vector_type(2))) float f32x2;
__device__ __forceinline__ unsigned pack2(float a, float b) {
  bf16x2_t r = __builtin_convertvector((f32x2){a, b}, bf16x2_t);
  return __builtin_bit_cast(unsigned, r);
}
__device__ __forceinline__ float lo_f(unsigned p) { return __uint_as_float(p << 16); }
__device__ __forceinline__ float hi_f(unsigned p) { return __uint_as_float(p & 0xffff0000u); }

#define TSDECL
#define TSS(i) do {} while (0)
#define TSFLUSH() do {} while (0)
#define TS1(i) TSS(i)
#define TS(i) TSS(8 + (i))
#define WAIT_V(n) asm volatile("s_waitcnt vmcnt(%0)" ::"n"(n) : "memory")
#define WAIT_L(n) asm volatile("s_waitcnt lgkmcnt(%0)" ::"n"(n) : "memory")
#define WS(n) asm volatile("%0:\n\ts_cbranch_execz %0f" :: "n"((n) + 10 * grp))
#define BARL() do { WAIT_L(0); BAR(); } while (0)
#define BAR() do { asm volatile("" ::: "memory"); __builtin_amdgcn_s_barrier(); asm volatile("" ::: "memory"); } while (0)

__device__ __forceinline__ bf16x4 tr_read(lds_cptr p) {
  return __builtin_amdgcn_ds_read_tr16_b64_v4i16((__attribute__((address_space(3))) bf16x4*)p);
}
__device__ __forceinline__ bf16x8 tr_frag(lds_cptr p) {
  bf16x4 lo = tr_read(p), hi = tr_read(p + 512);
  bf16x8 r;
  r[0] = lo[0]; r[1] = lo[1]; r[2] = lo[2]; r[3] = lo[3];
  r[4] = hi[0]; r[5] = hi[1]; r[6] = hi[2]; r[7] = hi[3];
  return r;
}
__device__ __forceinline__ void glds16(const void* gsrc, unsigned lds_dst) {
  unsigned keep;
  asm volatile("s_mov_b32 %0, m0\n\ts_mov_b32 m0, %2\n\ts_nop 0\n\tglobal_load_lds_dwordx4 %1, off\n\ts_mov_b32 m0, %0"
               : "=&s"(keep) : "v"(gsrc), "s"(lds_dst) : "memory");
}
#define GLDS(gptr, laddr) glds16((const void*)(gptr), (unsigned)(laddr))
__device__ __forceinline__ void glds16_sc1(const void* gsrc, unsigned lds_dst) {
  unsigned keep;
  asm volatile("s_mov_b32 %0, m0\n\ts_mov_b32 m0, %2\n\ts_nop 0\n\tglobal_load_lds_dwordx4 %1, off sc1\n\ts_mov_b32 m0, %0"
               : "=&s"(keep) : "v"(gsrc), "s"(lds_dst) : "memory");
}
#define GLDS_SC1(gptr, laddr) glds16_sc1((const void*)(gptr), (unsigned)(laddr))
__device__ __forceinline__ void glds16_nc(const void* gsrc, unsigned lds_dst) {
  unsigned keep;
  asm volatile("s_mov_b32 %0, m0\n\ts_mov_b32 m0, %2\n\ts_nop 0\n\tglobal_load_lds_dwordx4 %1, off\n\ts_mov_b32 m0, %0"
               : "=&s"(keep) : "v"(gsrc), "s"(lds_dst));
}
#define GLDS_NC(gptr, laddr) glds16_nc((const void*)(gptr), (unsigned)(laddr))
__device__ __forceinline__ void glds16_s(const void* sbase, unsigned voff, unsigned lds_dst) {
  asm volatile("s_mov_b32 m0, %2\n\ts_nop 0\n\tglobal_load_lds_dwordx4 %0, %1" :: "v"(voff), "s"(sbase), "s"(lds_dst) : "m0");
}

typedef unsigned long long u64;
__device__ __forceinline__ bool fresh(u64 stamp, u64 t_own) {
  return stamp - t_own - 100ull < __builtin_amdgcn_s_memrealtime() - t_own;
}
constexpr u64 TIMEOUT_ = 200000;
#define GIVEUP() (__builtin_amdgcn_s_memrealtime() - t_own > 100000000ull)
__device__ __forceinline__ float half_sum(float v) {
  v += __builtin_bit_cast(float, __builtin_amdgcn_update_dpp(0, __builtin_bit_cast(int, v), 0xB1, 0xf, 0xf, false));
  v += __builtin_bit_cast(float, __builtin_amdgcn_update_dpp(0, __builtin_bit_cast(int, v), 0x4E, 0xf, 0xf, false));
  v += __builtin_bit_cast(float, __builtin_amdgcn_update_dpp(0, __builtin_bit_cast(int, v), 0x141, 0xf, 0xf, false));
  v += __builtin_bit_cast(float, __builtin_amdgcn_update_dpp(0, __builtin_bit_cast(int, v), 0x140, 0xf, 0xf, false));
  { auto r = __builtin_amdgcn_permlane16_swap(__float_as_uint(v), __float_as_uint(v), false, false); v = __uint_as_float(r[0]) + __uint_as_float(r[1]); }
  return v;
}
__device__ __forceinline__ unsigned pack_stats(float mean, float var) {
  return (unsigned)f2bf(mean) | ((unsigned)f2bf(sqrtf(rbf(rbf(var) + 1e-5f))) << 16);
}
__device__ __forceinline__ unsigned row_stats_serial(const u16* xr) {
  float s = 0.f, q = 0.f;
#pragma unroll 1
  for (int k = 0; k < D_; ++k) s += bf2f(xr[k]);
  const float mean = s * (1.0f / D_);
#pragma unroll 1
  for (int k = 0; k < D_; ++k) { const float d = bf2f(xr[k]) - mean; q += d * d; }
  return pack_stats(mean, q * (1.0f / D_));
}
constexpr int G1_STAGE = 32768 + 5 * 8192;
#define PF_K0 4
#define CP_K0 5
#define DLT 1
#define PSLEEP 2
#define SD 6
#define NSC 2
#define PRE 3
#define PRB 0
#define PRL0 3
#define PRL1 1
#define NWPF 4
#define SNP 3
#define PUB_K 4

#define RLX_AGENT __ATOMIC_RELAXED, __HIP_MEMORY_SCOPE_AGENT

constexpr int G2_STAGE = 16384 + 2 * 8192;
constexpr int G2_NSTAGE = 5;
constexpr size_t MB_ = 1024 * 1024;
__global__ __launch_bounds__(512, 2) void fused_kernel(const u16* __restrict__ x, const u16* __restrict__ wqkv,
                                                       const u16* __restrict__ wg, const u16* __restrict__ wu,
                                                       const u16* __restrict__ wd, const u16* __restrict__ wo,
                                                       u16* __restrict__ out, char* __restrict__ ws) {
  u16* const halo = (u16*)(ws + 4 * MB_);
  u16* const abuf = (u16*)(ws + 16 * MB_);
  char* const slab = ws + 64 * MB_;
  unsigned* const flags = (unsigned*)(ws + 96 * MB_);
  u32x4* const rstat = (u32x4*)(ws + 97 * MB_);
  char* const wcopy = ws + 98 * MB_;
  u64* const done1 = (u64*)(ws + 100 * MB_);
  u64* const done2 = (u64*)(ws + 100 * MB_ + 131072);
  u64* const donec = (u64*)(ws + 100 * MB_ + 196608);
  u64* const acks = (u64*)(ws + 100 * MB_ + 262144);
  u64* const cstamp = (u64*)(ws + 100 * MB_ + 327680);
  float* const csum = (float*)(ws + 101 * MB_);
  __shared__ __attribute__((aligned(1024))) char shm[163840];
  constexpr int AUX = 2 * G1_STAGE + 8192;
  {
    u64 pc; int len;
    asm volatile("s_getpc_b64 %0\n.Lcode_beg:\n\ts_mov_b32 %1, .Lcode_end-.Lcode_beg" : "=s"(pc), "=s"(len));
    const int ol = ((blockIdx.x >> 3) + 32 * (threadIdx.x >> 6)) * 1024 + (threadIdx.x & 63) * 16;
    asm volatile("s_mov_b32 m0, %2\n\ts_nop 0\n\tglobal_load_lds_dwordx4 %0, %1"
                 :: "v"(ol < len - 16 ? ol : 0), "s"(pc & ~15ull), "s"((unsigned)(uintptr_t)(lds_cptr)shm + 2 * G1_STAGE + 7168) : "m0");
    __builtin_amdgcn_sched_barrier(0);
  }
  const u64 t_own = __builtin_amdgcn_s_memrealtime();
  TSDECL;
  TS1(7);
  const int tid = threadIdx.x, lane = tid & 63;
  const int wid = __builtin_amdgcn_readfirstlane(tid >> 6);
  const int bid = blockIdx.x, xcd = bid & 7, idx = bid >> 3;
  const int ntile = xcd * 4 + (idx & 3), mtile = idx >> 2;
  const int m0 = mtile * 256;
  const int hg = ntile >> 1;
  const bool tya = (ntile & 1) == 0;
  const int grp = wid >> 2, wc = (wid >> 1) & 1, wm = (((grp ^ ntile ^ 1) & 1) << 1) | (wid & 1), vid = wc * 4 + wm;
  const int srow = m0 + ntile * 8 + (wid & 3) * 2;
  lds_cptr shm3 = (lds_cptr)shm;
  const unsigned shm_base = (unsigned)(uintptr_t)shm3;
  const int r8 = tid >> 3, pos = tid & 7;
  const int phys = pos >> 1, half = pos & 1;
  const int fk = ((r8 >> 1) & 1) | (((r8 >> 3) & 1) << 1);
  const int vsrc = phys ^ fk;
  const int qstr = tya ? 3072 * 2 : F_ * 2;
  unsigned voa = (((r8 & 32) + 2 * (r8 & 15) + ((r8 >> 4) & 1)) * D_ + ((pos ^ (r8 & 7)) * 8)) * 2;
  unsigned vog = r8 * (F_ * 2) + vsrc * 32 + half * 16, voq = r8 * qstr + vsrc * 32 + half * 16;
  unsigned vom = r8 * qstr + (vsrc >> (tya ? 0 : 1)) * 32 + half * 16, vol = lane * 16;
  asm volatile("" : "+v"(voa), "+v"(vog), "+v"(voq), "+v"(vom), "+v"(vol));
  asm volatile("" :: "s"(x), "s"(wqkv), "s"(wg), "s"(wu), "s"(wd), "s"(wo), "s"(out), "s"(ws));
  const char* const ab = (const char*)(x + (size_t)m0 * D_);
  int kz = 0;
  asm volatile("" : "+s"(kz));
  auto stage_a = [&](int buf, int kt, int i, int h) {
    glds16_s(ab + (long)(((i * 64 + (h - 1) * 32) * D_ + kt * 64) * 2), voa, shm_base + buf * G1_STAGE + (wid - 4 + 4 * h) * 1024 + i * 8192);
  };
  if (wid >= 4) {
#pragma unroll
    for (int i = 0; i < 8; ++i) stage_a(0, kz, i >> 1, i & 1);
  } else {
#pragma unroll
    for (int i = 0; i < 4; ++i) glds16_s(x + (size_t)srow * D_ + i * 512, vol, shm_base + G1_STAGE + wid * 4096 + i * 1024);
  }
  const int kt0 = (hg >> 1) * 7, he = hg & 1;
  const char* const gb = (const char*)(wg + (kt0 + (tya ? 5 + he : 2 + he)) * 64);
  const char* const qb = tya ? (const char*)(wqkv + 64 * hg) : gb - 256;
  const long dgu = (const char*)wu - (const char*)wg, dp1 = tya ? 2048 : dgu;
  const char* const mb0 = tya ? qb + 4096 : (const char*)(wg + (kt0 + 4) * 64 + 32 * he);
  const char* mptr = mb0 + (((vsrc & 1) && !tya) ? dgu : 0) + vom;
  auto wload = [&](int p, int rows, unsigned dst) {
    long off = (long)(rows * (p < 3 ? qstr : F_ * 2)) + (p == 1 ? dp1 : (p == 4 ? dgu : 0));
    if (p == 2) { asm volatile("" : "+s"(off)); GLDS_NC(mptr + off, dst); }
    else glds16_s((p < 2 ? qb : gb) + off, p < 2 ? voq : vog, dst);
  };
  const int fr = lane & 15, fq = lane >> 4, q4 = fr >> 2, pp = lane & 3;
  const int ff = ((q4 >> 1) & 1) | ((fq & 1) << 1);
  int aoff[2], boff[2];
#pragma unroll
  for (int ks = 0; ks < 2; ++ks) aoff[ks] = (wm * 64 + fr) * 128 + (((ks * 4 + fq) ^ (fr & 7)) * 16);
#pragma unroll
  for (int j = 0; j < 2; ++j) boff[j] = 32768 + (8 * fq + q4) * 128 + (((2 * wc + j) ^ ff) * 32) + pp * 8;

  f32x4 acc[4][10];
#pragma unroll
  for (int a = 0; a < 4; ++a)
#pragma unroll
    for (int b = 0; b < 10; ++b) acc[a][b] = f32x4{0.f, 0.f, 0.f, 0.f};

  auto stage_part = [&](int buf, int kt, int i, int h) {
    if (i < 5) wload(i, kt * 64 + (h - 1) * 32, shm_base + buf * G1_STAGE + (wid - 4 + 4 * h) * 1024 + 32768 + i * 8192);
    else stage_a(buf, kt, i - 5, h);
  };
  constexpr int NT = D_ / 64;
  constexpr int DMP = 2 * G1_STAGE;
  constexpr int CF = AUX + 4096, QS = CF + 1280, CNT = CF + 3840;
  if (tid < 8) *(unsigned*)(shm + CNT + tid * 4) = 0u;
  bf16x8 af0[4], af1[4];
  bf16x4 bl[4], bh[4];
#pragma unroll
  for (int i = 0; i < 4; ++i) { af1[i] = bf16x8{0, 0, 0, 0, 0, 0, 0, 0}; bl[i] = bf16x4{0, 0, 0, 0}; bh[i] = bf16x4{0, 0, 0, 0}; }
#define SB0() __builtin_amdgcn_sched_barrier(0)
  auto mm = [&](int g, int mf, bool tr) {
    const bf16x8 b = __builtin_shufflevector(bl[g & 3], bh[g & 3], 0, 1, 2, 3, 4, 5, 6, 7);
    if (tr && ((g % 10) >> 1) == 2) acc[mf][g % 10] = __builtin_amdgcn_mfma_f32_16x16x32_bf16(g < 10 ? af0[mf] : af1[mf], b, acc[mf][g % 10], 0, 0, 0);
    else acc[mf][g % 10] = __builtin_amdgcn_mfma_f32_16x16x32_bf16(b, g < 10 ? af0[mf] : af1[mf], acc[mf][g % 10], 0, 0, 0);
  };
  auto correct = [&](auto gc) {
    constexpr int grp = decltype(gc)::value;
    int l = threadIdx.x & 63;
    WS(41);
    asm volatile("s_nop 7" : "+v"(l));
    const float* const tb = (const float*)(shm + DMP + wm * 2048);
    const float* const cf = (const float*)(shm + CF) + wc * 160;
    auto ld = [&](int f) { return f < 8 ? *(const f32x4*)(cf + (f < 4 ? f : f + 2) * 16 + (l >> 4) * 4) : *(const f32x4*)(tb + (f - 8) * 16 + (l >> 4) * 4); };
    float m[4];
    f32x4 cr[3];
#pragma unroll
    for (int mf = 0; mf < 4; ++mf) m[mf] = tb[mf * 16 + (l & 15)];
    cr[0] = ld(0);
    cr[1] = ld(1);
    const float c4 = cf[64 + (l & 15)], c5 = cf[80 + (l & 15)];
#pragma unroll
    for (int f = 0; f < 12; ++f) {
      if (f + 2 < 12) cr[(f + 2) % 3] = ld(f + 2);
      if (f == 6) WS(41);
      SB0();
      const f32x4 c = cr[f % 3];
#pragma unroll
      for (int i = 0; i < (f < 8 ? 4 : 2); ++i) {
        f32x4& a = f < 8 ? acc[i][f < 4 ? f : f + 2] : acc[f - 8][4 + i];
        float a0 = a[0], a1 = a[1], a2 = a[2], a3 = a[3];
        asm volatile("v_fmac_f32 %0, %4, %5\n\tv_fmac_f32 %1, %4, %6\n\tv_fmac_f32 %2, %4, %7\n\tv_fmac_f32 %3, %4, %8"
                     : "+v"(a0), "+v"(a1), "+v"(a2), "+v"(a3) : "v"(f < 8 ? m[i] : (i ? c5 : c4)), "v"(c[0]), "v"(c[1]), "v"(c[2]), "v"(c[3]));
        a = f32x4{a0, a1, a2, a3};
      }
      SB0();
    }
    asm volatile("s_nop 1");
  };
  auto kloop = [&](auto wmc) __attribute__((always_inline)) {
    constexpr bool LD = decltype(wmc)::value != 0, TA = true;
    constexpr int grp = LD ? 1 : 0;
    const int WID = wid;
    const char* pa = nullptr;
    unsigned vob = 0;
    f32x4 ca = {0.f, 0.f, 0.f, 0.f}, cb = {0.f, 0.f, 0.f, 0.f};
    bf16x8 sel = {0, 0, 0, 0, 0, 0, 0, 0};
    int tro = 0;
    u64 tg = 0;
    const int psh = (wid == 2 && !tya) ? 1 : 0, pstr = wid < 3 ? qstr : F_ * 2;
    const char* const pbase = wid == 0 ? qb : (wid == 1 ? qb + dp1 : (wid == 2 ? mb0 : gb));
    if (!LD) {
      const int rs = mtile * 8 + (lane >> 3), vs = ((lane & 7) >> 1) ^ (((rs >> 1) & 1) | ((mtile & 1) << 1));
      pa = pbase + ((psh & vs) ? dgu : 0) + (rs * pstr + (vs >> psh) * 32 + (lane & 1) * 16);
      vob = rs * (F_ * 2) + vs * 32 + (lane & 1) * 16;
      const short one = fr == fq ? 0x3f80 : 0;
      sel = bf16x8{one, one, one, one, one, one, one, one};
      tro = q4 * 128 + ((fq ^ ((q4 >> 1) | ((mtile & 1) << 1))) * 32) + pp * 8;
      asm volatile("" : "+v"(sel), "+v"(tro), "+v"(ca), "+v"(cb));
    }
    auto share = [&](int t, unsigned dsta, unsigned dstb) {
      long off = (long)(t * 64 * pstr);
      asm volatile("" : "+s"(off));
      GLDS_NC(pa + off, shm_base + dsta);
      if (wid == 0) glds16_s(gb + dgu + (long)(t * 64 * F_ * 2), vob, shm_base + dstb);
    };
    auto accum = [&](int oa, int ob) {
      ca = __builtin_amdgcn_mfma_f32_16x16x32_bf16(tr_frag(shm3 + tro + oa), sel, ca, 0, 0, 0);
      if (wid == 0) cb = __builtin_amdgcn_mfma_f32_16x16x32_bf16(tr_frag(shm3 + tro + ob), sel, cb, 0, 0, 0);
    };
    auto cgather = [&]() {
      int l = threadIdx.x & 63;
      asm volatile("s_memrealtime %0\n\ts_waitcnt lgkmcnt(0)" : "=s"(tg), "+v"(l) :: "memory");
      const char* const p4 = l < 32 ? (const char*)(csum + (ntile * 5 + 4) * 512 + wid * 16) + (l >> 2) * 256
                                    : (const char*)(cstamp + (ntile * 4 + (l < 36 ? wid : 0)) * 8);
      if (l < 40) GLDS_SC1(p4 + (l & 3) * 16, shm_base + QS + wid * 640);
#pragma unroll
      for (int i = 0; i < 2; ++i) GLDS_SC1((const char*)(csum + (ntile * 5 + wid) * 512) + i * 1024 + l * 16, shm_base + DMP + wid * 2048 + i * 1024);
    };
    if (LD) {
#pragma unroll
      for (int i = 0; i < 10; ++i) {
        stage_part(0, kz, i >> 1, i & 1);
        asm volatile("" : "+v"(acc[0][i]), "+v"(acc[1][i]), "+v"(acc[2][i]), "+v"(acc[3][i]));
      }
    }
    unsigned st0 = 0, st1 = 0;
    if (!LD) {
#pragma unroll
      for (int a = 0; a < 4; ++a)
        asm volatile("" : "+v"(acc[a][0]), "+v"(acc[a][1]), "+v"(acc[a][2]), "+v"(acc[a][3]), "+v"(acc[a][4]), "+v"(acc[a][5]), "+v"(acc[a][6]),
                     "+v"(acc[a][7]), "+v"(acc[a][8]), "+v"(acc[a][9]));
      WAIT_V(0);
      const char* const xr = shm + G1_STAGE + WID * 4096 + (lane >> 5) * 2048 + (lane & 31) * 16;
      const u32x4 xv0 = *(const u32x4*)xr, xv1 = *(const u32x4*)(xr + 512), xv2 = *(const u32x4*)(xr + 1024), xv3 = *(const u32x4*)(xr + 1536);
      f32x2 f[16];
#pragma unroll
      for (int i = 0; i < 4; ++i) {
        f[i] = f32x2{lo_f(xv0[i]), hi_f(xv0[i])}; f[4 + i] = f32x2{lo_f(xv1[i]), hi_f(xv1[i])};
        f[8 + i] = f32x2{lo_f(xv2[i]), hi_f(xv2[i])}; f[12 + i] = f32x2{lo_f(xv3[i]), hi_f(xv3[i])};
      }
      f32x2 sa = f[0], sb = f[1];
#pragma unroll
      for (int i = 2; i < 16; i += 2) { sa += f[i]; sb += f[i + 1]; }
      sa += sb;
      const float mean = half_sum(sa[0] + sa[1]) * (1.0f / D_);
      f32x2 qa = {0.f, 0.f}, qb = {0.f, 0.f};
#pragma unroll
      for (int i = 0; i < 16; i += 2) {
        const f32x2 d = f[i] - f32x2{mean, mean}, e = f[i + 1] - f32x2{mean, mean};
        qa = __builtin_elementwise_fma(d, d, qa); qb = __builtin_elementwise_fma(e, e, qb);
      }
      qa += qb;
      const unsigned ps = pack_stats(mean, half_sum(qa[0] + qa[1]) * (1.0f / D_));
      st0 = __builtin_amdgcn_readlane(ps, 0);
      st1 = __builtin_amdgcn_readlane(ps, 32);
    }
    asm volatile("" : "+v"(aoff[0]), "+v"(aoff[1]), "+v"(boff[0]), "+v"(boff[1]));
#define WARM(c) asm volatile("s_mov_b64 exec, 0\n\ts_cmp_eq_u32 %0, 0\n\ts_cbranch_scc1 " c "1f\n\ts_cmp_eq_u32 %0, 3\n\ts_cbranch_scc1 " c "5f\n\t" \
                 "s_cmp_eq_u32 %0, 1\n\ts_cbranch_scc1 " c "3f\n\ts_cmp_eq_u32 %0, 2\n\ts_cbranch_scc1 " c "4f\n\ts_cmp_eq_u32 %0, 5\n\ts_cbranch_scc1 " c "2f\n\t" \
                 "s_branch " c "6f\n.Lwarm_ret" c ":\n\ts_mov_b64 exec, -1" :: "s"((tya || ((wid + 1) & 2) == 0) ? wid & 3 : (wid & 3) + 4) : "scc")
    if constexpr (LD) WARM("5");
    else WARM("4");
    TS1(0);
#pragma clang loop unroll(disable)
    for (int kt = 0; kt < NT; ++kt) {
      const int cur = kt & 1;
      if (LD) WS(41);
      if (LD) WAIT_V(0);
      if (!LD && kt == NT - 1) { correct(wmc); __builtin_amdgcn_s_setprio(2); }
      if (!LD) WS(41);
      BARL();
      lds_cptr sb = shm3;
      if (LD && kt + 1 < NT) {
#pragma unroll
        for (int i = 0; i < 18; ++i) stage_part(cur ^ 1, kt + 1, i >> 1, i & 1);
      } else if (LD) {
        correct(wmc);
      }
      auto fadr = [&](int g) { return sb + boff[g & 1] + ((g % 10) >> 1) * 8192 + (g / 10) * 4096; };
      auto rdl = [&](int g) { bl[g & 3] = tr_read(fadr(g)); };
      auto rdh = [&](int g) { bh[g & 3] = tr_read(fadr(g) + 512); };
      auto rda = [&](bf16x8* af, int ks, int mf) { af[mf] = *(const __attribute__((address_space(3))) bf16x8*)(sb + aoff[ks] + mf * 2048); };
      SB0();
      __builtin_amdgcn_s_waitcnt(0xC07F);
#pragma unroll
      for (int gg = 0; gg < 20; ++gg) {
        const int g = (gg + 18) % 20;
#pragma unroll
        for (int mf = 0; mf < 4; ++mf) {
          SB0();
          mm(g, mf, TA);
          SB0();
          if (g == 18) {
            if (mf == 0) { rdl(0); rdh(0); }
            if (mf == 1) { rda(af0, 0, 0); rda(af0, 0, 1); }
            if (mf == 2) { rda(af0, 0, 2); rda(af0, 0, 3); }
            if (mf == 3) { rdl(1); rdh(1); }
          } else if (g != 17) {
            const int nb = (g + 3) % 20;
            if (mf == 0) rdl(nb);
            if (mf == 1) rdh(nb);
            if (mf == 2 && g >= 2 && g < 6) rda(af1, 1, g - 2);
          }
        }
      }
      SB0();
      {
        const int d = cur ? -G1_STAGE : G1_STAGE;
        aoff[0] += d; aoff[1] += d; boff[0] += d; boff[1] += d;
        asm volatile("" : "+v"(aoff[0]), "+v"(aoff[1]), "+v"(boff[0]), "+v"(boff[1]));
      }
      if (!LD && kt <= 19 - SD) {
        int lane = threadIdx.x & 63;
        asm volatile("" : "+v"(lane));
        auto clock = [&]() { u64 now; asm volatile("s_memrealtime %0\n\ts_waitcnt lgkmcnt(0)" : "=s"(now) :: "memory"); return now; };
        WS(41);
        WAIT_V(0);
        if (kt <= 16 - SD) {
          WS(41);
          if (kt >= 1) { accum(DMP + wid * 1024, DMP + 4096); WAIT_L(0); }
          if (kt <= 15 - SD) {
            share(kt + SD, DMP + wid * 1024, DMP + 4096);
          } else if ((lane & 15) < 4) {
            const __amdgpu_buffer_rsrc_t rc = __builtin_amdgcn_make_buffer_rsrc((void*)(csum + ntile * 5 * 512 + mtile * 64), 0, 5 * 2048, 0x00020000);
            const int co = (lane & 15) * 64 + (lane >> 4) * 16;
            __builtin_amdgcn_raw_buffer_store_b128(__builtin_bit_cast(u32x4, ca), rc, co + wid * 2048, 0, 16);
            if (wid == 0) __builtin_amdgcn_raw_buffer_store_b128(__builtin_bit_cast(u32x4, cb), rc, co + 4 * 2048, 0, 16);
          }
          if (kt < SD) {
            WS(41);
            if (kt <= SD - 3) share(kt + 2, DMP + 7168, DMP + 7168);
            const int sp = cur * G1_STAGE + 32768 + mtile * 1024;
            accum(sp + wid * 8192, sp + 4 * 8192);
            if (kt == PUB_K) {
              const u64 now = clock();
              if (lane == 0) {
                const __amdgpu_buffer_rsrc_t rs = __builtin_amdgcn_make_buffer_rsrc((void*)(rstat + srow), 0, 32, 0x00020000);
                __builtin_amdgcn_raw_buffer_store_b128(u32x4{st0, (unsigned)now, st0, (unsigned)(now >> 32)}, rs, 0, 0, 16);
                __builtin_amdgcn_raw_buffer_store_b128(u32x4{st1, (unsigned)now, st1, (unsigned)(now >> 32)}, rs, 16, 0, 16);
              }
            }
          }
          if (kt >= PF_K0 && kt < PF_K0 + 5) {
            WS(41);
#pragma unroll
            for (int i = 0; i < 2; ++i) {
              const int c = (bid * 4 + wid) * 10 + (kt - PF_K0) * 2 + i;
              if (c < 9216) {
                const u16* src = (c < 7168 ? wd + (size_t)c * 512 : wo + (size_t)(c - 7168) * 512) + lane * 8;
                GLDS_NC(src, shm_base + DMP + 7168);
              }
            }
          }
          if (kt >= CP_K0 && kt <= CP_K0 + 4) {
            if (kt < CP_K0 + 3) {
              WS(41);
              const int q1 = bid * 4 + WID + (kt - CP_K0) * 1024, q0 = q1 - 1024;
              if (q0 >= 0 && q0 < 1152) {
                const __amdgpu_buffer_rsrc_t rc = __builtin_amdgcn_make_buffer_rsrc((void*)(wcopy + (q0 & 1) * 589824 + (q0 >> 1) * 1024), 0, 1024, 0x00020000);
                __builtin_amdgcn_raw_buffer_store_b128(*(const u32x4*)(shm + AUX + WID * 1024 + lane * 16), rc, lane * 16, 0, 16);
                WAIT_L(0);
              }
              if (q1 < 1152) {
                const char* src = (const char*)((q1 >> 1) < 448 ? wd + (size_t)(q1 >> 1) * 8 * D_ : wo + (size_t)((q1 >> 1) - 448) * 8 * D_);
                src += ((3 - ((unsigned)(uintptr_t)src >> 7)) & 7) * 128 + (q1 & 1) * 1024;
                GLDS(src + (lane >> 3) * 2048 + (lane & 7) * 16, shm_base + AUX + WID * 1024);
              }
            } else if (kt == CP_K0 + 4) {
              if (WID == 0) {
                const u64 now = clock();
                if (lane == 0) __hip_atomic_store(&donec[bid], now, RLX_AGENT);
              }
              if (WID == 2) GLDS_SC1((const char*)(done1 + (bid & ~1) * 64) + lane * 16, shm_base + DMP + 7168);
            }
          }
        } else if (kt == 17 - SD) {
          WS(41);
          const u64 now = clock();
          if (lane == 0) __hip_atomic_store(&cstamp[(ntile * 4 + wid) * 8 + mtile], now, RLX_AGENT);
          if (WID >= 2) GLDS_SC1((const char*)donec + (WID - 2) * 1024 + lane * 16, shm_base + DMP + WID * 2048);
          GLDS_SC1(rstat + m0 + wid * 64 + (lane & 32) + 2 * (lane & 15) + ((lane >> 4) & 1), shm_base + AUX + wid * 1024);
          {
            const char* const wp = (const char*)wd + (size_t)(xcd < 4 ? 28 + 5 * (xcd & 1) : 5 - 5 * (xcd & 1)) * 131072 + (idx * 4 + wid) * 1024;
#pragma unroll
            for (int i = 0; i < NWPF; ++i) glds16_s(wp + (long)((i & 1) + ((i >> 1) & 1) * 14 + (i >> 2) * 7) * 131072, lane * 16, shm_base + DMP + wid * 2048 + 1024);
          }
        } else if (kt == 18 - SD) {
          WS(41);
          const u32x4 c4 = *(const u32x4*)(shm + DMP + WID * 2048 + lane * 16);
          u32x4 gr = *(const u32x4*)(shm + AUX + wid * 1024 + lane * 16);
          asm volatile("" :: "v"(c4), "v"(gr) : "memory");
          cgather();
          const u64 win = tg - t_own;
          auto frs = [&](unsigned lo, unsigned hi) { return (((u64)hi << 32) | lo) - t_own - 100ull < win; };
          if (WID >= 2) *(unsigned*)(shm + CNT - 4 + WID * 4) = __all(frs(c4[0], c4[1]) & frs(c4[2], c4[3])) ? 1u : 0u;
          if (__builtin_expect(!__all((gr[0] == gr[2]) & frs(gr[1], gr[3])), 0)) {
            const __amdgpu_buffer_rsrc_t rs = __builtin_amdgcn_make_buffer_rsrc((void*)(rstat + m0), 0, 4096, 0x00020000);
            const int prow = wid * 64 + (lane & 32) + 2 * (lane & 15) + ((lane >> 4) & 1);
            for (;;) {
              gr = __builtin_bit_cast(u32x4, __builtin_amdgcn_raw_buffer_load_b128(rs, prow * 16, 0, 16));
              if (__all((gr[0] == gr[2]) & fresh(((u64)gr[3] << 32) | gr[1], t_own))) break;
              if (__builtin_amdgcn_s_memrealtime() - t_own > TIMEOUT_) { gr[0] = row_stats_serial(x + (size_t)(m0 + prow) * D_); break; }
              __builtin_amdgcn_s_sleep(2);
            }
            *(unsigned*)(shm + AUX + wid * 1024 + lane * 16) = gr[0];
          }
        } else {
          WS(41);
          const char* const sc = shm + (lane < 32 ? DMP + wid * 2048 + lane * 8 : QS + wid * 640 + (lane & 7) * 8);
          const int st = lane < 32 ? 256 : 64;
          const unsigned g0 = *(const unsigned*)(shm + AUX + wid * 1024 + lane * 16);
          f32x2 w[8];
          bool late = false;
          for (;;) {
            const u64 sv = *(const u64*)(shm + QS + wid * 640 + 512 + (lane & 15) * 8);
#pragma unroll
            for (int r = 0; r < 8; ++r) w[r] = *(const f32x2*)(sc + r * st);
            if (__builtin_expect(__all(sv - t_own - 100ull < tg - t_own - 200ull), 1)) break;
            late = __builtin_amdgcn_s_memrealtime() - t_own > TIMEOUT_;
            if (late) break;
            __builtin_amdgcn_s_sleep(2);
            WAIT_L(0);
            cgather();
            WAIT_V(0);
          }
          WS(41);
          float t0 = w[0][0], t1 = w[0][1];
#pragma unroll
          for (int r = 1; r < 8; ++r)
            asm("v_add_f32 %0, %0, %2\n\tv_add_f32 %1, %1, %3" : "+v"(t0), "+v"(t1) : "v"(w[r][0]), "v"(w[r][1]));
          if (__builtin_expect(late, 0)) {
            const int gq = (lane >> 3) & 3;
            const char* pc = (lane < 32 ? pbase + ((psh & gq) ? dgu : 0) + (gq >> psh) * 32 : gb + dgu + wid * 32) + (lane & 7) * 4;
            t0 = 0.f; t1 = 0.f;
#pragma unroll 1
            for (int k = 0; k < D_; ++k) { const unsigned v = *(const unsigned*)(pc + (size_t)k * (lane < 32 ? pstr : F_ * 2)); t0 += lo_f(v); t1 += hi_f(v); }
          }
          if (lane < 40)
            *(f32x2*)((float*)(shm + CF) + (lane < 32 ? (lane >> 4) * 160 + wid * 32 + (lane & 15) * 2 : (wid >> 1) * 160 + 128 + (wid & 1) * 16 + (lane & 7) * 2)) = f32x2{t0, t1};
          const float nm = -lo_f(g0), dn = hi_f(g0), r = __builtin_amdgcn_rcpf(dn);
          float k1 = -2.3022081986f * r, d2 = dn * dn;
          asm volatile("" : "+v"(k1));
          float k2 = -0.10294324f * r;
          asm volatile("" : "+v"(d2), "+v"(k2));
          const f32x4 fc = {nm, k1, k2 * r * r, d2};
          *(f32x4*)(shm + AUX + wid * 1024 + lane * 16) = fc;
          float* const tw = (float*)(shm + DMP + WID * 2048) + lane;
#pragma unroll
          for (int i = 0; i < 4; ++i) tw[i * 64] = fc[i];
          ((float*)(shm + QS + WID * 640))[lane] = r;
        }
      }
      SB0();
    }
    WS(41);
#pragma unroll
    for (int g = 18; g < 20; ++g) {
#pragma unroll
      for (int mf = 0; mf < 4; ++mf) mm(g, mf, TA);
    }
  TS1(1);
  __builtin_amdgcn_sched_barrier(0);
  int tz = threadIdx.x;
  asm volatile("s_nop 7" : "+v"(tz));
  __builtin_amdgcn_sched_barrier(0);
  const int li = tz & 15, lq = (tz >> 4) & 3, ln = tz & 63;
  const __amdgpu_buffer_rsrc_t ra = __builtin_amdgcn_make_buffer_rsrc((void*)abuf, 0, 16 * 72 * 16384, 0x00020000);
  float* const svr = (float*)(shm + QS + wm * 640);
  float* const tb = (float*)(shm + DMP + wm * 2048);
  typedef __attribute__((address_space(3))) unsigned lds_u32;
  lds_u32* const cnt = (lds_u32*)(shm3 + CNT);
  unsigned vcnt = shm_base + CNT;
  asm volatile("" : "+v"(vcnt));
  auto signal = [&](unsigned v) {
    unsigned t;
    asm volatile("v_mov_b32 %0, %2\n\ts_mov_b64 exec, 1\n\tds_add_u32 %1, %0\n\ts_mov_b64 exec, -1" : "=&v"(t) : "v"(vcnt), "s"(v) : "memory");
  };
  auto await = [&](unsigned mask, unsigned val) {
    unsigned t, u;
    asm volatile("1:\n\tds_read_b32 %0, %2\n\ts_waitcnt lgkmcnt(0)\n\tv_readfirstlane_b32 %1, %0\n\ts_and_b32 %1, %1, %3\n\ts_cmp_eq_u32 %1, %4\n\t"
                 "s_cbranch_scc1 2f\n\ts_sleep 1\n\ts_branch 1b\n\t2:" : "=&v"(t), "=&s"(u) : "v"(vcnt), "s"(mask), "s"(val) : "memory", "scc");
  };
  if (grp) __builtin_amdgcn_s_setprio(1);
  signal(1u << (8 * grp));
  TS1(4);
  f32x4 rf[4];
#pragma unroll
  for (int mf = 0; mf < 4; ++mf) rf[mf] = *(const f32x4*)(shm + AUX + (wm * 64 + mf * 16 + li) * 16);
  const int sbase = ((mtile * 2 + (wm >> 1)) * 72 * 8 + (wm & 1) * 4) * 2048;
  auto putf = [&](int kg, int mf, u32x4 v) {
    __builtin_amdgcn_raw_buffer_store_b128(v, ra, ln * 16 + sbase + ((kg >> 1) * 8 + mf) * 2048 + (kg & 1) * 1024, 0, 16);
  };
  auto gelp = [&](f32x2 tg, f32x2 tu, f32x2 k12, f32x2 k22, f32x2 d2) {
    const f32x2 arg = tg * __builtin_elementwise_fma(tg * tg, k22, k12);
    const f32x2 dn = __builtin_elementwise_fma(f32x2{__builtin_amdgcn_exp2f(arg[0]), __builtin_amdgcn_exp2f(arg[1])}, d2, d2);
    const f32x2 r = (tg * tu) * f32x2{__builtin_amdgcn_rcpf(dn[0]), __builtin_amdgcn_rcpf(dn[1])};
    return pack2(r[0], r[1]);
  };
  auto gels = [&](const f32x4& g, const f32x4& u, const f32x4& k) {
    float t0, t1, t2, t3, n0, n1, n2, n3;
    asm("v_mul_f32 %0, %8, %8\n\tv_mul_f32 %1, %9, %9\n\tv_mul_f32 %2, %10, %10\n\tv_mul_f32 %3, %11, %11\n\t"
        "v_fma_f32 %0, %0, %17, %16\n\tv_fma_f32 %1, %1, %17, %16\n\tv_fma_f32 %2, %2, %17, %16\n\tv_fma_f32 %3, %3, %17, %16\n\t"
        "v_mul_f32 %0, %8, %0\n\tv_mul_f32 %1, %9, %1\n\tv_mul_f32 %2, %10, %2\n\tv_mul_f32 %3, %11, %3\n\t"
        "v_exp_f32 %0, %0\n\tv_mul_f32 %4, %8, %12\n\tv_exp_f32 %1, %1\n\tv_mul_f32 %5, %9, %13\n\t"
        "v_exp_f32 %2, %2\n\tv_mul_f32 %6, %10, %14\n\tv_exp_f32 %3, %3\n\tv_mul_f32 %7, %11, %15\n\t"
        "v_fma_f32 %0, %0, %18, %18\n\tv_fma_f32 %1, %1, %18, %18\n\tv_rcp_f32 %0, %0\n\tv_fma_f32 %2, %2, %18, %18\n\t"
        "v_rcp_f32 %1, %1\n\tv_fma_f32 %3, %3, %18, %18\n\tv_rcp_f32 %2, %2\n\tv_mul_f32 %0, %4, %0\n\t"
        "v_rcp_f32 %3, %3\n\tv_mul_f32 %1, %5, %1\n\tv_mul_f32 %2, %6, %2\n\tv_mul_f32 %3, %7, %3\n\t"
        "v_cvt_pk_bf16_f32 %0, %0, %1\n\tv_cvt_pk_bf16_f32 %1, %2, %3"
        : "=&v"(t0), "=&v"(t1), "=&v"(t2), "=&v"(t3), "=&v"(n0), "=&v"(n1), "=&v"(n2), "=&v"(n3)
        : "v"(g[0]), "v"(g[1]), "v"(g[2]), "v"(g[3]), "v"(u[0]), "v"(u[1]), "v"(u[2]), "v"(u[3]), "v"(k[1]), "v"(k[2]), "v"(k[3]));
    return u32x2{__float_as_uint(t0), __float_as_uint(t1)};
  };
  auto gel2 = [&](int p, int kg, int mf0 = 0, int mf1 = 4, int nsc = 0, bool opq = false, int ch = 42) {
#pragma unroll
    for (int mf = mf0; mf < mf1; ++mf) {
      const f32x2 k12 = {rf[mf][1], rf[mf][1]}, k22 = {rf[mf][2], rf[mf][2]}, d2 = {rf[mf][3], rf[mf][3]};
      u32x4 o;
      WS(ch);
      if (opq) asm volatile("" : "+v"(acc[mf][p * 2]), "+v"(acc[mf][p * 2 + 1]));
      if (mf < nsc) {
        const u32x2 a = gels(acc[mf][p * 2], acc[mf][p * 2 + 2], rf[mf]), b = gels(acc[mf][p * 2 + 1], acc[mf][p * 2 + 3], rf[mf]);
        o = u32x4{a[0], a[1], b[0], b[1]};
      } else {
#pragma unroll
        for (int i = 0; i < 4; ++i) {
          const int j = i >> 1, e = (i & 1) * 2;
          const f32x4 g = acc[mf][p * 2 + j], u = acc[mf][p * 2 + 2 + j];
          o[i] = gelp(f32x2{g[e], g[e + 1]}, f32x2{u[e], u[e + 1]}, k12, k22, d2);
        }
      }
      putf(kg, mf, o);
      if (mf < nsc) SB0();
    }
  };
  auto client = [&](int l) { return (((l & 7) * 4 + (mtile & 3)) * 8) + (((mtile >> 2) * 2 + ((l >> 4) & 1)) ^ ((l & 8) ? 5 : 0)); };
  auto notify = [&](u64 lanes) {
    int l = threadIdx.x & 63;
    asm volatile("" : "+v"(l));
    const unsigned off = (client(l) * 64 + (l & 32) + ntile) * 8;
    u64 now, vn;
    asm volatile("s_memrealtime %0\n\ts_nop 0\n\ts_waitcnt lgkmcnt(0)\n\tv_mov_b64 %1, %0\n\ts_mov_b64 exec, %4\n\tglobal_store_dwordx2 %2, %1, %3 sc1\n\ts_mov_b64 exec, -1"
                 : "=&s"(now), "=&v"(vn) : "v"(off), "s"(done1), "s"(lanes) : "memory");
  };
  const u64 myh = 0xffffull << ((wm >> 1) * 16);
  auto arrive = [&](auto pc) {
    constexpr int part = decltype(pc)::value;
    unsigned t;
    asm volatile("v_mov_b32 %0, 1\n\ts_mov_b64 exec, 1\n\tds_add_rtn_u32 %0, %1, %0 offset:%2\n\ts_mov_b64 exec, -1\n\ts_waitcnt lgkmcnt(0)"
                 : "=&v"(t) : "v"(vcnt), "n"(12 + 4 * part) : "memory");
    if (__builtin_amdgcn_readfirstlane(t) == 3u) notify(myh << (32 * part));
  };
  const int role = 1 - (xcd >> 2);
  const int xb = role ? xcd : xcd ^ 5, par = xb & 1;
  const int mt2 = ((xb >> 1) * 4 + (idx & 3)) * 2 + par, nt2 = idx >> 2;
  auto rsrc = [](u64 b, unsigned n) { return u32x4{(unsigned)b, (unsigned)(b >> 32) & 0xffffu, n, 0x00020000u}; };
  u32x4 rw[2];
  int wsh[2];
  unsigned vb[2][2], cokw = 0;
  auto wsetup = [&](const u16* w, int rows, int crow0, int l) {
    const unsigned vr = (wid >> 1) * 32 + (wid & 1) * 8 + ((l >> 5) & 1) * 16 + ((l >> 3) & 3), vc = (((l >> 1) & 3) ^ ((l >> 4) & 1)) * 32 + (l & 1) * 16;
    u64 bs = (u64)(uintptr_t)(wcopy + (nt2 >> 2) * 589824 + crow0 * 128 - 1024), bf = (u64)(uintptr_t)(w + nt2 * 128) - 1024;
    asm volatile("" : "+s"(bs), "+s"(bf));
#pragma unroll
    for (int i = 0; i < 2; ++i) {
      const bool slow = ((((unsigned)bf >> 7) + i) & 7) == (cokw ? 3u : 8u);
      const int sh = slow ? 7 : 11;
      rw[i] = rsrc(slow ? bs : bf + i * 128, ((rows - 1) << sh) + 128 + 1024);
      wsh[i] = 6 + sh;
      vb[i][0] = (vr << sh) + vc + 1024;
      vb[i][1] = ((vr + 4) << sh) + (vc ^ 64);
    }
  };
#define G2_W "s_add_u32 m0, %[l], %[ow]\n\ts_lshl_b32 %[t], %[k], %[s0]\n\tbuffer_load_dwordx4 %[v0], %[w0], %[t] offen sc1 lds\n\t" \
             "buffer_load_dwordx4 %[v1], %[w0], %[t] offen offset:1024 sc1 lds\n\t" \
             "s_add_u32 m0, m0, 8192\n\ts_lshl_b32 %[t], %[k], %[s1]\n\tbuffer_load_dwordx4 %[v2], %[w1], %[t] offen sc1 lds\n\t" \
             "buffer_load_dwordx4 %[v3], %[w1], %[t] offen offset:1024 sc1 lds"
#define G2_WOP [ow] "s"(16384 + wid * 2048), [k] "s"(kt), [s0] "s"(wsh[0]), [s1] "s"(wsh[1]), [w0] "s"(rw[0]), [w1] "s"(rw[1]), \
               [v0] "v"(vb[0][0]), [v1] "v"(vb[0][1]), [v2] "v"(vb[1][0]), [v3] "v"(vb[1][1])
#define G2_A "s_add_u32 m0, %[l], %[oa]\n\ts_lshl_b32 %[t], %[k], 14\n\tbuffer_load_dwordx4 %[va], %[a], %[t] offen sc1 lds\n\t" \
             "buffer_load_dwordx4 %[va], %[a], %[t] offen offset:1024 sc1 lds\n\t"
#define G2_A2 "buffer_load_dwordx4 %[va], %[a], %[t] offen offset:2048 sc1 lds\n\tbuffer_load_dwordx4 %[va], %[a], %[t] offen offset:3072 sc1 lds\n\t"
  constexpr int PD = G2_NSTAGE - 1;
  constexpr int NY = 28;
  const int ny = (xcd & 1) ? NY - DLT : NY + DLT;
  const u32x4 ra2 = rsrc((u64)(uintptr_t)abuf + mt2 * (72 * 16384), 72 * 16384);
  unsigned va, tab, nlo, nhi, sl = shm_base + 4 * G2_STAGE;
  const int kfirst = 28 * role + 5 * par;
  int q = 3, kt = kfirst + 15;
  u64 okb = 0;
  auto adv = [&]() { ++q; kt = __builtin_amdgcn_readlane(tab, q); sl = sl == shm_base + (G2_NSTAGE - 1) * G2_STAGE ? shm_base : sl + G2_STAGE; };
  auto stage_y = [&]() {
    unsigned t;
    asm volatile(G2_A G2_A2 G2_W : [t] "=&s"(t) : [l] "s"(sl), [oa] "s"((wid ^ role) * 4096), [a] "s"(ra2), [va] "v"(va), G2_WOP : "memory", "scc", "m0");
    adv();
  };
  auto digest = [&](u64 v) {
    const u64 r = __ballot(fresh(v, t_own));
    const unsigned hi = (unsigned)(r >> 32), lo = (unsigned)r | hi;
    okb |= __ballot(((nlo & ~lo) | (nhi & ~hi)) == 0u);
  };
  constexpr int SNAP = 28672;
  auto snapshot = [&]() {
    asm volatile("s_mov_b32 m0, %2\n\ts_mov_b64 exec, 0xffffffff\n\tglobal_load_lds_dwordx4 %0, %1 sc1\n\ts_mov_b64 exec, -1"
                 :: "v"(ln * 16), "s"(done1 + bid * 64), "s"(shm_base + SNAP + wid * 512) : "memory", "m0");
    SB0();
  };
  auto peek = [&]() { digest(*(const u64*)(shm + SNAP + wid * 512 + ln * 8)); };
  auto wpair = [&](int g, int i) {
    unsigned t;
    asm volatile("s_mov_b32 m0, %1\n\ts_lshl_b32 %0, %2, %3\n\tbuffer_load_dwordx4 %5, %4, %0 offen sc1 lds\n\tbuffer_load_dwordx4 %6, %4, %0 offen offset:1024 sc1 lds"
                 : "=&s"(t) : "s"(shm_base + (g + 1) * G2_STAGE + 16384 + i * 8192 + wid * 2048), "s"(kfirst + (g == 2 ? 14 : g)), "s"(wsh[i]), "s"(rw[i]),
                   "v"(vb[i][0]), "v"(vb[i][1]) : "memory", "scc", "m0");
    SB0();
  };
  auto aquad = [&](int g) {
    unsigned t;
    asm volatile(G2_A G2_A2 : [t] "=&s"(t) : [l] "s"(shm_base + (g + 1) * G2_STAGE), [oa] "s"((wid ^ role) * 4096), [a] "s"(ra2), [va] "v"(va),
                 [k] "s"(((okb >> g) & 1) ? kfirst + (g == 2 ? 14 : g) : 100) : "memory", "scc", "m0");
    SB0();
  };
  auto mid = [&](int ch) {
    WS(ch);
    cokw = __builtin_amdgcn_readfirstlane(cnt[1] & cnt[2]);
    wsetup(wd, F_, 0, ln);
    va = ln * 16 + wid * 4096;
    {
      const int ext = ln >= NY ? 1 : 0, qq = ln - ext * DLT, so = ln - ny;
      int j0 = 5 * par, j1 = 2 - 2 * par, j2 = 5 - 2 * par;
      asm volatile("" : "+s"(j1));
      asm volatile("" : "+s"(j2));
      const int j = (qq < 8 ? j0 : (qq < 20 ? j1 : j2)) + ((unsigned)(0x44449249244444ull >> (2 * qq)) & 3);
      const int xs = ((role ^ ext) << 2) | ((unsigned)(0xf5a0fd5a80f5a0ull >> (2 * qq)) & 3);
      unsigned bits = ((0x41a8282u >> (4 * j)) & 15u) << (4 * xs), ob = (so < 16 ? 1u : 0u) << (2 * so), ty = 7 * xs + j;
      asm volatile("" : "+v"(bits), "+v"(ob), "+v"(ty));
      tab = so < 0 ? ty : so;
      nlo = ln < 8 ? bits : 0u;
      nhi = so < 0 ? bits - nlo : ob;
    }
    asm volatile("" :: "s"(rw[0]), "s"(rw[1]), "s"(wsh[0]), "s"(wsh[1]), "s"(ra2));
    asm volatile("" : "+v"(vb[0][0]), "+v"(vb[0][1]), "+v"(vb[1][0]), "+v"(vb[1][1]), "+v"(va), "+v"(tab), "+v"(nlo), "+v"(nhi));
    WS(ch);
    await(0xffffu, 0x404u);
    wpair(0, 0);
    wpair(0, 1);
    WAIT_V(4);
    arrive(std::integral_constant<int, 0>{});
    __builtin_amdgcn_s_setprio(PRB);
  };
  int tsel = ntile & 1;
  asm volatile("" : "+s"(tsel));
  if (tsel != 0) {
    if (grp == 0) gel2(0, 2 * (kt0 + he) + wc, 0, NSC, NSC);
    else gel2(0, 2 * (kt0 + he) + wc, 0, NSC);
    gel2(0, 2 * (kt0 + he) + wc, NSC, 4);
    if (grp == 0) mid(42);
#pragma unroll
    for (int mf = 0; mf < 4; ++mf) {
      f32x4 t[4];
      if (!(mf & 1)) WS(46);
#pragma unroll
      for (int i = 1; i < 4; ++i) t[i] = *(const f32x4*)(tb + i * 64 + mf * 16 + lq * 4);
      const f32x4 g = acc[mf][4], u = acc[mf][5];
      u32x2 o;
#pragma unroll
      for (int e = 0; e < 4; e += 2)
        o[e >> 1] = gelp(f32x2{g[e], g[e + 1]}, f32x2{u[e], u[e + 1]}, f32x2{t[1][e], t[1][e + 1]}, f32x2{t[2][e], t[2][e + 1]}, f32x2{t[3][e], t[3][e + 1]});
      *(u32x2*)(shm + wm * 4096 + mf * 1024 + wc * 512 + li * 32 + lq * 8) = o;
      if (mf == 1 || grp == 0) SB0();
      if (grp == 0) {
        wpair(1 + (mf >> 1), mf & 1);
        if (mf == SNP) snapshot();
      }
    }
    signal(0x10000u << vid);
    TS1(5);
    if (grp) {
      gel2(3, 2 * (kt0 + 2 + he) + wc, 0, 4, 0, false, 46);
    } else {
#pragma unroll
      for (int mf = 0; mf < 4; ++mf) {
        gel2(3, 2 * (kt0 + 2 + he) + wc, mf, mf + 1, 0, false, 46);
        SB0();
        if (mf == 0) { WAIT_V(7 - 2 * SNP); peek(); }
        if (mf < 3) aquad(mf);
        if (mf == 2) snapshot();
      }
    }
    WS(46);
    await(0x110000u << wm, 0x110000u << wm);
#pragma unroll
    for (int i = 0; i < 2; ++i) putf(2 * (kt0 + 4) + he, 2 * wc + i, __builtin_bit_cast(u32x4, tr_frag(shm3 + wm * 4096 + wc * 2048 + i * 1024 + ln * 8)));
    if (grp == 0) WAIT_V(9);
    TS1(6);
  } else {
    WS(43);
    if (grp) __builtin_amdgcn_s_setprio(PRL0);
    float rd[4];
#pragma unroll
    for (int mf = 0; mf < 4; ++mf) rd[mf] = svr[mf * 16 + li];
    auto cvs = [&](const f32x4& a, const f32x4& b, const f32x4& r) {
      float t0, t1, t2, t3, t4, t5, t6, t7;
      asm("v_mul_f32 %0, %8, %16\n\tv_mul_f32 %4, %9, %17\n\tv_mul_f32 %1, %10, %18\n\tv_mul_f32 %5, %11, %19\n\t"
          "v_mul_f32 %2, %12, %16\n\tv_mul_f32 %6, %13, %17\n\tv_mul_f32 %3, %14, %18\n\tv_mul_f32 %7, %15, %19\n\t"
          "v_cvt_pk_bf16_f32 %0, %0, %4\n\tv_cvt_pk_bf16_f32 %1, %1, %5\n\tv_cvt_pk_bf16_f32 %2, %2, %6\n\tv_cvt_pk_bf16_f32 %3, %3, %7"
          : "=&v"(t0), "=&v"(t1), "=&v"(t2), "=&v"(t3), "=&v"(t4), "=&v"(t5), "=&v"(t6), "=&v"(t7)
          : "v"(a[0]), "v"(a[1]), "v"(a[2]), "v"(a[3]), "v"(b[0]), "v"(b[1]), "v"(b[2]), "v"(b[3]), "v"(r[0]), "v"(r[1]), "v"(r[2]), "v"(r[3]));
      return u32x4{__float_as_uint(t0), __float_as_uint(t1), __float_as_uint(t2), __float_as_uint(t3)};
    };
    auto cvp = [&](const f32x4& a, const f32x4& b) { return u32x4{pack2(a[0], a[1]), pack2(a[2], a[3]), pack2(b[0], b[1]), pack2(b[2], b[3])}; };
    auto cvk = [&](int mf, bool sc) {
      const f32x4 r4 = {rd[mf], rd[mf], rd[mf], rd[mf]};
      return sc ? cvs(acc[mf][2], acc[mf][3], r4) : cvp(acc[mf][2] * r4, acc[mf][3] * r4);
    };
    auto cvv = [&](int mf, bool sc) {
      const f32x4 r4 = *(const f32x4*)(svr + mf * 16 + lq * 4);
      return sc ? cvs(acc[mf][4], acc[mf][5], r4) : cvp(acc[mf][4] * r4, acc[mf][5] * r4);
    };
    u32x4 kx[4], vx[4], qx[4], hx[4];
    const bf16x8 ones = {0x3f80, 0x3f80, 0x3f80, 0x3f80, 0x3f80, 0x3f80, 0x3f80, 0x3f80};
    kx[2] = cvk(2, true); kx[3] = cvk(3, true);
    vx[2] = cvv(2, true); vx[3] = cvv(3, true);
    const bool giveh = !grp && wm == 3 && mtile < 7, needh = grp && wm == 0 && mtile > 0;
    char* const hptr = (char*)halo + (size_t)((needh ? mtile - 1 : mtile) * 32 + ntile + wc) * 16384;
    WS(43);
    if (grp || wm < 3) {
      char* const ex = shm + vid * 4096 + ln * 16;
      *(u32x4*)ex = kx[2]; *(u32x4*)(ex + 1024) = kx[3]; *(u32x4*)(ex + 2048) = vx[2]; *(u32x4*)(ex + 3072) = vx[3];
      signal(0x10000u << vid);
    } else if (giveh) {
      const __amdgpu_buffer_rsrc_t rh = __builtin_amdgcn_make_buffer_rsrc((void*)hptr, 0, 9216, 0x00020000);
      const u64 now = __builtin_amdgcn_s_memrealtime();
      const unsigned tag = (unsigned)now;
#pragma unroll
      for (int r = 0; r < 8; ++r) {
        const u32x4 w = r < 4 ? kx[2 + (r >> 1)] : vx[r >> 1];
        __builtin_amdgcn_raw_buffer_store_b128(u32x4{w[2 * (r & 1)], tag, w[2 * (r & 1) + 1], tag}, rh, ln * 16 + r * 1024, 0, 16);
      }
      __builtin_amdgcn_raw_buffer_store_b128(u32x4{tag, (unsigned)(now >> 32), tag, (unsigned)(now >> 32)}, rh, ln * 16 + 8192, 0, 16);
    }
    if (grp == 0) { gel2(3, 2 * (kt0 + 5 + he) + wc, 0, 4, NSC - 1, true, 43); mid(43); }
    WS(44 - grp);
    kx[0] = cvk(0, false); kx[1] = cvk(1, false);
    vx[0] = cvv(0, false); vx[1] = cvv(1, false);
#pragma unroll
    for (int mf = 0; mf < 4; ++mf) qx[mf] = cvp(acc[mf][0], acc[mf][1]);
    f32x4 mk0, mk1;
#pragma unroll
    for (int j = 0; j < 4; ++j) {
      const int k0 = 4 * lq + j, k1 = 16 + 4 * lq + j;
      mk0[j] = (k0 >= li + 9 && k0 <= li + 16) ? 0.f : -1.0e30f;
      mk1[j] = (k1 >= li + 9 && k1 <= li + 16) ? 0.f : -1.0e30f;
    }
    auto max3 = [](float a, float b, float c) { float r; asm("v_max3_f32 %0, %1, %2, %3" : "=v"(r) : "v"(a), "v"(b), "v"(c)); return r; };
    auto max2 = [](float a, float b) { float r; asm("v_max_f32 %0, %1, %2" : "=v"(r) : "v"(a), "v"(b)); return r; };
    auto tiles = [&](int t0, const u32x4* kp, const u32x4* vp, bool first_rows) {
      const f32x4 z = {0.f, 0.f, 0.f, 0.f};
      const f32x4 mz = {-1.0e30f, -1.0e30f, -1.0e30f, -1.0e30f};
      WS(44);
      f32x4 s0[2], s1[2];
#pragma unroll
      for (int n = 0; n < 2; ++n) {
        const bf16x8 qf = __builtin_bit_cast(bf16x8, qx[t0 + n]);
        s0[n] = __builtin_amdgcn_mfma_f32_16x16x32_bf16(__builtin_bit_cast(bf16x8, kp[n]), qf, first_rows ? mz : mk0, 0, 0, 0);
        s1[n] = __builtin_amdgcn_mfma_f32_16x16x32_bf16(__builtin_bit_cast(bf16x8, kx[t0 + n]), qf, mk1, 0, 0, 0);
      }
      SB0();
      if (grp == 0) { if (t0) wpair(1, 1); else aquad(1); }
      asm volatile("s_nop 7\n\ts_nop 3" : "+v"(s0[0]), "+v"(s1[0]), "+v"(s0[1]), "+v"(s1[1]));
      float mx[2];
#pragma unroll
      for (int n = 0; n < 2; ++n) mx[n] = max3(max3(s0[n][0], s0[n][1], s0[n][2]), max3(s0[n][3], s1[n][0], s1[n][1]), max2(s1[n][2], s1[n][3]));
#pragma unroll
      for (int n = 0; n < 2; ++n) { auto r = __builtin_amdgcn_permlane16_swap(__float_as_uint(mx[n]), __float_as_uint(mx[n]), false, false);
        mx[n] = max2(__uint_as_float(r[0]), __uint_as_float(r[1])); }
#pragma unroll
      for (int n = 0; n < 2; ++n) { auto r = __builtin_amdgcn_permlane32_swap(__float_as_uint(mx[n]), __float_as_uint(mx[n]), false, false);
        mx[n] = max2(__uint_as_float(r[0]), __uint_as_float(r[1])); }
      SB0();
      if (grp == 0) {
        if (t0) wpair(2, 0); else aquad(2);
        snapshot();
      }
      WS(44);
      constexpr float cs2 = 0.17677669529663687f * 1.4426950408889634f;
      f32x4 os[2], o0[2], o1[2];
#pragma unroll
      for (int n = 0; n < 2; ++n) {
        const float c = cs2 * rd[t0 + n], nm = -mx[n] * c;
        float e8[8];
#pragma unroll
        for (int j = 0; j < 4; ++j) {
          e8[j] = __builtin_amdgcn_exp2f(__builtin_fmaf(s0[n][j], c, nm));
          e8[4 + j] = __builtin_amdgcn_exp2f(__builtin_fmaf(s1[n][j], c, nm));
        }
        u32x4 pk;
#pragma unroll
        for (int j = 0; j < 4; ++j) pk[j] = pack2(e8[2 * j], e8[2 * j + 1]);
        const bf16x8 pf = __builtin_bit_cast(bf16x8, pk);
        const u32x4 va = {vp[n][0], vp[n][1], vx[t0 + n][0], vx[t0 + n][1]}, vb = {vp[n][2], vp[n][3], vx[t0 + n][2], vx[t0 + n][3]};
        os[n] = __builtin_amdgcn_mfma_f32_16x16x32_bf16(ones, pf, z, 0, 0, 0);
        o0[n] = __builtin_amdgcn_mfma_f32_16x16x32_bf16(__builtin_bit_cast(bf16x8, va), pf, z, 0, 0, 0);
        o1[n] = __builtin_amdgcn_mfma_f32_16x16x32_bf16(__builtin_bit_cast(bf16x8, vb), pf, z, 0, 0, 0);
      }
      if (grp == 0 && t0) { SB0(); wpair(2, 1); }
      WS(44);
#pragma unroll
      for (int n = 0; n < 2; ++n) {
        const float inv = __builtin_amdgcn_rcpf(os[n][0]);
        const f32x4 n0 = o0[n] * inv, n1 = o1[n] * inv;
        putf(112 + ntile + wc, t0 + n, u32x4{pack2(n0[0], n0[1]), pack2(n0[2], n0[3]), pack2(n1[0], n1[1]), pack2(n1[2], n1[3])});
      }
    };
    TS1(5);
    if (grp == 0) wpair(1, 0);
    tiles(2, &kx[0], &vx[0], false);
    constexpr int HL = 131072, HS = DMP + 1024;
    auto hload = [&]() {
#pragma unroll
      for (int r = 0; r < 9; ++r) GLDS_SC1(hptr + r * 1024 + ln * 16, shm_base + (r < 8 ? HL + wc * 8192 + r * 1024 : HS + wc * 2048));
    };
    if (needh) { WS(43); await(0xffffu, 0x404u); hload(); }
    if (grp) { __builtin_amdgcn_s_setprio(PRL1); gel2(3, 2 * (kt0 + 5 + he) + wc, 0, 2, 0, true, 43); __builtin_amdgcn_s_setprio(PRL0); }
    else {
      WAIT_V(4);
      peek();
      aquad(0);
    }
    TS1(6);
    if (!grp || wm > 0) {
      await(0x8000u << vid, 0x8000u << vid);
#pragma unroll
      for (int i = 0; i < 4; ++i) hx[i] = *(const u32x4*)(shm + (vid - 1) * 4096 + i * 1024 + ln * 16);
    } else if (!needh) {
#pragma unroll
      for (int i = 0; i < 4; ++i) hx[i] = u32x4{0u, 0u, 0u, 0u};
    } else {
      bool late = false;
      WS(44);
      WAIT_V(2);
      for (;;) {
        const char* const hl = shm + HL + wc * 8192 + ln * 16;
        const u32x4 sp = *(const u32x4*)(shm + HS + wc * 2048 + ln * 16);
        unsigned df = 0;
#pragma unroll
        for (int i = 0; i < 4; ++i) {
          const u32x4 g0 = *(const u32x4*)(hl + i * 2048), g1 = *(const u32x4*)(hl + i * 2048 + 1024);
          df |= (g0[1] ^ sp[0]) | (g0[3] ^ sp[0]) | (g1[1] ^ sp[0]) | (g1[3] ^ sp[0]);
          hx[i] = u32x4{g0[0], g0[2], g1[0], g1[2]};
        }
        if (__builtin_expect(__all((df == 0) & fresh(((u64)sp[1] << 32) | sp[0], t_own)), 1)) break;
        late = __builtin_amdgcn_s_memrealtime() - t_own > TIMEOUT_;
        if (late) break;
        __builtin_amdgcn_s_sleep(1);
        WAIT_L(0);
        hload();
        WAIT_V(0);
      }
      if (__builtin_expect(late, 0)) {
        char* const fb = shm + HL + wc * 8192 + ln * 16;
#pragma unroll 1
        for (int i = 0; i < 32; ++i) {
          const int rec = i >> 3, e = i & 7;
          const int row = m0 - 32 + 2 * (rec < 2 ? li : lq * 4 + (e & 3)) + (rec & 1);
          const int col = (rec < 2 ? D_ + lq * 4 + (e & 3) : 2 * D_ + li) + (ntile + wc) * 32 + (e >> 2) * 16;
          const u16* xr = x + (size_t)row * D_;
          const unsigned stt = row_stats_serial(xr);
          const float mb = lo_f(stt);
          float a = 0.f;
#pragma unroll 1
          for (int k = 0; k < D_; ++k) a += (bf2f(xr[k]) - mb) * bf2f(wqkv[(size_t)k * 3072 + col]);
          *(u16*)(fb + rec * 1024 + e * 2) = f2bf(a / hi_f(stt));
        }
#pragma unroll
        for (int i = 0; i < 4; ++i) hx[i] = *(const u32x4*)(fb + i * 1024);
      }
    }
    tiles(0, &hx[0], &hx[2], grp && mtile == 0 && wm == 0);
    if (grp) { __builtin_amdgcn_s_setprio(PRL1); gel2(3, 2 * (kt0 + 5 + he) + wc, 2, 4, 0, true, 44); }
    else WAIT_V(7);
  }
  TS1(2);
  auto stamp = [&](u64* p) {
    WAIT_V(0);
    u64 now;
    asm volatile("s_memrealtime %0\n\ts_waitcnt lgkmcnt(0)" : "=s"(now) :: "memory");
    if ((threadIdx.x & 63) == 0) __hip_atomic_store(p, now, RLX_AGENT);
  };
  TS1(3);
  const bool cok = cokw != 0u;
  {
  __builtin_amdgcn_sched_barrier(0);
  int tid = threadIdx.x & 255;
  asm volatile("" : "+v"(tid));
  __builtin_amdgcn_sched_barrier(0);
  const int lane = tid & 63;
  const int wm = (wid >> 1) & 1, wn = wid & 1;
  const int mtile = mt2, ntile = nt2;
  const int m0 = mtile * 128, n0 = ntile * 128;
  TS(0);
  constexpr int EP = 288;
  char* const img = shm + ((ny + 13) % G2_NSTAGE) * G2_STAGE;
  if constexpr (!LD) {
    WS(45);
    const int p = wid;
    const u64* const mbox = &done1[bid * 64 + lane];
    auto stage_s = [&]() {
      unsigned t;
      asm volatile(G2_A G2_W : [t] "=&s"(t) : [l] "s"(sl), [oa] "s"((p >> 1) * 8192 + (p & 1) * 2048), [a] "s"(ra2), [va] "v"(va), G2_WOP : "memory", "scc", "m0");
      adv();
    };
    u64 ren = 0;
    auto need = [&]() {
      while (!((okb >> q) & 1)) {
        digest(__hip_atomic_load(mbox, RLX_AGENT));
        if (!((okb >> q) & 1)) {
          __builtin_amdgcn_s_sleep(PSLEEP);
          if (__builtin_expect(!cok, 0)) notify(ren);
          if (GIVEUP()) break;
        }
      }
      asm volatile("" ::: "memory");
    };
    __builtin_amdgcn_s_setprio(0);
    if ((okb & 7) != 7) {
      const unsigned done = (unsigned)okb;
#pragma unroll 1
      for (q = 0; q < 3; ++q)
        if (!((done >> q) & 1)) {
          need();
          aquad(q);
        }
      if (done & 4) WAIT_V(0);
      WAIT_V(4);
    }
    TS(4);
    BARL();
    WS(45);
    need();
    stage_y();
    WAIT_V(8);
    peek();
    BAR();
    ren = myh << 32;
    if (p == 1) notify(ren);
    WS(45);
    need();
    stage_y();
    u64 fl = 0;
    auto check = [&]() {
      if (__builtin_expect(okb != ~0ull, 0)) {
        WS(45);
        if (!(q & 1)) {
          asm volatile("" : "+v"(fl));
          digest(fl);
          if (okb != ~0ull) asm volatile("global_load_dwordx2 %0, %1, off sc1" : "+v"(fl) : "v"(mbox) : "memory");
        }
        need();
      }
    };
    WS(45);
#pragma unroll 1
    while (q < ny) {
      WAIT_V(16);
      BAR();
      if (__builtin_expect(q == PD + 1, 0)) {
        if (p == 0) notify(ren ^ 0xffffffff00000000ull);
        ren = 0xffffffff00000000ull;
      }
      check();
      stage_y();
    }
    WS(45);
    va = lane * 16 + (p >> 1) * 8192 + role * 4096 + (p & 1) * 2048 + (56 << 14);
    wsetup(wo, D_, F_, lane);
    const u64* const pf = done2 + (bid ^ 5) * 8 + (lane & 3);
    u64 f2 = 0;
    WS(45);
#pragma unroll 1
    for (int g = 0; g < 16; ++g) {
      WAIT_V(12);
      BAR();
      check();
      if (__builtin_expect(g == 14, 0)) asm volatile("global_load_dwordx2 %0, %1, off sc1" : "+v"(f2) : "v"(pf) : "memory");
      stage_s();
    }
    WAIT_V(12);
    BAR();
    WS(45);
    {
      asm volatile("" : "+v"(f2), "+v"(fl));
      while (!__all(fresh(f2, t_own))) {
        if (__builtin_expect(!cok, 0)) { notify(ren); if (lane < 4) __hip_atomic_store(done2 + bid * 8 + lane, __builtin_amdgcn_s_memrealtime(), RLX_AGENT); }
        __builtin_amdgcn_s_sleep(1);
        f2 = __hip_atomic_load(pf, RLX_AGENT);
        if (GIVEUP()) break;
      }
      asm volatile("" ::: "memory");
      const char* ps = (const char*)slab + (size_t)(bid ^ 5) * 16384 + p * 4096 + lane * 16;
#pragma unroll
      for (int i = 0; i < 4; ++i) GLDS_SC1(ps + i * 1024, sl + p * 4096 + i * 1024);
    }
    WS(45);
    WAIT_V(10);
    BAR();
    WAIT_V(4);
    BAR();
    WAIT_V(0);
    BAR();
    u32x4 xv[4];
#pragma unroll
    for (int i = 0; i < 4; ++i) {
      const int c = i * 256 + tid, r = c >> 4;
      xv[i] = *(const u32x4*)(x + (size_t)(m0 + (r >> 5) * 64 + role * 32 + (r & 31)) * D_ + n0 + (c & 15) * 8);
    }
    WS(45);
    BAR();
#pragma unroll
    for (int i = 0; i < 4; ++i) {
      const int c = i * 256 + tid, r = c >> 4;
      const u32x4 tv = *(const u32x4*)(img + ((r & 32) + (r & 1) * 16 + ((r >> 1) & 15)) * EP + (c & 15) * 16);
      u32x4 ov;
      if (i == 2) WS(45);
#pragma unroll
      for (int e = 0; e < 4; ++e) ov[e] = pack2(lo_f(xv[i][e]) + lo_f(tv[e]), hi_f(xv[i][e]) + hi_f(tv[e]));
      __builtin_nontemporal_store(ov, (u32x4*)(out + (size_t)(m0 + (r >> 5) * 64 + role * 32 + (r & 31)) * D_ + n0 + (c & 15) * 8));
    }
    TS(5);
    if (tid == 0) __hip_atomic_store(&acks[bid], __builtin_amdgcn_s_memrealtime(), RLX_AGENT);
    if (__builtin_expect(!cok, 0) && p == 0) {
      const bool mine = lane <= 32;
      const u64* const pa = &acks[lane < 32 ? client(lane) : bid ^ 5];
      while (!__all(!mine || fresh(__hip_atomic_load(pa, RLX_AGENT), t_own))) {
        notify(ren);
        if (lane < 4) __hip_atomic_store(done2 + bid * 8 + lane, __builtin_amdgcn_s_memrealtime(), RLX_AGENT);
        __builtin_amdgcn_s_sleep(32);
        if (GIVEUP()) break;
      }
    }
  } else {
    WS(45);
    TS(6);
    f32x4 acc[4][4], sacc[2][4];
    const int fr = lane & 15, fq = lane >> 4, q4 = fr >> 2, pp = lane & 3;
    const int ff = ((q4 >> 1) & 1) | ((fq & 1) << 1);
    int aof = G2_STAGE + wm * 8192 + lane * 16;
    int boff[4];
#pragma unroll
    for (int j = 0; j < 4; ++j) boff[j] = G2_STAGE + 16384 + wn * 8192 + (8 * fq + q4) * 128 + ((j ^ ff) * 32) + pp * 8;
#pragma unroll
    for (int b = 0; b < 4; ++b) {
#pragma unroll
      for (int a = 0; a < 4; ++a) acc[a][b] = f32x4{0.f, 0.f, 0.f, 0.f};
      sacc[0][b] = f32x4{0.f, 0.f, 0.f, 0.f}; sacc[1][b] = f32x4{0.f, 0.f, 0.f, 0.f};
    }
#define SB() __builtin_amdgcn_sched_barrier(0)
    struct Fr { bf16x8 a[4]; bf16x4 bl[4], bh[4]; };
    Fr f0, f1;
    auto rd = [&](Fr& f, lds_cptr sb, int ks, int t, bool sp) {
      constexpr int ord[12] = {0, 1, 8, 2, 3, 4, 5, 6, 7, 9, 10, 11};
      const int o = ord[t];
      if (o >= 8) f.a[o - 8] = *(const __attribute__((address_space(3))) bf16x8*)(sb + aof + (o - 8) * 2048 + ks * 1024);
      else if (o & 1) f.bh[o >> 1] = tr_read(sb + boff[o >> 1] + ks * 4096 + 512);
      else f.bl[o >> 1] = tr_read(sb + boff[o >> 1] + ks * 4096);
    };
    auto bfr = [&](const Fr& c, int j) { return __builtin_shufflevector(c.bl[j], c.bh[j], 0, 1, 2, 3, 4, 5, 6, 7); };
    auto half = [&](const Fr& c, Fr& n, int ks) {
      lds_cptr sb = shm3;
#pragma unroll
      for (int t = 0; t < 16; ++t) {
        const int mf = t >> 2, j = t & 3;
        SB();
        acc[mf][j] = __builtin_amdgcn_mfma_f32_16x16x32_bf16(bfr(c, j), c.a[mf], acc[mf][j], 0, 0, 0);
        SB();
        if (t < 12) rd(n, sb, ks, t, false);
      }
      SB();
    };
    auto rds = [&](Fr* n, lds_cptr sb, int t) {
      if (t < 10) { rd(n[t / 5], sb, t / 5, 2 * (t % 5), true); rd(n[t / 5], sb, t / 5, 2 * (t % 5) + 1, true); }
    };
    auto stg = [&](const Fr* c, Fr* n, bool load = true) {
      lds_cptr sb = shm3;
#pragma unroll
      for (int t = 0; t < 16; ++t) {
        const int ks = t >> 3, mf = (t >> 2) & 1, j = t & 3;
        SB();
        sacc[mf][j] = __builtin_amdgcn_mfma_f32_16x16x32_bf16(bfr(c[ks], j), c[ks].a[mf], sacc[mf][j], 0, 0, 0);
        SB();
        if (load) rds(n, sb, t);
      }
      SB();
    };
    asm volatile("" : "+v"(aof), "+v"(boff[0]), "+v"(boff[1]), "+v"(boff[2]), "+v"(boff[3]));
    BARL();
    TS(7);
    __builtin_amdgcn_s_waitcnt(0xC07F);
    SB();
#pragma unroll
    for (int t = 0; t < 12; ++t) rd(f0, shm3, 0, t, false);
    int so = G2_STAGE;
    auto nxt = [&]() {
      const int d = so == (G2_NSTAGE - 1) * G2_STAGE ? -(G2_NSTAGE - 1) * G2_STAGE : G2_STAGE;
      so += d; aof += d; boff[0] += d; boff[1] += d; boff[2] += d; boff[3] += d;
      asm volatile("" : "+v"(aof), "+v"(boff[0]), "+v"(boff[1]), "+v"(boff[2]), "+v"(boff[3]));
    };
    WS(45);
#pragma unroll 1
    for (int g = 0; g < ny - 1; ++g) {
      half(f0, f1, 1);
      nxt();
      BAR();
      half(f1, f0, 0);
      WAIT_V(0);
    }
    WS(45);
    half(f0, f1, 1);
    nxt();
    WS(45);
    BAR();
    Fr sa[2], sb2[2];
#pragma unroll
    for (int t = 0; t < 16; ++t) {
      const int mf = (t >> 2) ^ 2, j = t & 3;
      SB();
      acc[mf][j] = __builtin_amdgcn_mfma_f32_16x16x32_bf16(bfr(f1, j), f1.a[mf], acc[mf][j], 0, 0, 0);
      SB();
      rds(sa, shm3, t);
    }
    SB();
    asm volatile("" : "+v"(acc[0][0]), "+v"(acc[0][1]), "+v"(acc[0][2]), "+v"(acc[0][3]), "+v"(acc[1][0]), "+v"(acc[1][1]), "+v"(acc[1][2]), "+v"(acc[1][3]));
    WS(45);
    TS(2);
    {
      const __amdgpu_buffer_rsrc_t rs = __builtin_amdgcn_make_buffer_rsrc((void*)((char*)slab + (size_t)bid * 16384), 0, 16384, 0x00020000);
#pragma unroll
      for (int f = 0; f < 4; ++f) {
        const f32x4 a = acc[2 + (f >> 1)][(f & 1) * 2], b = acc[2 + (f >> 1)][(f & 1) * 2 + 1];
        __builtin_amdgcn_raw_buffer_store_b128(u32x4{pack2(a[0], a[1]), pack2(a[2], a[3]), pack2(b[0], b[1]), pack2(b[2], b[3])}, rs,
                                               (f * 256 + tid) * 16, 0, 16);
      }
    }
    SB();
    WS(45);
#pragma unroll 1
    for (int i = 0; i < 7; ++i) {
      nxt();
      BAR();
      stg(sa, sb2);
      nxt();
      WS(45);
      BAR();
      stg(sb2, sa);
      if (i == 2) {
        stamp(done2 + bid * 8 + (wid & 3));
      }
    }
    WS(45);
    nxt();
    BAR();
    stg(sa, sb2);
    WS(45);
    BAR();
    u32x4 pv[4];
    {
      nxt();
      lds_cptr sb = shm3 + so + tid * 16;
#pragma unroll
      for (int f = 0; f < 4; ++f) pv[f] = *(const __attribute__((address_space(3))) u32x4*)(sb + f * 4096);
    }
    stg(sb2, sa, false);
    TS(1);
#pragma unroll
    for (int mf = 0; mf < 2; ++mf)
#pragma unroll
      for (int j = 0; j < 4; ++j) {
        if (!(j & 1)) WS(41);
        const u32x4 pw = pv[mf * 2 + (j >> 1)];
        const f32x4 sv = sacc[mf][j];
        const f32x4 ya = acc[mf][j] + f32x4{lo_f(pw[2 * (j & 1)]), hi_f(pw[2 * (j & 1)]), lo_f(pw[2 * (j & 1) + 1]), hi_f(pw[2 * (j & 1) + 1])};
        u32x2 o;
#pragma unroll
        for (int e = 0; e < 2; ++e) {
          const f32x2 y2 = {ya[2 * e], ya[2 * e + 1]}, s2 = {sv[2 * e], sv[2 * e + 1]};
          const f32x2 ar = y2 * f32x2{-1.4426950408889634f, -1.4426950408889634f};
          const f32x2 dn = f32x2{__builtin_amdgcn_exp2f(ar[0]), __builtin_amdgcn_exp2f(ar[1])} + f32x2{1.0f, 1.0f};
          const f32x2 t = (s2 * y2) * f32x2{__builtin_amdgcn_rcpf(dn[0]), __builtin_amdgcn_rcpf(dn[1])};
          o[e] = pack2(t[0], t[1]);
        }
        *(u32x2*)(img + (wm * 32 + mf * 16 + fr) * EP + (wn * 64 + j * 16 + fq * 4) * 2) = o;
      }
    BARL();
    TS(3);
  }
  TSFLUSH();
  }
  };
  if (wid < 4) kloop(std::integral_constant<int, 0>{});
  else kloop(std::integral_constant<int, 1>{});
  asm volatile("s_branch .Lcode_end\n41:\n42:\n43:\n44:\n45:\n46:\n\ts_branch .Lwarm_ret4\n51:\n52:\n53:\n54:\n55:\n56:\n\ts_branch .Lwarm_ret5\n.Lcode_end:");
}

extern "C" void kernel_launch(void* const* d_in, const int* in_sizes, int n_in, void* d_out, int out_size, void* d_ws,
                              size_t ws_size, hipStream_t stream) {
  const u16* A_x = (const u16*)d_in[0];
  const u16* A_wqkv = (const u16*)d_in[1];
  const u16* A_wo = (const u16*)d_in[2];
  const u16* B_wg = (const u16*)d_in[4];
  const u16* B_wu = (const u16*)d_in[5];
  const u16* B_wd = (const u16*)d_in[6];
  u16* out = (u16*)d_out;
  fused_kernel<<<256, 512, 0, stream>>>(A_x, A_wqkv, B_wg, B_wu, B_wd, A_wo, out, (char*)d_ws);
}
```

```cpp
#include <hip/hip_runtime.h>
#include <hip/hip_bf16.h>
#include <stdint.h>
#include <type_traits>
#include <utility>

typedef unsigned short u16;
using bf16x8 = __attribute__((ext_vector_type(8))) short;
using bf16x4 = __attribute__((ext_vector_type(4))) short;
using f32x4 = __attribute__((ext_vector_type(4))) float;
using u32x2 = __attribute__((ext_vector_type(2))) unsigned;
using u32x4 = __attribute__((ext_vector_type(4))) unsigned;
typedef __attribute__((address_space(3))) const char* lds_cptr;

template <class F, int... I> __device__ __forceinline__ void sfor_(F&& f, std::integer_sequence<int, I...>) { (f(std::integral_constant<int, I>{}), ...); }
template <int N, class F> __device__ __forceinline__ void sfor(F&& f) { sfor_(f, std::make_integer_sequence<int, N>{}); }

constexpr int S_ = 2048, D_ = 1024, F_ = 3584;

__device__ __forceinline__ float bf2f(u16 h) { return __uint_as_float((unsigned)h << 16); }
__device__ __forceinline__ u16 f2bf(float f) {
  unsigned u = __float_as_uint(f);
  u += 0x7fffu + ((u >> 16) & 1u);
  return (u16)(u >> 16);
}
__device__ __forceinline__ float rbf(float f) { return bf2f(f2bf(f)); }
typedef __attribute__((ext_vector_type(2))) __bf16 bf16x2_t;
typedef __attribute__((ext_vector_type(2))) float f32x2;
__device__ __forceinline__ unsigned pack2(float a, float b) {
  bf16x2_t r = __builtin_convertvector((f32x2){a, b}, bf16x2_t);
  return __builtin_bit_cast(unsigned, r);
}
__device__ __forceinline__ float lo_f(unsigned p) { return __uint_as_float(p << 16); }
__device__ __forceinline__ float hi_f(unsigned p) { return __uint_as_float(p & 0xffff0000u); }

#define TSDECL
#define TSS(i) do {} while (0)
#define TSFLUSH() do {} while (0)
#define TS1(i) TSS(i)
#define TS(i) TSS(8 + (i))
#define WAIT_V(n) asm volatile("s_waitcnt vmcnt(%0)" ::"n"(n) : "memory")
#define WAIT_L(n) asm volatile("s_waitcnt lgkmcnt(%0)" ::"n"(n) : "memory")
#define BARL() do { WAIT_L(0); BAR(); } while (0)
#define BAR() do { asm volatile("" ::: "memory"); __builtin_amdgcn_s_barrier(); asm volatile("" ::: "memory"); } while (0)

__device__ __forceinline__ bf16x4 tr_read(lds_cptr p) {
  return __builtin_amdgcn_ds_read_tr16_b64_v4i16((__attribute__((address_space(3))) bf16x4*)p);
}
__device__ __forceinline__ bf16x8 tr_frag(lds_cptr p) {
  bf16x4 lo = tr_read(p), hi = tr_read(p + 512);
  bf16x8 r;
  r[0] = lo[0]; r[1] = lo[1]; r[2] = lo[2]; r[3] = lo[3];
  r[4] = hi[0]; r[5] = hi[1]; r[6] = hi[2]; r[7] = hi[3];
  return r;
}
__device__ __forceinline__ void glds16(const void* gsrc, unsigned lds_dst) {
  unsigned keep;
  asm volatile("s_mov_b32 %0, m0\n\ts_mov_b32 m0, %2\n\ts_nop 0\n\tglobal_load_lds_dwordx4 %1, off\n\ts_mov_b32 m0, %0"
               : "=&s"(keep) : "v"(gsrc), "s"(lds_dst) : "memory");
}
#define GLDS(gptr, laddr) glds16((const void*)(gptr), (unsigned)(laddr))
__device__ __forceinline__ void glds16_sc1(const void* gsrc, unsigned lds_dst) {
  unsigned keep;
  asm volatile("s_mov_b32 %0, m0\n\ts_mov_b32 m0, %2\n\ts_nop 0\n\tglobal_load_lds_dwordx4 %1, off sc1\n\ts_mov_b32 m0, %0"
               : "=&s"(keep) : "v"(gsrc), "s"(lds_dst) : "memory");
}
#define GLDS_SC1(gptr, laddr) glds16_sc1((const void*)(gptr), (unsigned)(laddr))
__device__ __forceinline__ void glds16_nc(const void* gsrc, unsigned lds_dst) {
  unsigned keep;
  asm volatile("s_mov_b32 %0, m0\n\ts_mov_b32 m0, %2\n\ts_nop 0\n\tglobal_load_lds_dwordx4 %1, off\n\ts_mov_b32 m0, %0"
               : "=&s"(keep) : "v"(gsrc), "s"(lds_dst));
}
#define GLDS_NC(gptr, laddr) glds16_nc((const void*)(gptr), (unsigned)(laddr))
__device__ __forceinline__ void glds16_s(const void* sbase, unsigned voff, unsigned lds_dst) {
  asm volatile("s_mov_b32 m0, %2\n\ts_nop 0\n\tglobal_load_lds_dwordx4 %0, %1" :: "v"(voff), "s"(sbase), "s"(lds_dst) : "m0");
}

__device__ __forceinline__ void glds16_p(const void* sbase, unsigned voff, unsigned lds_dst) {
  asm volatile("s_mov_b32 m0, %2\n\ts_nop 3\n\tglobal_load_lds_dwordx4 %0, %1" :: "v"(voff), "s"(sbase), "s"(lds_dst) : "m0");
}

typedef unsigned long long u64;
__device__ __forceinline__ bool fresh(u64 stamp, u64 t_own) {
  return stamp - t_own - 100ull < __builtin_amdgcn_s_memrealtime() - t_own;
}
constexpr u64 TIMEOUT_ = 200000;
#define GIVEUP() (__builtin_amdgcn_s_memrealtime() - t_own > 100000000ull)
__device__ __forceinline__ float half_sum(float v) {
  v += __builtin_bit_cast(float, __builtin_amdgcn_update_dpp(0, __builtin_bit_cast(int, v), 0xB1, 0xf, 0xf, false));
  v += __builtin_bit_cast(float, __builtin_amdgcn_update_dpp(0, __builtin_bit_cast(int, v), 0x4E, 0xf, 0xf, false));
  v += __builtin_bit_cast(float, __builtin_amdgcn_update_dpp(0, __builtin_bit_cast(int, v), 0x141, 0xf, 0xf, false));
  v += __builtin_bit_cast(float, __builtin_amdgcn_update_dpp(0, __builtin_bit_cast(int, v), 0x140, 0xf, 0xf, false));
  { auto r = __builtin_amdgcn_permlane16_swap(__float_as_uint(v), __float_as_uint(v), false, false); v = __uint_as_float(r[0]) + __uint_as_float(r[1]); }
  return v;
}
__device__ __forceinline__ unsigned pack_stats(float mean, float var) {
  return (unsigned)f2bf(mean) | ((unsigned)f2bf(sqrtf(rbf(rbf(var) + 1e-5f))) << 16);
}
__device__ __forceinline__ unsigned row_stats_serial(const u16* xr) {
  float s = 0.f, q = 0.f;
#pragma unroll 1
  for (int k = 0; k < D_; ++k) s += bf2f(xr[k]);
  const float mean = s * (1.0f / D_);
#pragma unroll 1
  for (int k = 0; k < D_; ++k) { const float d = bf2f(xr[k]) - mean; q += d * d; }
  return pack_stats(mean, q * (1.0f / D_));
}
constexpr int G1_STAGE = 32768 + 5 * 8192;
#define PF_K0 6
#define CP_K0 5
#define DLT 1
#define PSLEEP 2
#define SD 6
#define NSC 2
#define PRB 0
#define PRL0 3
#define PRL1 1
#define NWPF 4
#define SNP 3
#define RD0 21
#define NP1 8
#define ESH 1
#define EROW 0
#define KS0 2
#define PUB_K 4

#define RLX_AGENT __ATOMIC_RELAXED, __HIP_MEMORY_SCOPE_AGENT

constexpr int G2_STAGE = 16384 + 2 * 8192;
constexpr int G2_NSTAGE = 5;
constexpr size_t MB_ = 1024 * 1024;
__global__ __launch_bounds__(512, 2) void fused_kernel(const u16* __restrict__ x, const u16* __restrict__ wqkv,
                                                       const u16* __restrict__ wg, const u16* __restrict__ wu,
                                                       const u16* __restrict__ wd, const u16* __restrict__ wo,
                                                       u16* __restrict__ out, char* __restrict__ ws) {
  u16* const halo = (u16*)(ws + 4 * MB_);
  u16* const abuf = (u16*)(ws + 16 * MB_);
  char* const slab = ws + 64 * MB_;
  unsigned* const flags = (unsigned*)(ws + 96 * MB_);
  u32x4* const rstat = (u32x4*)(ws + 97 * MB_);
  char* const wcopy = ws + 98 * MB_;
  u64* const done1 = (u64*)(ws + 100 * MB_);
  u64* const done2 = (u64*)(ws + 100 * MB_ + 131072);
  u64* const donec = (u64*)(ws + 100 * MB_ + 196608);
  u64* const acks = (u64*)(ws + 100 * MB_ + 262144);
  u64* const cstamp = (u64*)(ws + 100 * MB_ + 327680);
  float* const csum = (float*)(ws + 101 * MB_);
  __shared__ __attribute__((aligned(1024))) char shm[163840];
  constexpr int AUX = 2 * G1_STAGE + 8192;
  {
    u64 pc; int len;
    asm volatile("s_getpc_b64 %0\n.Lcode_beg:\n\ts_mov_b32 %1, .Lcode_end-.Lcode_beg" : "=s"(pc), "=s"(len));
    const int ol = ((blockIdx.x >> 3) + 32 * (threadIdx.x >> 6)) * 1024 + (threadIdx.x & 63) * 16;
    asm volatile("s_mov_b32 m0, %2\n\ts_nop 0\n\tglobal_load_lds_dwordx4 %0, %1"
                 :: "v"(ol < len - 16 ? ol : 0), "s"(pc & ~15ull), "s"((unsigned)(uintptr_t)(lds_cptr)shm + 2 * G1_STAGE + 7168) : "m0");
    __builtin_amdgcn_sched_barrier(0);
  }
  const u64 t_own = __builtin_amdgcn_s_memrealtime();
  TSDECL;
  TS1(7);
  const int tid = threadIdx.x, lane = tid & 63;
  const int wid = __builtin_amdgcn_readfirstlane(tid >> 6);
  const int bid = blockIdx.x, xcd = bid & 7, idx = bid >> 3;
  const int ntile = xcd * 4 + (idx & 3), mtile = idx >> 2;
  const int m0 = mtile * 256;
  const int hg = ntile >> 1;
  const bool tya = (ntile & 1) == 0;
  const int grp = wid >> 2, wc = (wid >> 1) & 1, wm = (((grp ^ ntile ^ 1) & 1) << 1) | (wid & 1), vid = wc * 4 + wm;
  const int srow = m0 + ntile * 8 + (wid & 3) * 2;
  lds_cptr shm3 = (lds_cptr)shm;
  const unsigned shm_base = (unsigned)(uintptr_t)shm3;
  const int r8 = tid >> 3, pos = tid & 7;
  const int phys = pos >> 1, half = pos & 1;
  const int fk = ((r8 >> 1) & 1) | (((r8 >> 3) & 1) << 1);
  const int vsrc = phys ^ fk;
  const int qstr = tya ? 3072 * 2 : F_ * 2;
  unsigned voa = (((r8 & 32) + 2 * (r8 & 15) + ((r8 >> 4) & 1)) * D_ + ((pos ^ (r8 & 7)) * 8)) * 2;
  unsigned vog = r8 * (F_ * 2) + vsrc * 32 + half * 16, voq = r8 * qstr + vsrc * 32 + half * 16;
  unsigned vom = r8 * qstr + (vsrc >> (tya ? 0 : 1)) * 32 + half * 16;
  asm volatile("" : "+v"(voa), "+v"(vog), "+v"(voq), "+v"(vom));
  asm volatile("" :: "s"(x), "s"(wqkv), "s"(wg), "s"(wu), "s"(wd), "s"(wo), "s"(out), "s"(ws));
  const char* const ab = (const char*)(x + (size_t)m0 * D_);
  int kz = 0;
  asm volatile("" : "+s"(kz));
  auto stage_a = [&](int buf, int kt, int i, int h) {
    glds16_s(ab + (long)(((i * 64 + (h - 1) * 32) * D_ + kt * 64) * 2), voa, shm_base + buf * G1_STAGE + (wid - 4 + 4 * h) * 1024 + i * 8192);
  };
#pragma unroll
  for (int i = 0; i < 4; ++i) stage_a(0, 0, i, 1);
  const int kt0 = (hg >> 1) * 7, he = hg & 1;
  const char* const gb = (const char*)(wg + (kt0 + (tya ? 5 + he : 2 + he)) * 64);
  const char* const qb = tya ? (const char*)(wqkv + 64 * hg) : gb - 256;
  const long dgu = (const char*)wu - (const char*)wg, dp1 = tya ? 2048 : dgu;
  const char* const mb0 = tya ? qb + 4096 : (const char*)(wg + (kt0 + 4) * 64 + 32 * he);
  const char* mptr = mb0 + (((vsrc & 1) && !tya) ? dgu : 0) + vom;
  auto wload = [&](int p, int rows, unsigned dst) {
    long off = (long)(rows * (p < 3 ? qstr : F_ * 2)) + (p == 1 ? dp1 : (p == 4 ? dgu : 0));
    if (p == 2) { asm volatile("" : "+s"(off)); GLDS_NC(mptr + off, dst); }
    else glds16_s((p < 2 ? qb : gb) + off, p < 2 ? voq : vog, dst);
  };
#pragma unroll
  for (int i = 0; i < 5; ++i) wload(i, 0, shm_base + wid * 1024 + 32768 + i * 8192);
  __builtin_amdgcn_sched_barrier(0);
  const int fr = lane & 15, fq = lane >> 4, q4 = fr >> 2, pp = lane & 3;
  const int ff = ((q4 >> 1) & 1) | ((fq & 1) << 1);
  int aoff[2], boff[2];
#pragma unroll
  for (int ks = 0; ks < 2; ++ks) aoff[ks] = (wm * 64 + fr) * 128 + (((ks * 4 + fq) ^ (fr & 7)) * 16);
#pragma unroll
  for (int j = 0; j < 2; ++j) boff[j] = 32768 + (8 * fq + q4) * 128 + (((2 * wc + j) ^ ff) * 32) + pp * 8;

  f32x4 acc[4][10];
#pragma unroll
  for (int a = 0; a < 4; ++a)
#pragma unroll
    for (int b = 0; b < 10; ++b) acc[a][b] = f32x4{0.f, 0.f, 0.f, 0.f};

  auto stage_part = [&](int buf, int kt, int i, int h) {
    if (i < 5) wload(i, kt * 64 + (h - 1) * 32, shm_base + buf * G1_STAGE + (wid - 4 + 4 * h) * 1024 + 32768 + i * 8192);
    else stage_a(buf, kt, i - 5, h);
  };
  constexpr int NT = D_ / 64;
  constexpr int DMP = 2 * G1_STAGE;
  constexpr int CF = AUX + 4096, QS = CF + 1280, CNT = CF + 3840;
  bf16x8 af0[4], af1[4];
  bf16x4 bl[10], bh[10];
#pragma unroll
  for (int i = 0; i < 4; ++i) { af1[i] = bf16x8{0, 0, 0, 0, 0, 0, 0, 0}; bl[i] = bf16x4{0, 0, 0, 0}; bh[i] = bf16x4{0, 0, 0, 0}; }
#define SB0() __builtin_amdgcn_sched_barrier(0)
  auto mm = [&](int n, int f, int ks, int mf) {
    const bf16x8 b = __builtin_shufflevector(bl[n % 10], bh[n % 10], 0, 1, 2, 3, 4, 5, 6, 7);
    if ((f >> 1) == 2) acc[mf][f] = __builtin_amdgcn_mfma_f32_16x16x32_bf16(ks ? af1[mf] : af0[mf], b, acc[mf][f], 0, 0, 0);
    else acc[mf][f] = __builtin_amdgcn_mfma_f32_16x16x32_bf16(b, ks ? af1[mf] : af0[mf], acc[mf][f], 0, 0, 0);
  };
  auto correct = [&]() {
    int l = threadIdx.x & 63;
    asm volatile("s_nop 7" : "+v"(l));
    const float* const tb = (const float*)(shm + DMP + wm * 2048);
    const float* const cf = (const float*)(shm + CF) + wc * 160;
    auto ld = [&](int f) { return f < 8 ? *(const f32x4*)(cf + (f < 4 ? f : f + 2) * 16 + (l >> 4) * 4) : *(const f32x4*)(tb + (f - 8) * 16 + (l >> 4) * 4); };
    float m[4];
    f32x4 cr[3];
#pragma unroll
    for (int mf = 0; mf < 4; ++mf) m[mf] = tb[mf * 16 + (l & 15)];
    cr[0] = ld(0);
    cr[1] = ld(1);
    const float c4 = cf[64 + (l & 15)], c5 = cf[80 + (l & 15)];
#pragma unroll
    for (int f = 0; f < 12; ++f) {
      if (f + 2 < 12) cr[(f + 2) % 3] = ld(f + 2);
      SB0();
      const f32x4 c = cr[f % 3];
#pragma unroll
      for (int i = 0; i < (f < 8 ? 4 : 2); ++i) {
        f32x4& a = f < 8 ? acc[i][f < 4 ? f : f + 2] : acc[f - 8][4 + i];
        float a0 = a[0], a1 = a[1], a2 = a[2], a3 = a[3];
        asm volatile("v_fmac_f32 %0, %4, %5\n\tv_fmac_f32 %1, %4, %6\n\tv_fmac_f32 %2, %4, %7\n\tv_fmac_f32 %3, %4, %8"
                     : "+v"(a0), "+v"(a1), "+v"(a2), "+v"(a3) : "v"(f < 8 ? m[i] : (i ? c5 : c4)), "v"(c[0]), "v"(c[1]), "v"(c[2]), "v"(c[3]));
        a = f32x4{a0, a1, a2, a3};
      }
      SB0();
    }
    asm volatile("s_nop 1");
  };
  auto kloop = [&](auto wmc) __attribute__((always_inline)) {
    constexpr bool LD = decltype(wmc)::value != 0;
    constexpr int grp = LD ? 1 : 0;
    const int WID = wid;
    const char* pa = nullptr;
    unsigned vob = 0;
    f32x4 ca = {0.f, 0.f, 0.f, 0.f}, cb = {0.f, 0.f, 0.f, 0.f};
    bf16x8 sel = {0, 0, 0, 0, 0, 0, 0, 0};
    int tro = 0;
    u64 tg = 0;
    const int psh = (wid == 2 && !tya) ? 1 : 0, pstr = wid < 3 ? qstr : F_ * 2;
    const char* const pbase = wid == 0 ? qb : (wid == 1 ? qb + dp1 : (wid == 2 ? mb0 : gb));
    if (!LD) {
      const int rs = mtile * 8 + (lane >> 3), vs = ((lane & 7) >> 1) ^ (((rs >> 1) & 1) | ((mtile & 1) << 1));
      pa = pbase + ((psh & vs) ? dgu : 0) + (rs * pstr + (vs >> psh) * 32 + (lane & 1) * 16);
      vob = rs * (F_ * 2) + vs * 32 + (lane & 1) * 16;
      const short one = fr == fq ? 0x3f80 : 0;
      sel = bf16x8{one, one, one, one, one, one, one, one};
      tro = q4 * 128 + ((fq ^ ((q4 >> 1) | ((mtile & 1) << 1))) * 32) + pp * 8;
      asm volatile("" : "+v"(sel), "+v"(tro), "+v"(ca), "+v"(cb));
    }
    auto share = [&](int t, unsigned dsta, unsigned dstb) {
      long off = (long)(t * 64 * pstr);
      asm volatile("" : "+s"(off));
      GLDS_NC(pa + off, shm_base + dsta);
      if (wid == 0) glds16_p(gb + dgu + (long)(t * 64 * F_ * 2), vob, shm_base + dstb);
    };
    auto accum = [&](int oa, int ob) {
      ca = __builtin_amdgcn_mfma_f32_16x16x32_bf16(tr_frag(shm3 + tro + oa), sel, ca, 0, 0, 0);
      if (wid == 0) cb = __builtin_amdgcn_mfma_f32_16x16x32_bf16(tr_frag(shm3 + tro + ob), sel, cb, 0, 0, 0);
    };
    auto cgather = [&]() {
      int l = threadIdx.x & 63;
      asm volatile("s_memrealtime %0\n\ts_waitcnt lgkmcnt(0)" : "=s"(tg), "+v"(l) :: "memory");
      const char* const p4 = l < 32 ? (const char*)(csum + (ntile * 5 + 4) * 512 + wid * 16) + (l >> 2) * 256
                                    : (const char*)(cstamp + (ntile * 4 + (l < 36 ? wid : 0)) * 8);
      if (l < 40) GLDS_SC1(p4 + (l & 3) * 16, shm_base + QS + wid * 640);
#pragma unroll
      for (int i = 0; i < 2; ++i) GLDS_SC1((const char*)(csum + (ntile * 5 + wid) * 512) + i * 1024 + l * 16, shm_base + DMP + wid * 2048 + i * 1024);
    };
    if (!LD) {
#pragma unroll
      for (int i = 0; i < ESH; ++i) share(2 + i, DMP + 7168, DMP + 7168);
      if (EROW) {
#pragma unroll
        for (int i = 0; i < 2; ++i) glds16_p(x + (size_t)(srow + i) * D_, lane * 16, shm_base + AUX + wid * 2048 + i * 1024);
      }
    }
#pragma unroll
    for (int a = 0; a < 4; ++a)
      asm volatile("" : "+v"(acc[a][0]), "+v"(acc[a][1]), "+v"(acc[a][2]), "+v"(acc[a][3]), "+v"(acc[a][4]), "+v"(acc[a][5]), "+v"(acc[a][6]),
                   "+v"(acc[a][7]), "+v"(acc[a][8]), "+v"(acc[a][9]));
    asm volatile("" : "+v"(aoff[0]), "+v"(aoff[1]), "+v"(boff[0]), "+v"(boff[1]));
    TS1(3);
    if constexpr (LD) {
#pragma unroll
      for (int i = 0; i < NP1; ++i) stage_part(1, kz + 1, i >> 1, i & 1);
      TS1(4);
      WAIT_V(NP1);
    } else {
      TS1(4);
      if (wid) WAIT_V(ESH + 2 * EROW);
      WAIT_V(2 * ESH + 2 * EROW);
    }
    TS1(0);
#pragma clang loop unroll(disable)
    for (int kt = 0; kt < NT; ++kt) {
      const int cur = kt & 1;
      if (!LD && kt == NT - 1) { correct(); __builtin_amdgcn_s_setprio(2); }
      BARL();
      lds_cptr sb = shm3;
      if (LD) {
        unsigned k2 = kt - 1;
        asm volatile("" : "+s"(k2));
        if (k2 < NT - 2) {
#pragma unroll
          for (int i = 0; i < NP1; ++i) stage_part(cur ^ 1, kt + 1, i >> 1, i & 1);
        }
      }
      if (LD && kt + 1 < NT) {
#pragma unroll
        for (int i = NP1; i < 18; ++i) stage_part(cur ^ 1, kt + 1, i >> 1, i & 1);
      } else if (LD) {
        correct();
      }
      auto fadr = [&](int n) {
        const int f = n < 2 ? 8 + n : (n < 4 ? 11 - n : (n - 4) >> 1), ks = n < 2 ? 1 : (n < 4 ? 0 : n & 1);
        return sb + boff[f & 1] + (f >> 1) * 8192 + ks * 4096;
      };
      auto rdl = [&](int n) { bl[n % 10] = tr_read(fadr(n % 20)); };
      auto rdh = [&](int n) { bh[n % 10] = tr_read(fadr(n % 20) + 512); };
      auto rda = [&](bf16x8* af, int ks, int mf) { af[mf] = *(const __attribute__((address_space(3))) bf16x8*)(sb + aoff[ks] + mf * 2048); };
      SB0();
      __builtin_amdgcn_s_waitcnt(0xC07F);
      sfor<80>([&](auto uc) __attribute__((always_inline)) {
        constexpr int u = decltype(uc)::value;
        SB0();
        if constexpr (u < 16) {
          const int n = u >> 2;
          mm(n, n < 2 ? 8 + n : 11 - n, n < 2 ? 1 : 0, (n & 1) ? 3 - (u & 3) : u & 3);
        } else {
          const int f = (u - 16) >> 3, i = (u >> 1) & 3, ks = (u ^ i) & 1;
          if ((u & 7) == 0) { asm volatile("" :: "v"(bl[(5 + 2 * f) % 10]), "v"(bh[(5 + 2 * f) % 10])); SB0(); }
          mm(4 + 2 * f + ks, f, ks, (f & 1) ? 3 - i : i);
        }
        SB0();
        if (u == 0) { rdl(2); rdh(2); }
        if (u == 1) { rda(af0, 0, 0); rda(af0, 0, 1); }
        if (u == 2) { rda(af0, 0, 2); rda(af0, 0, 3); }
        if (u == 3) { rdl(3); rdh(3); }
        if constexpr (u >= 4 && u < 8) rda(af1, 1, 7 - u);
        if (u >= 8 && u < 16) { if (u & 1) rdh(u >> 1); else rdl(u >> 1); }
        if (u >= RD0 && u < RD0 + 55 && (u & 1)) {
          const int n = 8 + ((u - RD0) >> 2);
          if ((u - RD0) & 2) rdh(n); else rdl(n);
        }
      });
      SB0();
      {
        const int d = cur ? -G1_STAGE : G1_STAGE;
        aoff[0] += d; aoff[1] += d; boff[0] += d; boff[1] += d;
        asm volatile("" : "+v"(aoff[0]), "+v"(aoff[1]), "+v"(boff[0]), "+v"(boff[1]));
      }
      if (LD) WAIT_V(0);
      if (!LD && kt <= 19 - SD) {
        int lane = threadIdx.x & 63;
        asm volatile("" : "+v"(lane));
        auto clock = [&]() { u64 now; asm volatile("s_memrealtime %0\n\ts_waitcnt lgkmcnt(0)" : "=s"(now) :: "memory"); return now; };
        WAIT_V(0);
        if (kt <= 16 - SD) {
          if (kt >= 1 + KS0) { accum(DMP + wid * 1024, DMP + 4096); WAIT_L(0); }
          if (kt <= 15 - SD) {
            if (kt >= KS0) share(kt + SD, DMP + wid * 1024, DMP + 4096);
          } else if ((lane & 15) < 4) {
            const __amdgpu_buffer_rsrc_t rc = __builtin_amdgcn_make_buffer_rsrc((void*)(csum + ntile * 5 * 512 + mtile * 64), 0, 5 * 2048, 0x00020000);
            const int co = (lane & 15) * 64 + (lane >> 4) * 16;
            __builtin_amdgcn_raw_buffer_store_b128(__builtin_bit_cast(u32x4, ca), rc, co + wid * 2048, 0, 16);
            if (wid == 0) __builtin_amdgcn_raw_buffer_store_b128(__builtin_bit_cast(u32x4, cb), rc, co + 4 * 2048, 0, 16);
          }
          if (kt < SD + KS0) {
            if (kt <= SD + KS0 - 3 - ESH) share(kt + 2 + ESH, DMP + 7168, DMP + 7168);
            const int sp = cur * G1_STAGE + 32768 + mtile * 1024;
            accum(sp + wid * 8192, sp + 4 * 8192);
            if (kt == 1 - EROW || kt == 2 - EROW) {
              const bf16x8 ones = {0x3f80, 0x3f80, 0x3f80, 0x3f80, 0x3f80, 0x3f80, 0x3f80, 0x3f80};
              const char* const xr = shm + AUX + wid * 2048 + (lane & 8) * 128 + (lane >> 4) * 128 + (lane & 7) * 16;
              const bf16x8 r0 = *(const bf16x8*)xr, r1 = *(const bf16x8*)(xr + 512);
              const f32x4 z = {0.f, 0.f, 0.f, 0.f};
              const f32x4 s1 = __builtin_amdgcn_mfma_f32_16x16x32_bf16(ones, r1, __builtin_amdgcn_mfma_f32_16x16x32_bf16(ones, r0, z, 0, 0, 0), 0, 0, 0);
              const f32x4 s2 = __builtin_amdgcn_mfma_f32_16x16x32_bf16(r1, r1, __builtin_amdgcn_mfma_f32_16x16x32_bf16(r0, r0, z, 0, 0, 0), 0, 0, 0);
              float t0 = __uint_as_float((unsigned)tg) + s1[0];
              asm volatile("" : "+v"(t0));
              const float t1 = __uint_as_float((unsigned)(tg >> 32)) + ((lane & 2) ? ((lane & 1) ? s2[3] : s2[2]) : ((lane & 1) ? s2[1] : s2[0]));
              tg = ((u64)__float_as_uint(t1) << 32) | __float_as_uint(t0);
            }
            if (kt < 2 - EROW) {
#pragma unroll
              for (int i = 0; i < 2; ++i) glds16_p(x + (size_t)(srow + i) * D_ + (kt + EROW) * 512, lane * 16, shm_base + AUX + wid * 2048 + i * 1024);
            }
            if (kt == PUB_K) {
              if (wid == 0 && lane < 8) *(unsigned*)(shm + CNT + lane * 4) = 0u;
              const bool dg = ((lane >> 2) & 3) == (lane >> 4);
              float sm = half_sum(dg ? __uint_as_float((unsigned)tg) : 0.f);
              asm volatile("" : "+v"(sm));
              const float sq = half_sum(dg ? __uint_as_float((unsigned)(tg >> 32)) : 0.f);
              const u64 now = clock();
              if ((lane & 31) == 0) {
                const __amdgpu_buffer_rsrc_t rs = __builtin_amdgcn_make_buffer_rsrc((void*)(rstat + srow), 0, 32, 0x00020000);
                __builtin_amdgcn_raw_buffer_store_b128(u32x4{__float_as_uint(sm), (unsigned)now, __float_as_uint(sq), (unsigned)now ^ (unsigned)(now >> 32)}, rs,
                                                       lane >> 1, 0, 16);
              }
            }
          }
          if (kt >= PF_K0 && kt < PF_K0 + 5) {
#pragma unroll
            for (int i = 0; i < 2; ++i) {
              const int c = (bid * 4 + wid) * 10 + (kt - PF_K0) * 2 + i;
              if (c < 9216) {
                const u16* src = (c < 7168 ? wd + (size_t)c * 512 : wo + (size_t)(c - 7168) * 512) + lane * 8;
                GLDS_NC(src, shm_base + DMP + 7168);
              }
            }
          }
          if (kt >= CP_K0 && kt <= CP_K0 + 4) {
            if (kt < CP_K0 + 3) {
              const int q1 = bid * 4 + WID + (kt - CP_K0) * 1024, q0 = q1 - 1024;
              if (q0 >= 0 && q0 < 1152) {
                const __amdgpu_buffer_rsrc_t rc = __builtin_amdgcn_make_buffer_rsrc((void*)(wcopy + (q0 & 1) * 589824 + (q0 >> 1) * 1024), 0, 1024, 0x00020000);
                __builtin_amdgcn_raw_buffer_store_b128(*(const u32x4*)(shm + AUX + WID * 1024 + lane * 16), rc, lane * 16, 0, 16);
                WAIT_L(0);
              }
              if (q1 < 1152) {
                const char* src = (const char*)((q1 >> 1) < 448 ? wd + (size_t)(q1 >> 1) * 8 * D_ : wo + (size_t)((q1 >> 1) - 448) * 8 * D_);
                src += ((3 - ((unsigned)(uintptr_t)src >> 7)) & 7) * 128 + (q1 & 1) * 1024;
                GLDS(src + (lane >> 3) * 2048 + (lane & 7) * 16, shm_base + AUX + WID * 1024);
              }
            } else if (kt == CP_K0 + 4) {
              if (WID == 0) {
                const u64 now = clock();
                if (lane == 0) __hip_atomic_store(&donec[bid], now, RLX_AGENT);
              }
              if (WID == 2) GLDS_SC1((const char*)(done1 + (bid & ~1) * 64) + lane * 16, shm_base + DMP + 7168);
            }
          }
        } else if (kt == 17 - SD) {
          const u64 now = clock();
          if (lane == 0) __hip_atomic_store(&cstamp[(ntile * 4 + wid) * 8 + mtile], now, RLX_AGENT);
          if (WID >= 2) GLDS_SC1((const char*)donec + (WID - 2) * 1024 + lane * 16, shm_base + DMP + WID * 2048);
          GLDS_SC1(rstat + m0 + wid * 64 + (lane & 32) + 2 * (lane & 15) + ((lane >> 4) & 1), shm_base + AUX + wid * 1024);
          {
            const char* const wp = (const char*)wd + (size_t)(xcd < 4 ? 28 + 5 * (xcd & 1) : 5 - 5 * (xcd & 1)) * 131072 + (idx * 4 + wid) * 1024;
#pragma unroll
            for (int i = 0; i < NWPF; ++i) glds16_p(wp + (long)((i & 1) + ((i >> 1) & 1) * 14 + (i >> 2) * 7) * 131072, lane * 16, shm_base + DMP + wid * 2048 + 1024);
          }
        } else if (kt == 18 - SD) {
          const u32x4 c4 = *(const u32x4*)(shm + DMP + WID * 2048 + lane * 16);
          u32x4 gr = *(const u32x4*)(shm + AUX + wid * 1024 + lane * 16);
          asm volatile("" :: "v"(c4), "v"(gr) : "memory");
          cgather();
          const u64 win = tg - t_own;
          auto frs = [&](unsigned lo, unsigned hi) { return (((u64)hi << 32) | lo) - t_own - 100ull < win; };
          if (WID >= 2) *(unsigned*)(shm + CNT - 4 + WID * 4) = __all(frs(c4[0], c4[1]) & frs(c4[2], c4[3])) ? 1u : 0u;
          bool late = false;
          if (__builtin_expect(!__all(frs(gr[1], gr[1] ^ gr[3])), 0)) {
            const __amdgpu_buffer_rsrc_t rs = __builtin_amdgcn_make_buffer_rsrc((void*)(rstat + m0), 0, 4096, 0x00020000);
            const int prow = wid * 64 + (lane & 32) + 2 * (lane & 15) + ((lane >> 4) & 1);
            for (;;) {
              gr = __builtin_bit_cast(u32x4, __builtin_amdgcn_raw_buffer_load_b128(rs, prow * 16, 0, 16));
              if (__all(fresh(((u64)(gr[1] ^ gr[3]) << 32) | gr[1], t_own))) break;
              if (__builtin_amdgcn_s_memrealtime() - t_own > TIMEOUT_) { gr[1] = row_stats_serial(x + (size_t)(m0 + prow) * D_); late = true; break; }
              __builtin_amdgcn_s_sleep(2);
            }
          }
          float mean = __uint_as_float(gr[0]) * (1.0f / D_);
          asm volatile("" : "+v"(mean));
          const unsigned ps = pack_stats(mean, fmaxf(__builtin_fmaf(-mean, mean, __uint_as_float(gr[2]) * (1.0f / D_)), 0.f));
          *(unsigned*)(shm + AUX + wid * 1024 + lane * 16) = late ? gr[1] : ps;
        } else {
          const char* const sc = shm + (lane < 32 ? DMP + wid * 2048 + lane * 8 : QS + wid * 640 + (lane & 7) * 8);
          const int st = lane < 32 ? 256 : 64;
          const unsigned g0 = *(const unsigned*)(shm + AUX + wid * 1024 + lane * 16);
          f32x2 w[8];
          bool late = false;
          for (;;) {
            const u64 sv = *(const u64*)(shm + QS + wid * 640 + 512 + (lane & 15) * 8);
#pragma unroll
            for (int r = 0; r < 8; ++r) w[r] = *(const f32x2*)(sc + r * st);
            if (__builtin_expect(__all(sv - t_own - 100ull < tg - t_own - 200ull), 1)) break;
            late = __builtin_amdgcn_s_memrealtime() - t_own > TIMEOUT_;
            if (late) break;
            __builtin_amdgcn_s_sleep(2);
            WAIT_L(0);
            cgather();
            WAIT_V(0);
          }
          float t0 = w[0][0], t1 = w[0][1];
#pragma unroll
          for (int r = 1; r < 8; ++r)
            asm("v_add_f32 %0, %0, %2\n\tv_add_f32 %1, %1, %3" : "+v"(t0), "+v"(t1) : "v"(w[r][0]), "v"(w[r][1]));
          if (__builtin_expect(late, 0)) {
            const int gq = (lane >> 3) & 3;
            const char* pc = (lane < 32 ? pbase + ((psh & gq) ? dgu : 0) + (gq >> psh) * 32 : gb + dgu + wid * 32) + (lane & 7) * 4;
            t0 = 0.f; t1 = 0.f;
#pragma unroll 1
            for (int k = 0; k < D_; ++k) { const unsigned v = *(const unsigned*)(pc + (size_t)k * (lane < 32 ? pstr : F_ * 2)); t0 += lo_f(v); t1 += hi_f(v); }
          }
          if (lane < 40)
            *(f32x2*)((float*)(shm + CF) + (lane < 32 ? (lane >> 4) * 160 + wid * 32 + (lane & 15) * 2 : (wid >> 1) * 160 + 128 + (wid & 1) * 16 + (lane & 7) * 2)) = f32x2{t0, t1};
          const float nm = -lo_f(g0), dn = hi_f(g0), r = __builtin_amdgcn_rcpf(dn);
          float k1 = -2.3022081986f * r, d2 = dn * dn;
          asm volatile("" : "+v"(k1));
          float k2 = -0.10294324f * r;
          asm volatile("" : "+v"(d2), "+v"(k2));
          const f32x4 fc = {nm, k1, k2 * r * r, d2};
          *(f32x4*)(shm + AUX + wid * 1024 + lane * 16) = fc;
          float* const tw = (float*)(shm + DMP + WID * 2048) + lane;
#pragma unroll
          for (int i = 0; i < 4; ++i) tw[i * 64] = fc[i];
          ((float*)(shm + QS + WID * 640))[lane] = r;
        }
      }
      SB0();
    }
#pragma unroll
    for (int u = 0; u < 8; ++u) mm(u >> 2, 8 + (u >> 2), 1, u & 3);
  TS1(1);
  __builtin_amdgcn_sched_barrier(0);
  int tz = threadIdx.x;
  asm volatile("s_nop 7" : "+v"(tz));
  __builtin_amdgcn_sched_barrier(0);
  const int li = tz & 15, lq = (tz >> 4) & 3, ln = tz & 63;
  const __amdgpu_buffer_rsrc_t ra = __builtin_amdgcn_make_buffer_rsrc((void*)abuf, 0, 16 * 72 * 16384, 0x00020000);
  float* const svr = (float*)(shm + QS + wm * 640);
  float* const tb = (float*)(shm + DMP + wm * 2048);
  typedef __attribute__((address_space(3))) unsigned lds_u32;
  lds_u32* const cnt = (lds_u32*)(shm3 + CNT);
  unsigned vcnt = shm_base + CNT;
  asm volatile("" : "+v"(vcnt));
  auto signal = [&](unsigned v) {
    unsigned t;
    asm volatile("v_mov_b32 %0, %2\n\ts_mov_b64 exec, 1\n\tds_add_u32 %1, %0\n\ts_mov_b64 exec, -1" : "=&v"(t) : "v"(vcnt), "s"(v) : "memory");
  };
  auto await = [&](unsigned mask, unsigned val) {
    unsigned t, u;
    asm volatile("1:\n\tds_read_b32 %0, %2\n\ts_waitcnt lgkmcnt(0)\n\tv_readfirstlane_b32 %1, %0\n\ts_and_b32 %1, %1, %3\n\ts_cmp_eq_u32 %1, %4\n\t"
                 "s_cbranch_scc1 2f\n\ts_sleep 1\n\ts_branch 1b\n\t2:" : "=&v"(t), "=&s"(u) : "v"(vcnt), "s"(mask), "s"(val) : "memory", "scc");
  };
  if (grp) __builtin_amdgcn_s_setprio(1);
  signal(1u << (8 * grp));
  f32x4 rf[4];
#pragma unroll
  for (int mf = 0; mf < 4; ++mf) rf[mf] = *(const f32x4*)(shm + AUX + (wm * 64 + mf * 16 + li) * 16);
  const int sbase = ((mtile * 2 + (wm >> 1)) * 72 * 8 + (wm & 1) * 4) * 2048;
  auto putf = [&](int kg, int mf, u32x4 v) {
    __builtin_amdgcn_raw_buffer_store_b128(v, ra, ln * 16 + sbase + ((kg >> 1) * 8 + mf) * 2048 + (kg & 1) * 1024, 0, 16);
  };
  auto gelp = [&](f32x2 tg, f32x2 tu, f32x2 k12, f32x2 k22, f32x2 d2) {
    const f32x2 arg = tg * __builtin_elementwise_fma(tg * tg, k22, k12);
    const f32x2 dn = __builtin_elementwise_fma(f32x2{__builtin_amdgcn_exp2f(arg[0]), __builtin_amdgcn_exp2f(arg[1])}, d2, d2);
    const f32x2 r = (tg * tu) * f32x2{__builtin_amdgcn_rcpf(dn[0]), __builtin_amdgcn_rcpf(dn[1])};
    return pack2(r[0], r[1]);
  };
  auto gels = [&](const f32x4& g, const f32x4& u, const f32x4& k) {
    float t0, t1, t2, t3, n0, n1, n2, n3;
    asm("v_mul_f32 %0, %8, %8\n\tv_mul_f32 %1, %9, %9\n\tv_mul_f32 %2, %10, %10\n\tv_mul_f32 %3, %11, %11\n\t"
        "v_fma_f32 %0, %0, %17, %16\n\tv_fma_f32 %1, %1, %17, %16\n\tv_fma_f32 %2, %2, %17, %16\n\tv_fma_f32 %3, %3, %17, %16\n\t"
        "v_mul_f32 %0, %8, %0\n\tv_mul_f32 %1, %9, %1\n\tv_mul_f32 %2, %10, %2\n\tv_mul_f32 %3, %11, %3\n\t"
        "v_exp_f32 %0, %0\n\tv_mul_f32 %4, %8, %12\n\tv_exp_f32 %1, %1\n\tv_mul_f32 %5, %9, %13\n\t"
        "v_exp_f32 %2, %2\n\tv_mul_f32 %6, %10, %14\n\tv_exp_f32 %3, %3\n\tv_mul_f32 %7, %11, %15\n\t"
        "v_fma_f32 %0, %0, %18, %18\n\tv_fma_f32 %1, %1, %18, %18\n\tv_rcp_f32 %0, %0\n\tv_fma_f32 %2, %2, %18, %18\n\t"
        "v_rcp_f32 %1, %1\n\tv_fma_f32 %3, %3, %18, %18\n\tv_rcp_f32 %2, %2\n\tv_mul_f32 %0, %4, %0\n\t"
        "v_rcp_f32 %3, %3\n\tv_mul_f32 %1, %5, %1\n\tv_mul_f32 %2, %6, %2\n\tv_mul_f32 %3, %7, %3\n\t"
        "v_cvt_pk_bf16_f32 %0, %0, %1\n\tv_cvt_pk_bf16_f32 %1, %2, %3"
        : "=&v"(t0), "=&v"(t1), "=&v"(t2), "=&v"(t3), "=&v"(n0), "=&v"(n1), "=&v"(n2), "=&v"(n3)
        : "v"(g[0]), "v"(g[1]), "v"(g[2]), "v"(g[3]), "v"(u[0]), "v"(u[1]), "v"(u[2]), "v"(u[3]), "v"(k[1]), "v"(k[2]), "v"(k[3]));
    return u32x2{__float_as_uint(t0), __float_as_uint(t1)};
  };
  auto gel2 = [&](int p, int kg, int mf0 = 0, int mf1 = 4, int nsc = 0, bool opq = false) {
#pragma unroll
    for (int mf = mf0; mf < mf1; ++mf) {
      const f32x2 k12 = {rf[mf][1], rf[mf][1]}, k22 = {rf[mf][2], rf[mf][2]}, d2 = {rf[mf][3], rf[mf][3]};
      u32x4 o;
      if (opq) asm volatile("" : "+v"(acc[mf][p * 2]), "+v"(acc[mf][p * 2 + 1]));
      if (mf < nsc) {
        const u32x2 a = gels(acc[mf][p * 2], acc[mf][p * 2 + 2], rf[mf]), b = gels(acc[mf][p * 2 + 1], acc[mf][p * 2 + 3], rf[mf]);
        o = u32x4{a[0], a[1], b[0], b[1]};
      } else {
#pragma unroll
        for (int i = 0; i < 4; ++i) {
          const int j = i >> 1, e = (i & 1) * 2;
          const f32x4 g = acc[mf][p * 2 + j], u = acc[mf][p * 2 + 2 + j];
          o[i] = gelp(f32x2{g[e], g[e + 1]}, f32x2{u[e], u[e + 1]}, k12, k22, d2);
        }
      }
      putf(kg, mf, o);
      if (mf < nsc) SB0();
    }
  };
  auto client = [&](int l) { return (((l & 7) * 4 + (mtile & 3)) * 8) + (((mtile >> 2) * 2 + ((l >> 4) & 1)) ^ ((l & 8) ? 5 : 0)); };
  auto notify = [&](u64 lanes) {
    int l = threadIdx.x & 63;
    asm volatile("" : "+v"(l));
    const unsigned off = (client(l) * 64 + (l & 32) + ntile) * 8;
    u64 now, vn;
    asm volatile("s_memrealtime %0\n\ts_nop 0\n\ts_waitcnt lgkmcnt(0)\n\tv_mov_b64 %1, %0\n\ts_mov_b64 exec, %4\n\tglobal_store_dwordx2 %2, %1, %3 sc1\n\ts_mov_b64 exec, -1"
                 : "=&s"(now), "=&v"(vn) : "v"(off), "s"(done1), "s"(lanes) : "memory");
  };
  const u64 myh = 0xffffull << ((wm >> 1) * 16);
  auto arrive = [&](auto pc) {
    constexpr int part = decltype(pc)::value;
    unsigned t;
    asm volatile("v_mov_b32 %0, 1\n\ts_mov_b64 exec, 1\n\tds_add_rtn_u32 %0, %1, %0 offset:%2\n\ts_mov_b64 exec, -1\n\ts_waitcnt lgkmcnt(0)"
                 : "=&v"(t) : "v"(vcnt), "n"(12 + 4 * part) : "memory");
    if (__builtin_amdgcn_readfirstlane(t) == 3u) notify(myh << (32 * part));
  };
  const int role = 1 - (xcd >> 2);
  const int xb = role ? xcd : xcd ^ 5, par = xb & 1;
  const int mt2 = ((xb >> 1) * 4 + (idx & 3)) * 2 + par, nt2 = idx >> 2;
  auto rsrc = [](u64 b, unsigned n) { return u32x4{(unsigned)b, (unsigned)(b >> 32) & 0xffffu, n, 0x00020000u}; };
  u32x4 rw[2];
  int wsh[2];
  unsigned vb[2][2], cokw = 0;
  auto wsetup = [&](const u16* w, int rows, int crow0, int l) {
    const unsigned vr = (wid >> 1) * 32 + (wid & 1) * 8 + ((l >> 5) & 1) * 16 + ((l >> 3) & 3), vc = (((l >> 1) & 3) ^ ((l >> 4) & 1)) * 32 + (l & 1) * 16;
    u64 bs = (u64)(uintptr_t)(wcopy + (nt2 >> 2) * 589824 + crow0 * 128 - 1024), bf = (u64)(uintptr_t)(w + nt2 * 128) - 1024;
    asm volatile("" : "+s"(bs), "+s"(bf));
#pragma unroll
    for (int i = 0; i < 2; ++i) {
      const bool slow = ((((unsigned)bf >> 7) + i) & 7) == (cokw ? 3u : 8u);
      const int sh = slow ? 7 : 11;
      rw[i] = rsrc(slow ? bs : bf + i * 128, ((rows - 1) << sh) + 128 + 1024);
      wsh[i] = 6 + sh;
      vb[i][0] = (vr << sh) + vc + 1024;
      vb[i][1] = ((vr + 4) << sh) + (vc ^ 64);
    }
  };
#define G2_W "s_add_u32 m0, %[l], %[ow]\n\ts_lshl_b32 %[t], %[k], %[s0]\n\tbuffer_load_dwordx4 %[v0], %[w0], %[t] offen sc1 lds\n\t" \
             "buffer_load_dwordx4 %[v1], %[w0], %[t] offen offset:1024 sc1 lds\n\t" \
             "s_add_u32 m0, m0, 8192\n\ts_lshl_b32 %[t], %[k], %[s1]\n\tbuffer_load_dwordx4 %[v2], %[w1], %[t] offen sc1 lds\n\t" \
             "buffer_load_dwordx4 %[v3], %[w1], %[t] offen offset:1024 sc1 lds"
#define G2_WOP [ow] "s"(16384 + wid * 2048), [k] "s"(kt), [s0] "s"(wsh[0]), [s1] "s"(wsh[1]), [w0] "s"(rw[0]), [w1] "s"(rw[1]), \
               [v0] "v"(vb[0][0]), [v1] "v"(vb[0][1]), [v2] "v"(vb[1][0]), [v3] "v"(vb[1][1])
#define G2_A "s_add_u32 m0, %[l], %[oa]\n\ts_lshl_b32 %[t], %[k], 14\n\tbuffer_load_dwordx4 %[va], %[a], %[t] offen sc1 lds\n\t" \
             "buffer_load_dwordx4 %[va], %[a], %[t] offen offset:1024 sc1 lds\n\t"
#define G2_A2 "buffer_load_dwordx4 %[va], %[a], %[t] offen offset:2048 sc1 lds\n\tbuffer_load_dwordx4 %[va], %[a], %[t] offen offset:3072 sc1 lds\n\t"
  constexpr int PD = G2_NSTAGE - 1;
  constexpr int NY = 28;
  const int ny = (xcd & 1) ? NY - DLT : NY + DLT;
  const u32x4 ra2 = rsrc((u64)(uintptr_t)abuf + mt2 * (72 * 16384), 72 * 16384);
  unsigned va, tab, nlo, nhi, sl = shm_base + 4 * G2_STAGE;
  const int kfirst = 28 * role + 5 * par;
  int q = 3, kt = kfirst + 15;
  u64 okb = 0;
  auto adv = [&]() { ++q; kt = __builtin_amdgcn_readlane(tab, q); sl = sl == shm_base + (G2_NSTAGE - 1) * G2_STAGE ? shm_base : sl + G2_STAGE; };
  auto stage_y = [&]() {
    unsigned t;
    asm volatile(G2_A G2_A2 G2_W : [t] "=&s"(t) : [l] "s"(sl), [oa] "s"((wid ^ role) * 4096), [a] "s"(ra2), [va] "v"(va), G2_WOP : "memory", "scc", "m0");
    adv();
  };
  auto digest = [&](u64 v) {
    const u64 r = __ballot(fresh(v, t_own));
    const unsigned hi = (unsigned)(r >> 32), lo = (unsigned)r | hi;
    okb |= __ballot(((nlo & ~lo) | (nhi & ~hi)) == 0u);
  };
  constexpr int SNAP = 24576;
  auto snapshot = [&]() {
    asm volatile("s_mov_b32 m0, %2\n\ts_mov_b64 exec, 0xffffffff\n\tglobal_load_lds_dwordx4 %0, %1 sc1\n\ts_mov_b64 exec, -1"
                 :: "v"(ln * 16), "s"(done1 + bid * 64), "s"(shm_base + SNAP + wid * 2048) : "memory", "m0");
    SB0();
  };
  auto peek = [&]() { digest(*(const u64*)(shm + SNAP + wid * 2048 + ln * 8)); };
  auto wpair = [&](int g, int i) {
    unsigned t;
    asm volatile("s_mov_b32 m0, %1\n\ts_lshl_b32 %0, %2, %3\n\tbuffer_load_dwordx4 %5, %4, %0 offen sc1 lds\n\tbuffer_load_dwordx4 %6, %4, %0 offen offset:1024 sc1 lds"
                 : "=&s"(t) : "s"(shm_base + (g + 1) * G2_STAGE + 16384 + i * 8192 + wid * 2048), "s"(kfirst + (g == 2 ? 14 : g)), "s"(wsh[i]), "s"(rw[i]),
                   "v"(vb[i][0]), "v"(vb[i][1]) : "memory", "scc", "m0");
    SB0();
  };
  auto aquad = [&](int g) {
    unsigned t;
    asm volatile(G2_A G2_A2 : [t] "=&s"(t) : [l] "s"(shm_base + (g + 1) * G2_STAGE), [oa] "s"((wid ^ role) * 4096), [a] "s"(ra2), [va] "v"(va),
                 [k] "s"(((okb >> g) & 1) ? kfirst + (g == 2 ? 14 : g) : 100) : "memory", "scc", "m0");
    SB0();
  };
  auto mid = [&]() {
    cokw = __builtin_amdgcn_readfirstlane(cnt[1] & cnt[2]);
    wsetup(wd, F_, 0, ln);
    va = ln * 16 + wid * 4096;
    {
      const int ext = ln >= NY ? 1 : 0, qq = ln - ext * DLT, so = ln - ny;
      int j0 = 5 * par, j1 = 2 - 2 * par, j2 = 5 - 2 * par;
      asm volatile("" : "+s"(j1));
      asm volatile("" : "+s"(j2));
      const int j = (qq < 8 ? j0 : (qq < 20 ? j1 : j2)) + ((unsigned)(0x44449249244444ull >> (2 * qq)) & 3);
      const int xs = ((role ^ ext) << 2) | ((unsigned)(0xf5a0fd5a80f5a0ull >> (2 * qq)) & 3);
      unsigned bits = ((0x41a8282u >> (4 * j)) & 15u) << (4 * xs), ob = (so < 16 ? 1u : 0u) << (2 * so), ty = 7 * xs + j;
      asm volatile("" : "+v"(bits), "+v"(ob), "+v"(ty));
      tab = so < 0 ? ty : so;
      nlo = ln < 8 ? bits : 0u;
      nhi = so < 0 ? bits - nlo : ob;
    }
    asm volatile("" :: "s"(rw[0]), "s"(rw[1]), "s"(wsh[0]), "s"(wsh[1]), "s"(ra2));
    asm volatile("" : "+v"(vb[0][0]), "+v"(vb[0][1]), "+v"(vb[1][0]), "+v"(vb[1][1]), "+v"(va), "+v"(tab), "+v"(nlo), "+v"(nhi));
    await(0xffffu, 0x404u);
    wpair(0, 0);
    wpair(0, 1);
    WAIT_V(4);
    arrive(std::integral_constant<int, 0>{});
    __builtin_amdgcn_s_setprio(PRB);
  };
  int tsel = ntile & 1;
  asm volatile("" : "+s"(tsel));
  if (tsel != 0) {
    if (grp == 0) gel2(0, 2 * (kt0 + he) + wc, 0, NSC, NSC);
    else gel2(0, 2 * (kt0 + he) + wc, 0, NSC);
    gel2(0, 2 * (kt0 + he) + wc, NSC, 4);
    if (grp == 0) mid();
#pragma unroll
    for (int mf = 0; mf < 4; ++mf) {
      f32x4 t[4];
#pragma unroll
      for (int i = 1; i < 4; ++i) t[i] = *(const f32x4*)(tb + i * 64 + mf * 16 + lq * 4);
      const f32x4 g = acc[mf][4], u = acc[mf][5];
      u32x2 o;
#pragma unroll
      for (int e = 0; e < 4; e += 2)
        o[e >> 1] = gelp(f32x2{g[e], g[e + 1]}, f32x2{u[e], u[e + 1]}, f32x2{t[1][e], t[1][e + 1]}, f32x2{t[2][e], t[2][e + 1]}, f32x2{t[3][e], t[3][e + 1]});
      *(u32x2*)(shm + wm * 4096 + mf * 1024 + wc * 512 + li * 32 + lq * 8) = o;
      if (mf == 1 || grp == 0) SB0();
      if (grp == 0) {
        wpair(1 + (mf >> 1), mf & 1);
        if (mf == SNP) snapshot();
      }
    }
    signal(0x10000u << vid);
    TS1(5);
    if (grp) {
      gel2(3, 2 * (kt0 + 2 + he) + wc, 0, 4);
    } else {
#pragma unroll
      for (int mf = 0; mf < 4; ++mf) {
        gel2(3, 2 * (kt0 + 2 + he) + wc, mf, mf + 1);
        SB0();
        if (mf == 0) { WAIT_V(7 - 2 * SNP); peek(); }
        if (mf < 3) aquad(mf);
        if (mf == 2) snapshot();
      }
    }
    await(0x110000u << wm, 0x110000u << wm);
#pragma unroll
    for (int i = 0; i < 2; ++i) putf(2 * (kt0 + 4) + he, 2 * wc + i, __builtin_bit_cast(u32x4, tr_frag(shm3 + wm * 4096 + wc * 2048 + i * 1024 + ln * 8)));
    if (grp == 0) WAIT_V(9);
    TS1(6);
  } else {
    if (grp) __builtin_amdgcn_s_setprio(PRL0);
    float rd[4];
#pragma unroll
    for (int mf = 0; mf < 4; ++mf) rd[mf] = svr[mf * 16 + li];
    auto cvs = [&](const f32x4& a, const f32x4& b, const f32x4& r) {
      float t0, t1, t2, t3, t4, t5, t6, t7;
      asm("v_mul_f32 %0, %8, %16\n\tv_mul_f32 %4, %9, %17\n\tv_mul_f32 %1, %10, %18\n\tv_mul_f32 %5, %11, %19\n\t"
          "v_mul_f32 %2, %12, %16\n\tv_mul_f32 %6, %13, %17\n\tv_mul_f32 %3, %14, %18\n\tv_mul_f32 %7, %15, %19\n\t"
          "v_cvt_pk_bf16_f32 %0, %0, %4\n\tv_cvt_pk_bf16_f32 %1, %1, %5\n\tv_cvt_pk_bf16_f32 %2, %2, %6\n\tv_cvt_pk_bf16_f32 %3, %3, %7"
          : "=&v"(t0), "=&v"(t1), "=&v"(t2), "=&v"(t3), "=&v"(t4), "=&v"(t5), "=&v"(t6), "=&v"(t7)
          : "v"(a[0]), "v"(a[1]), "v"(a[2]), "v"(a[3]), "v"(b[0]), "v"(b[1]), "v"(b[2]), "v"(b[3]), "v"(r[0]), "v"(r[1]), "v"(r[2]), "v"(r[3]));
      return u32x4{__float_as_uint(t0), __float_as_uint(t1), __float_as_uint(t2), __float_as_uint(t3)};
    };
    auto cvp = [&](const f32x4& a, const f32x4& b) { return u32x4{pack2(a[0], a[1]), pack2(a[2], a[3]), pack2(b[0], b[1]), pack2(b[2], b[3])}; };
    auto cvk = [&](int mf, bool sc) {
      const f32x4 r4 = {rd[mf], rd[mf], rd[mf], rd[mf]};
      return sc ? cvs(acc[mf][2], acc[mf][3], r4) : cvp(acc[mf][2] * r4, acc[mf][3] * r4);
    };
    auto cvv = [&](int mf, bool sc) {
      const f32x4 r4 = *(const f32x4*)(svr + mf * 16 + lq * 4);
      return sc ? cvs(acc[mf][4], acc[mf][5], r4) : cvp(acc[mf][4] * r4, acc[mf][5] * r4);
    };
    u32x4 kx[4], vx[4], qx[4], hx[4];
    const bf16x8 ones = {0x3f80, 0x3f80, 0x3f80, 0x3f80, 0x3f80, 0x3f80, 0x3f80, 0x3f80};
    kx[2] = cvk(2, true); kx[3] = cvk(3, true);
    vx[2] = cvv(2, true); vx[3] = cvv(3, true);
    const bool giveh = !grp && wm == 3 && mtile < 7, needh = grp && wm == 0 && mtile > 0;
    char* const hptr = (char*)halo + (size_t)((needh ? mtile - 1 : mtile) * 32 + ntile + wc) * 16384;
    if (grp || wm < 3) {
      char* const ex = shm + (vid - wc) * 4096 + ln * 16;
      *(u32x4*)ex = kx[2]; *(u32x4*)(ex + 1024) = kx[3]; *(u32x4*)(ex + 2048) = vx[2]; *(u32x4*)(ex + 3072) = vx[3];
      signal(0x10000u << vid);
    } else if (giveh) {
      const __amdgpu_buffer_rsrc_t rh = __builtin_amdgcn_make_buffer_rsrc((void*)hptr, 0, 9216, 0x00020000);
      const u64 now = __builtin_amdgcn_s_memrealtime();
      const unsigned tag = (unsigned)now;
#pragma unroll
      for (int r = 0; r < 8; ++r) {
        const u32x4 w = r < 4 ? kx[2 + (r >> 1)] : vx[r >> 1];
        __builtin_amdgcn_raw_buffer_store_b128(u32x4{w[2 * (r & 1)], tag, w[2 * (r & 1) + 1], tag}, rh, ln * 16 + r * 1024, 0, 16);
      }
      __builtin_amdgcn_raw_buffer_store_b128(u32x4{tag, (unsigned)(now >> 32), tag, (unsigned)(now >> 32)}, rh, ln * 16 + 8192, 0, 16);
    }
    if (grp == 0) { gel2(3, 2 * (kt0 + 5 + he) + wc, 0, 4, NSC - 1, true); mid(); }
    kx[0] = cvk(0, false); kx[1] = cvk(1, false);
    vx[0] = cvv(0, false); vx[1] = cvv(1, false);
#pragma unroll
    for (int mf = 0; mf < 4; ++mf) qx[mf] = cvp(acc[mf][0], acc[mf][1]);
    f32x4 mk0, mk1;
#pragma unroll
    for (int j = 0; j < 4; ++j) {
      const int k0 = 4 * lq + j, k1 = 16 + 4 * lq + j;
      mk0[j] = (k0 >= li + 9 && k0 <= li + 16) ? 0.f : -1.0e30f;
      mk1[j] = (k1 >= li + 9 && k1 <= li + 16) ? 0.f : -1.0e30f;
    }
    auto max3 = [](float a, float b, float c) { float r; asm("v_max3_f32 %0, %1, %2, %3" : "=v"(r) : "v"(a), "v"(b), "v"(c)); return r; };
    auto max2 = [](float a, float b) { float r; asm("v_max_f32 %0, %1, %2" : "=v"(r) : "v"(a), "v"(b)); return r; };
    auto tiles = [&](int t0, const u32x4* kp, const u32x4* vp, bool first_rows) {
      const f32x4 z = {0.f, 0.f, 0.f, 0.f};
      const f32x4 mz = {-1.0e30f, -1.0e30f, -1.0e30f, -1.0e30f};
      f32x4 s0[2], s1[2];
#pragma unroll
      for (int n = 0; n < 2; ++n) {
        const bf16x8 qf = __builtin_bit_cast(bf16x8, qx[t0 + n]);
        s0[n] = __builtin_amdgcn_mfma_f32_16x16x32_bf16(__builtin_bit_cast(bf16x8, kp[n]), qf, first_rows ? mz : mk0, 0, 0, 0);
        s1[n] = __builtin_amdgcn_mfma_f32_16x16x32_bf16(__builtin_bit_cast(bf16x8, kx[t0 + n]), qf, mk1, 0, 0, 0);
      }
      SB0();
      if (grp == 0) { if (t0) wpair(1, 1); else aquad(1); }
      asm volatile("s_nop 7\n\ts_nop 3" : "+v"(s0[0]), "+v"(s1[0]), "+v"(s0[1]), "+v"(s1[1]));
      float mx[2];
#pragma unroll
      for (int n = 0; n < 2; ++n) mx[n] = max3(max3(s0[n][0], s0[n][1], s0[n][2]), max3(s0[n][3], s1[n][0], s1[n][1]), max2(s1[n][2], s1[n][3]));
#pragma unroll
      for (int n = 0; n < 2; ++n) { auto r = __builtin_amdgcn_permlane16_swap(__float_as_uint(mx[n]), __float_as_uint(mx[n]), false, false);
        mx[n] = max2(__uint_as_float(r[0]), __uint_as_float(r[1])); }
#pragma unroll
      for (int n = 0; n < 2; ++n) { auto r = __builtin_amdgcn_permlane32_swap(__float_as_uint(mx[n]), __float_as_uint(mx[n]), false, false);
        mx[n] = max2(__uint_as_float(r[0]), __uint_as_float(r[1])); }
      SB0();
      if (grp == 0) {
        if (t0) wpair(2, 0); else aquad(2);
        snapshot();
      }
      constexpr float cs2 = 0.17677669529663687f * 1.4426950408889634f;
      f32x4 os[2], o0[2], o1[2];
#pragma unroll
      for (int n = 0; n < 2; ++n) {
        const float c = cs2 * rd[t0 + n], nm = -mx[n] * c;
        float e8[8];
#pragma unroll
        for (int j = 0; j < 4; ++j) {
          e8[j] = __builtin_amdgcn_exp2f(__builtin_fmaf(s0[n][j], c, nm));
          e8[4 + j] = __builtin_amdgcn_exp2f(__builtin_fmaf(s1[n][j], c, nm));
        }
        u32x4 pk;
#pragma unroll
        for (int j = 0; j < 4; ++j) pk[j] = pack2(e8[2 * j], e8[2 * j + 1]);
        const bf16x8 pf = __builtin_bit_cast(bf16x8, pk);
        const u32x4 va = {vp[n][0], vp[n][1], vx[t0 + n][0], vx[t0 + n][1]}, vb = {vp[n][2], vp[n][3], vx[t0 + n][2], vx[t0 + n][3]};
        os[n] = __builtin_amdgcn_mfma_f32_16x16x32_bf16(ones, pf, z, 0, 0, 0);
        o0[n] = __builtin_amdgcn_mfma_f32_16x16x32_bf16(__builtin_bit_cast(bf16x8, va), pf, z, 0, 0, 0);
        o1[n] = __builtin_amdgcn_mfma_f32_16x16x32_bf16(__builtin_bit_cast(bf16x8, vb), pf, z, 0, 0, 0);
      }
      if (grp == 0 && t0) { SB0(); wpair(2, 1); }
#pragma unroll
      for (int n = 0; n < 2; ++n) {
        const float inv = __builtin_amdgcn_rcpf(os[n][0]);
        const f32x4 n0 = o0[n] * inv, n1 = o1[n] * inv;
        putf(112 + ntile + wc, t0 + n, u32x4{pack2(n0[0], n0[1]), pack2(n0[2], n0[3]), pack2(n1[0], n1[1]), pack2(n1[2], n1[3])});
      }
    };
    TS1(5);
    if (grp == 0) wpair(1, 0);
    tiles(2, &kx[0], &vx[0], false);
    constexpr int HL = 131072, HS = DMP + 1024;
    auto hload = [&]() {
#pragma unroll
      for (int r = 0; r < 9; ++r) GLDS_SC1(hptr + r * 1024 + ln * 16, shm_base + (r < 8 ? HL + wc * 8192 + r * 1024 : HS + wc * 2048));
    };
    if (needh) { await(0xffffu, 0x404u); hload(); }
    if (grp) { __builtin_amdgcn_s_setprio(PRL1); gel2(3, 2 * (kt0 + 5 + he) + wc, 0, 2, 0, true); __builtin_amdgcn_s_setprio(PRL0); }
    else {
      WAIT_V(4);
      peek();
      aquad(0);
    }
    TS1(6);
    if (!grp || wm > 0) {
      await(0x8000u << vid, 0x8000u << vid);
#pragma unroll
      for (int i = 0; i < 4; ++i) hx[i] = *(const u32x4*)(shm + (vid - wc - 1) * 4096 + i * 1024 + ln * 16);
    } else if (!needh) {
#pragma unroll
      for (int i = 0; i < 4; ++i) hx[i] = u32x4{0u, 0u, 0u, 0u};
    } else {
      bool late = false;
      WAIT_V(2);
      for (;;) {
        const char* const hl = shm + HL + wc * 8192 + ln * 16;
        const u32x4 sp = *(const u32x4*)(shm + HS + wc * 2048 + ln * 16);
        unsigned df = 0;
#pragma unroll
        for (int i = 0; i < 4; ++i) {
          const u32x4 g0 = *(const u32x4*)(hl + i * 2048), g1 = *(const u32x4*)(hl + i * 2048 + 1024);
          df |= (g0[1] ^ sp[0]) | (g0[3] ^ sp[0]) | (g1[1] ^ sp[0]) | (g1[3] ^ sp[0]);
          hx[i] = u32x4{g0[0], g0[2], g1[0], g1[2]};
        }
        if (__builtin_expect(__all((df == 0) & fresh(((u64)sp[1] << 32) | sp[0], t_own)), 1)) break;
        late = __builtin_amdgcn_s_memrealtime() - t_own > TIMEOUT_;
        if (late) break;
        __builtin_amdgcn_s_sleep(1);
        WAIT_L(0);
        hload();
        WAIT_V(0);
      }
      if (__builtin_expect(late, 0)) {
        char* const fb = shm + HL + wc * 8192 + ln * 16;
#pragma unroll 1
        for (int i = 0; i < 32; ++i) {
          const int rec = i >> 3, e = i & 7;
          const int row = m0 - 32 + 2 * (rec < 2 ? li : lq * 4 + (e & 3)) + (rec & 1);
          const int col = (rec < 2 ? D_ + lq * 4 + (e & 3) : 2 * D_ + li) + (ntile + wc) * 32 + (e >> 2) * 16;
          const u16* xr = x + (size_t)row * D_;
          const unsigned stt = row_stats_serial(xr);
          const float mb = lo_f(stt);
          float a = 0.f;
#pragma unroll 1
          for (int k = 0; k < D_; ++k) a += (bf2f(xr[k]) - mb) * bf2f(wqkv[(size_t)k * 3072 + col]);
          *(u16*)(fb + rec * 1024 + e * 2) = f2bf(a / hi_f(stt));
        }
#pragma unroll
        for (int i = 0; i < 4; ++i) hx[i] = *(const u32x4*)(fb + i * 1024);
      }
    }
    tiles(0, &hx[0], &hx[2], grp && mtile == 0 && wm == 0);
    if (grp) { __builtin_amdgcn_s_setprio(PRL1); gel2(3, 2 * (kt0 + 5 + he) + wc, 2, 4, 0, true); }
    else WAIT_V(7);
  }
  TS1(2);
  auto stamp = [&](u64* p) {
    WAIT_V(0);
    u64 now;
    asm volatile("s_memrealtime %0\n\ts_waitcnt lgkmcnt(0)" : "=s"(now) :: "memory");
    if ((threadIdx.x & 63) == 0) __hip_atomic_store(p, now, RLX_AGENT);
  };
  const bool cok = cokw != 0u;
  {
  __builtin_amdgcn_sched_barrier(0);
  int tid = threadIdx.x & 255;
  asm volatile("" : "+v"(tid));
  __builtin_amdgcn_sched_barrier(0);
  const int lane = tid & 63;
  const int wm = (wid >> 1) & 1, wn = wid & 1;
  const int mtile = mt2, ntile = nt2;
  const int m0 = mtile * 128, n0 = ntile * 128;
  TS(0);
  constexpr int EP = 288;
  char* const img = shm + ((ny + 13) % G2_NSTAGE) * G2_STAGE;
  if constexpr (!LD) {
    const int p = wid;
    const u64* const mbox = &done1[bid * 64 + lane];
    auto stage_s = [&]() {
      unsigned t;
      asm volatile(G2_A G2_W : [t] "=&s"(t) : [l] "s"(sl), [oa] "s"((p >> 1) * 8192 + (p & 1) * 2048), [a] "s"(ra2), [va] "v"(va), G2_WOP : "memory", "scc", "m0");
      adv();
    };
    u64 ren = 0;
    auto need = [&]() {
      while (!((okb >> q) & 1)) {
        digest(__hip_atomic_load(mbox, RLX_AGENT));
        if (!((okb >> q) & 1)) {
          __builtin_amdgcn_s_sleep(PSLEEP);
          if (__builtin_expect(!cok, 0)) notify(ren);
          if (GIVEUP()) break;
        }
      }
      asm volatile("" ::: "memory");
    };
    __builtin_amdgcn_s_setprio(0);
    if ((okb & 7) != 7) {
      const unsigned done = (unsigned)okb;
#pragma unroll 1
      for (q = 0; q < 3; ++q)
        if (!((done >> q) & 1)) {
          need();
          aquad(q);
        }
      if (done & 4) WAIT_V(0);
      WAIT_V(4);
    }
    TS(4);
    BARL();
    need();
    stage_y();
    WAIT_V(8);
    peek();
    need();
    stage_y();
    u64 fl = 0;
    auto check = [&]() {
      if (__builtin_expect(okb != ~0ull, 0)) {
        if (!(q & 1)) {
          asm volatile("" : "+v"(fl));
          digest(fl);
          if (okb != ~0ull) asm volatile("global_load_dwordx2 %0, %1, off sc1" : "+v"(fl) : "v"(mbox) : "memory");
        }
        need();
      }
    };
#pragma unroll 1
    while (q < ny) {
      WAIT_V(16);
      BAR();
      if (__builtin_expect(q == PD + 1, 0)) {
        ren = 0xffffffff00000000ull;
        if (p == 0) notify(ren);
      }
      check();
      stage_y();
    }
    va = lane * 16 + (p >> 1) * 8192 + role * 4096 + (p & 1) * 2048 + (56 << 14);
    wsetup(wo, D_, F_, lane);
    const u64* const pf = done2 + (bid ^ 5) * 8 + (lane & 3);
    u64 f2 = 0;
#pragma unroll 1
    for (int g = 0; g < 16; ++g) {
      WAIT_V(12);
      BAR();
      check();
      if (__builtin_expect(g == 14, 0)) asm volatile("global_load_dwordx2 %0, %1, off sc1" : "+v"(f2) : "v"(pf) : "memory");
      stage_s();
    }
    WAIT_V(12);
    BAR();
    {
      asm volatile("" : "+v"(f2), "+v"(fl));
      while (!__all(fresh(f2, t_own))) {
        if (__builtin_expect(!cok, 0)) { notify(ren); if (lane < 4) __hip_atomic_store(done2 + bid * 8 + lane, __builtin_amdgcn_s_memrealtime(), RLX_AGENT); }
        __builtin_amdgcn_s_sleep(1);
        f2 = __hip_atomic_load(pf, RLX_AGENT);
        if (GIVEUP()) break;
      }
      asm volatile("" ::: "memory");
      const char* ps = (const char*)slab + (size_t)(bid ^ 5) * 16384 + p * 4096 + lane * 16;
#pragma unroll
      for (int i = 0; i < 4; ++i) GLDS_SC1(ps + i * 1024, sl + p * 4096 + i * 1024);
    }
    WAIT_V(10);
    BAR();
    WAIT_V(4);
    BAR();
    WAIT_V(0);
    BAR();
    u32x4 xv[4];
#pragma unroll
    for (int i = 0; i < 4; ++i) {
      const int c = i * 256 + tid, r = c >> 4;
      xv[i] = *(const u32x4*)(x + (size_t)(m0 + (r >> 5) * 64 + role * 32 + (r & 31)) * D_ + n0 + (c & 15) * 8);
    }
    BAR();
#pragma unroll
    for (int i = 0; i < 4; ++i) {
      const int c = i * 256 + tid, r = c >> 4;
      const u32x4 tv = *(const u32x4*)(img + ((r & 32) + (r & 1) * 16 + ((r >> 1) & 15)) * EP + (c & 15) * 16);
      u32x4 ov;
#pragma unroll
      for (int e = 0; e < 4; ++e) ov[e] = pack2(lo_f(xv[i][e]) + lo_f(tv[e]), hi_f(xv[i][e]) + hi_f(tv[e]));
      __builtin_nontemporal_store(ov, (u32x4*)(out + (size_t)(m0 + (r >> 5) * 64 + role * 32 + (r & 31)) * D_ + n0 + (c & 15) * 8));
    }
    TS(5);
    if (tid == 0) __hip_atomic_store(&acks[bid], __builtin_amdgcn_s_memrealtime(), RLX_AGENT);
    if (__builtin_expect(!cok, 0) && p == 0) {
      const bool mine = lane <= 32;
      const u64* const pa = &acks[lane < 32 ? client(lane) : bid ^ 5];
      while (!__all(!mine || fresh(__hip_atomic_load(pa, RLX_AGENT), t_own))) {
        notify(ren);
        if (lane < 4) __hip_atomic_store(done2 + bid * 8 + lane, __builtin_amdgcn_s_memrealtime(), RLX_AGENT);
        __builtin_amdgcn_s_sleep(32);
        if (GIVEUP()) break;
      }
    }
  } else {
    TS(6);
    f32x4 acc[4][4], sacc[2][4];
    const int fr = lane & 15, fq = lane >> 4, q4 = fr >> 2, pp = lane & 3;
    const int ff = ((q4 >> 1) & 1) | ((fq & 1) << 1);
    int aof = G2_STAGE + wm * 8192 + lane * 16;
    int boff[4];
#pragma unroll
    for (int j = 0; j < 4; ++j) boff[j] = G2_STAGE + 16384 + wn * 8192 + (8 * fq + q4) * 128 + ((j ^ ff) * 32) + pp * 8;
#pragma unroll
    for (int b = 0; b < 4; ++b) {
#pragma unroll
      for (int a = 0; a < 4; ++a) acc[a][b] = f32x4{0.f, 0.f, 0.f, 0.f};
      sacc[0][b] = f32x4{0.f, 0.f, 0.f, 0.f}; sacc[1][b] = f32x4{0.f, 0.f, 0.f, 0.f};
    }
#define SB() __builtin_amdgcn_sched_barrier(0)
    struct Fr { bf16x8 a[4]; bf16x4 bl[4], bh[4]; };
    Fr f0[2], f1[2];
    auto rd = [&](Fr& f, lds_cptr sb, int ks, int t, bool sp) {
      constexpr int ord[12] = {0, 1, 8, 2, 3, 4, 5, 6, 7, 9, 10, 11};
      const int o = ord[t];
      if (o >= 8) f.a[o - 8] = *(const __attribute__((address_space(3))) bf16x8*)(sb + aof + (o - 8) * 2048 + ks * 1024);
      else if (o & 1) f.bh[o >> 1] = tr_read(sb + boff[o >> 1] + ks * 4096 + 512);
      else f.bl[o >> 1] = tr_read(sb + boff[o >> 1] + ks * 4096);
    };
    auto bfr = [&](const Fr& c, int j) { return __builtin_shufflevector(c.bl[j], c.bh[j], 0, 1, 2, 3, 4, 5, 6, 7); };
    auto ystg = [&](const Fr* c, Fr* n, int kind) {
      lds_cptr sb = shm3;
#pragma unroll
      for (int t = 0; t < 16; ++t) {
        const int mf = (t >> 2) ^ kind, j = (t & 4) ? 3 - (t & 3) : t & 3, ks = t & 1;
        SB();
        asm volatile("v_mfma_f32_16x16x32_bf16 %0, %1, %2, %0\n\tv_mfma_f32_16x16x32_bf16 %0, %3, %4, %0"
                     : "+v"(acc[mf][j]) : "v"(bfr(c[ks], j)), "v"(c[ks].a[mf]), "v"(bfr(c[ks ^ 1], j)), "v"(c[ks ^ 1].a[mf]));
        SB();
        const int h = kind == 2 ? 5 : 6;
        if (t < 2 * h) { rd(n[t / h], sb, t / h, 2 * (t % h), true); rd(n[t / h], sb, t / h, 2 * (t % h) + 1, true); }
      }
      SB();
      __builtin_amdgcn_s_waitcnt(0xC07F);
    };
    auto rds = [&](Fr* n, lds_cptr sb, int t) {
      if (t < 10) { rd(n[t / 5], sb, t / 5, 2 * (t % 5), true); rd(n[t / 5], sb, t / 5, 2 * (t % 5) + 1, true); }
    };
    auto stg = [&](const Fr* c, Fr* n, bool load = true) {
      lds_cptr sb = shm3;
#pragma unroll
      for (int t = 0; t < 16; ++t) {
        const int ks = t >> 3, mf = (t >> 2) & 1, j = (t & 4) ? 3 - (t & 3) : t & 3;
        SB();
        sacc[mf][j] = __builtin_amdgcn_mfma_f32_16x16x32_bf16(bfr(c[ks], j), c[ks].a[mf], sacc[mf][j], 0, 0, 0);
        SB();
        if (load) rds(n, sb, t);
      }
      SB();
    };
    asm volatile("" : "+v"(aof), "+v"(boff[0]), "+v"(boff[1]), "+v"(boff[2]), "+v"(boff[3]));
    asm volatile("" : "+v"(sacc[0][0]), "+v"(sacc[0][1]), "+v"(sacc[0][2]), "+v"(sacc[0][3]), "+v"(sacc[1][0]), "+v"(sacc[1][1]), "+v"(sacc[1][2]), "+v"(sacc[1][3]));
#pragma unroll
    for (int a = 0; a < 4; ++a) asm volatile("" : "+v"(acc[a][0]), "+v"(acc[a][1]), "+v"(acc[a][2]), "+v"(acc[a][3]));
    BARL();
    TS(7);
    __builtin_amdgcn_s_waitcnt(0xC07F);
    SB();
#pragma unroll
    for (int t = 0; t < 24; ++t) rd(f0[t & 1], shm3, t & 1, t >> 1, false);
    int so = G2_STAGE;
    auto nxt = [&]() {
      const int d = so == (G2_NSTAGE - 1) * G2_STAGE ? -(G2_NSTAGE - 1) * G2_STAGE : G2_STAGE;
      so += d; aof += d; boff[0] += d; boff[1] += d; boff[2] += d; boff[3] += d;
      asm volatile("" : "+v"(aof), "+v"(boff[0]), "+v"(boff[1]), "+v"(boff[2]), "+v"(boff[3]));
    };
    nxt();
    __builtin_amdgcn_s_waitcnt(0xC07F);
#pragma unroll 1
    for (int g = 0; g < ny - 2; g += 2) {
      ystg(f0, f1, 0);
      WAIT_V(0);
      nxt();
      BAR();
      ystg(f1, f0, 0);
      nxt();
      BAR();
    }
    if (!(DLT & 1)) { ystg(f0, f1, 0); nxt(); BAR(); }
    Fr sa[2], sb2[2];
    ystg((DLT & 1) ? f0 : f1, sa, 2);
    asm volatile("" : "+v"(acc[0][0]), "+v"(acc[0][1]), "+v"(acc[0][2]), "+v"(acc[0][3]), "+v"(acc[1][0]), "+v"(acc[1][1]), "+v"(acc[1][2]), "+v"(acc[1][3]));
    TS(2);
    {
      const __amdgpu_buffer_rsrc_t rs = __builtin_amdgcn_make_buffer_rsrc((void*)((char*)slab + (size_t)bid * 16384), 0, 16384, 0x00020000);
#pragma unroll
      for (int f = 0; f < 4; ++f) {
        const f32x4 a = acc[2 + (f >> 1)][(f & 1) * 2], b = acc[2 + (f >> 1)][(f & 1) * 2 + 1];
        __builtin_amdgcn_raw_buffer_store_b128(u32x4{pack2(a[0], a[1]), pack2(a[2], a[3]), pack2(b[0], b[1]), pack2(b[2], b[3])}, rs,
                                               (f * 256 + tid) * 16, 0, 16);
      }
    }
    SB();
#pragma unroll 1
    for (int i = 0; i < 7; ++i) {
      nxt();
      BAR();
      stg(sa, sb2);
      nxt();
      BAR();
      stg(sb2, sa);
      if (i == 2) {
        stamp(done2 + bid * 8 + (wid & 3));
      }
    }
    nxt();
    BAR();
    stg(sa, sb2);
    BAR();
    u32x4 pv[4];
    {
      nxt();
      lds_cptr sb = shm3 + so + tid * 16;
#pragma unroll
      for (int f = 0; f < 4; ++f) pv[f] = *(const __attribute__((address_space(3))) u32x4*)(sb + f * 4096);
    }
    stg(sb2, sa, false);
    TS(1);
#pragma unroll
    for (int mf = 0; mf < 2; ++mf)
#pragma unroll
      for (int j = 0; j < 4; ++j) {
        const u32x4 pw = pv[mf * 2 + (j >> 1)];
        const f32x4 sv = sacc[mf][j];
        const f32x4 ya = acc[mf][j] + f32x4{lo_f(pw[2 * (j & 1)]), hi_f(pw[2 * (j & 1)]), lo_f(pw[2 * (j & 1) + 1]), hi_f(pw[2 * (j & 1) + 1])};
        u32x2 o;
#pragma unroll
        for (int e = 0; e < 2; ++e) {
          const f32x2 y2 = {ya[2 * e], ya[2 * e + 1]}, s2 = {sv[2 * e], sv[2 * e + 1]};
          const f32x2 ar = y2 * f32x2{-1.4426950408889634f, -1.4426950408889634f};
          const f32x2 dn = f32x2{__builtin_amdgcn_exp2f(ar[0]), __builtin_amdgcn_exp2f(ar[1])} + f32x2{1.0f, 1.0f};
          const f32x2 t = (s2 * y2) * f32x2{__builtin_amdgcn_rcpf(dn[0]), __builtin_amdgcn_rcpf(dn[1])};
          o[e] = pack2(t[0], t[1]);
        }
        *(u32x2*)(img + (wm * 32 + mf * 16 + fr) * EP + (wn * 64 + j * 16 + fq * 4) * 2) = o;
      }
    BARL();
    TS(3);
  }
  TSFLUSH();
  }
  };
  if (wid < 4) kloop(std::integral_constant<int, 0>{});
  else kloop(std::integral_constant<int, 1>{});
  asm volatile(".Lcode_end:");
}

extern "C" void kernel_launch(void* const* d_in, const int* in_sizes, int n_in, void* d_out, int out_size, void* d_ws,
                              size_t ws_size, hipStream_t stream) {
  const u16* A_x = (const u16*)d_in[0];
  const u16* A_wqkv = (const u16*)d_in[1];
  const u16* A_wo = (const u16*)d_in[2];
  const u16* B_wg = (const u16*)d_in[4];
  const u16* B_wu = (const u16*)d_in[5];
  const u16* B_wd = (const u16*)d_in[6];
  u16* out = (u16*)d_out;
  fused_kernel<<<256, 512, 0, stream>>>(A_x, A_wqkv, B_wg, B_wu, B_wd, A_wo, out, (char*)d_ws);
}
```

```cpp
#include <hip/hip_runtime.h>
#include <hip/hip_bf16.h>
#include <stdint.h>
#include <type_traits>
#include <utility>

typedef unsigned short u16;
using bf16x8 = __attribute__((ext_vector_type(8))) short;
using bf16x4 = __attribute__((ext_vector_type(4))) short;
using f32x4 = __attribute__((ext_vector_type(4))) float;
using u32x2 = __attribute__((ext_vector_type(2))) unsigned;
using u32x4 = __attribute__((ext_vector_type(4))) unsigned;
typedef __attribute__((address_space(3))) const char* lds_cptr;

template <class F, int... I> __device__ __forceinline__ void sfor_(F&& f, std::integer_sequence<int, I...>) { (f(std::integral_constant<int, I>{}), ...); }
template <int N, class F> __device__ __forceinline__ void sfor(F&& f) { sfor_(f, std::make_integer_sequence<int, N>{}); }

constexpr int S_ = 2048, D_ = 1024, F_ = 3584;

__device__ __forceinline__ float bf2f(u16 h) { return __uint_as_float((unsigned)h << 16); }
__device__ __forceinline__ u16 f2bf(float f) {
  unsigned u = __float_as_uint(f);
  u += 0x7fffu + ((u >> 16) & 1u);
  return (u16)(u >> 16);
}
__device__ __forceinline__ float rbf(float f) { return bf2f(f2bf(f)); }
typedef __attribute__((ext_vector_type(2))) __bf16 bf16x2_t;
typedef __attribute__((ext_vector_type(2))) float f32x2;
__device__ __forceinline__ unsigned pack2(float a, float b) {
  bf16x2_t r = __builtin_convertvector((f32x2){a, b}, bf16x2_t);
  return __builtin_bit_cast(unsigned, r);
}
__device__ __forceinline__ float lo_f(unsigned p) { return __uint_as_float(p << 16); }
__device__ __forceinline__ float hi_f(unsigned p) { return __uint_as_float(p & 0xffff0000u); }

#define TSDECL
#define TSS(i) do {} while (0)
#define TSFLUSH() do {} while (0)
#define TS1(i) TSS(i)
#define TS(i) TSS(8 + (i))
#define WAIT_V(n) asm volatile("s_waitcnt vmcnt(%0)" ::"n"(n) : "memory")
#define WAIT_L(n) asm volatile("s_waitcnt lgkmcnt(%0)" ::"n"(n) : "memory")
#define BARL() do { WAIT_L(0); BAR(); } while (0)
#define BAR() do { asm volatile("" ::: "memory"); __builtin_amdgcn_s_barrier(); asm volatile("" ::: "memory"); } while (0)

__device__ __forceinline__ bf16x4 tr_read(lds_cptr p) {
  return __builtin_amdgcn_ds_read_tr16_b64_v4i16((__attribute__((address_space(3))) bf16x4*)p);
}
__device__ __forceinline__ bf16x8 tr_frag(lds_cptr p) {
  bf16x4 lo = tr_read(p), hi = tr_read(p + 512);
  bf16x8 r;
  r[0] = lo[0]; r[1] = lo[1]; r[2] = lo[2]; r[3] = lo[3];
  r[4] = hi[0]; r[5] = hi[1]; r[6] = hi[2]; r[7] = hi[3];
  return r;
}
__device__ __forceinline__ void glds16(const void* gsrc, unsigned lds_dst) {
  unsigned keep;
  asm volatile("s_mov_b32 %0, m0\n\ts_mov_b32 m0, %2\n\ts_nop 0\n\tglobal_load_lds_dwordx4 %1, off\n\ts_mov_b32 m0, %0"
               : "=&s"(keep) : "v"(gsrc), "s"(lds_dst) : "memory");
}
#define GLDS(gptr, laddr) glds16((const void*)(gptr), (unsigned)(laddr))
__device__ __forceinline__ void glds16_sc1(const void* gsrc, unsigned lds_dst) {
  unsigned keep;
  asm volatile("s_mov_b32 %0, m0\n\ts_mov_b32 m0, %2\n\ts_nop 0\n\tglobal_load_lds_dwordx4 %1, off sc1\n\ts_mov_b32 m0, %0"
               : "=&s"(keep) : "v"(gsrc), "s"(lds_dst) : "memory");
}
#define GLDS_SC1(gptr, laddr) glds16_sc1((const void*)(gptr), (unsigned)(laddr))
__device__ __forceinline__ void glds16_nc(const void* gsrc, unsigned lds_dst) {
  unsigned keep;
  asm volatile("s_mov_b32 %0, m0\n\ts_mov_b32 m0, %2\n\ts_nop 0\n\tglobal_load_lds_dwordx4 %1, off\n\ts_mov_b32 m0, %0"
               : "=&s"(keep) : "v"(gsrc), "s"(lds_dst));
}
#define GLDS_NC(gptr, laddr) glds16_nc((const void*)(gptr), (unsigned)(laddr))
__device__ __forceinline__ void glds16_s(const void* sbase, unsigned voff, unsigned lds_dst) {
  asm volatile("s_mov_b32 m0, %2\n\ts_nop 0\n\tglobal_load_lds_dwordx4 %0, %1" :: "v"(voff), "s"(sbase), "s"(lds_dst) : "m0");
}

__device__ __forceinline__ void glds16_p(const void* sbase, unsigned voff, unsigned lds_dst) {
  asm volatile("s_mov_b32 m0, %2\n\ts_nop 3\n\tglobal_load_lds_dwordx4 %0, %1" :: "v"(voff), "s"(sbase), "s"(lds_dst) : "m0");
}

typedef unsigned long long u64;
__device__ __forceinline__ bool fresh(u64 stamp, u64 t_own) {
  return stamp - t_own - 100ull < __builtin_amdgcn_s_memrealtime() - t_own;
}
constexpr u64 TIMEOUT_ = 200000;
#define GIVEUP() (__builtin_amdgcn_s_memrealtime() - t_own > 100000000ull)
__device__ __forceinline__ float half_sum(float v) {
  v += __builtin_bit_cast(float, __builtin_amdgcn_update_dpp(0, __builtin_bit_cast(int, v), 0xB1, 0xf, 0xf, false));
  v += __builtin_bit_cast(float, __builtin_amdgcn_update_dpp(0, __builtin_bit_cast(int, v), 0x4E, 0xf, 0xf, false));
  v += __builtin_bit_cast(float, __builtin_amdgcn_update_dpp(0, __builtin_bit_cast(int, v), 0x141, 0xf, 0xf, false));
  v += __builtin_bit_cast(float, __builtin_amdgcn_update_dpp(0, __builtin_bit_cast(int, v), 0x140, 0xf, 0xf, false));
  { auto r = __builtin_amdgcn_permlane16_swap(__float_as_uint(v), __float_as_uint(v), false, false); v = __uint_as_float(r[0]) + __uint_as_float(r[1]); }
  return v;
}
__device__ __forceinline__ unsigned pack_stats(float mean, float var) {
  return (unsigned)f2bf(mean) | ((unsigned)f2bf(sqrtf(rbf(rbf(var) + 1e-5f))) << 16);
}
__device__ __forceinline__ unsigned row_stats_serial(const u16* xr) {
  float s = 0.f, q = 0.f;
#pragma unroll 1
  for (int k = 0; k < D_; ++k) s += bf2f(xr[k]);
  const float mean = s * (1.0f / D_);
#pragma unroll 1
  for (int k = 0; k < D_; ++k) { const float d = bf2f(xr[k]) - mean; q += d * d; }
  return pack_stats(mean, q * (1.0f / D_));
}
constexpr int G1_STAGE = 32768 + 5 * 8192;
#define PF_K0 6
#define CP_K0 5
#define DLT 1
#define PSLEEP 2
#define SD 6
#define NSC 2
#define PRB 0
#define PRL0 3
#define PRL1 1
#define NWPF 4
#define SNP 3
#define RD0 21
#define NP1 8
#define ESH 1
#define EROW 0
#define KS0 2
#define PUB_K 4

#define RLX_AGENT __ATOMIC_RELAXED, __HIP_MEMORY_SCOPE_AGENT

constexpr int G2_STAGE = 16384 + 2 * 8192;
constexpr int G2_NSTAGE = 5;
constexpr size_t MB_ = 1024 * 1024;
__global__ __launch_bounds__(512, 2) __attribute__((amdgpu_num_vgpr(127))) void fused_kernel(const u16* __restrict__ x, const u16* __restrict__ wqkv,
                                                       const u16* __restrict__ wg, const u16* __restrict__ wu,
                                                       const u16* __restrict__ wd, const u16* __restrict__ wo,
                                                       u16* __restrict__ out, char* __restrict__ ws) {
  u16* const halo = (u16*)(ws + 4 * MB_);
  u16* const abuf = (u16*)(ws + 16 * MB_);
  char* const slab = ws + 64 * MB_;
  unsigned* const flags = (unsigned*)(ws + 96 * MB_);
  u32x4* const rstat = (u32x4*)(ws + 97 * MB_);
  char* const wcopy = ws + 98 * MB_;
  u64* const done1 = (u64*)(ws + 102 * MB_ + 512);
  u64* const done2 = (u64*)(ws + 102 * MB_);
  u64* const donec = (u64*)(ws + 100 * MB_ + 196608);
  u64* const acks = (u64*)(ws + 100 * MB_ + 262144);
  u64* const cstamp = (u64*)(ws + 100 * MB_ + 327680);
  float* const csum = (float*)(ws + 101 * MB_);
  __shared__ __attribute__((aligned(1024))) char shm[163840];
  constexpr int AUX = 2 * G1_STAGE + 8192;
  {
    u64 pc; int len;
    asm volatile("s_getpc_b64 %0\n.Lcode_beg:\n\ts_mov_b32 %1, .Lcode_end-.Lcode_beg" : "=s"(pc), "=s"(len));
    const int ol = ((blockIdx.x >> 3) + 32 * (threadIdx.x >> 6)) * 1024 + (threadIdx.x & 63) * 16;
    asm volatile("s_mov_b32 m0, %2\n\ts_nop 0\n\tglobal_load_lds_dwordx4 %0, %1"
                 :: "v"(ol < len - 16 ? ol : 0), "s"(pc & ~15ull), "s"((unsigned)(uintptr_t)(lds_cptr)shm + 2 * G1_STAGE + 7168) : "m0");
    __builtin_amdgcn_sched_barrier(0);
  }
  const u64 t_own = __builtin_amdgcn_s_memrealtime();
  __builtin_amdgcn_sched_barrier(0);
  TSDECL;
  const int tid = threadIdx.x, lane = tid & 63;
  const int wid = __builtin_amdgcn_readfirstlane(tid >> 6);
  const int bid = blockIdx.x, xcd = bid & 7, idx = bid >> 3;
  const int ntile = xcd * 4 + (idx & 3), mtile = idx >> 2;
  const int m0 = mtile * 256;
  const int hg = ntile >> 1;
  const bool tya = (ntile & 1) == 0;
  const int grp = wid >> 2, wc = (wid >> 1) & 1, wm = (((grp ^ ntile ^ 1) & 1) << 1) | (wid & 1), vid = wc * 4 + wm;
  const int srow = m0 + ntile * 8 + (wid & 3) * 2;
  lds_cptr shm3 = (lds_cptr)shm;
  const unsigned shm_base = (unsigned)(uintptr_t)shm3;
  const int r8 = tid >> 3, pos = tid & 7;
  const int phys = pos >> 1, half = pos & 1;
  const int fk = ((r8 >> 1) & 1) | (((r8 >> 3) & 1) << 1);
  const int vsrc = phys ^ fk;
  const int qstr = tya ? 3072 * 2 : F_ * 2;
  unsigned voa = (((r8 & 32) + 2 * (r8 & 15) + ((r8 >> 4) & 1)) * D_ + ((pos ^ (r8 & 7)) * 8)) * 2;
  unsigned vog = r8 * (F_ * 2) + vsrc * 32 + half * 16, voq = r8 * qstr + vsrc * 32 + half * 16;
  unsigned vom = r8 * qstr + (vsrc >> (tya ? 0 : 1)) * 32 + half * 16;
  asm volatile("" : "+v"(voa), "+v"(vog), "+v"(voq), "+v"(vom));
  asm volatile("" :: "s"(x), "s"(wqkv), "s"(wg), "s"(wu), "s"(wd), "s"(wo), "s"(out), "s"(ws));
  const char* const ab = (const char*)(x + (size_t)m0 * D_);
  int kz = 0;
  asm volatile("" : "+s"(kz));
  auto stage_a = [&](int buf, int kt, int i, int h) {
    glds16_s(ab + (long)(((i * 64 + (h - 1) * 32) * D_ + kt * 64) * 2), voa, shm_base + buf * G1_STAGE + (wid - 4 + 4 * h) * 1024 + i * 8192);
  };
#pragma unroll
  for (int i = 0; i < 4; ++i) stage_a(0, 0, i, 1);
  const int kt0 = (hg >> 1) * 7, he = hg & 1;
  const char* const gb = (const char*)(wg + (kt0 + (tya ? 5 + he : 2 + he)) * 64);
  const char* const qb = tya ? (const char*)(wqkv + 64 * hg) : gb - 256;
  const long dgu = (const char*)wu - (const char*)wg, dp1 = tya ? 2048 : dgu;
  const char* const mb0 = tya ? qb + 4096 : (const char*)(wg + (kt0 + 4) * 64 + 32 * he);
  const char* mptr = mb0 + (((vsrc & 1) && !tya) ? dgu : 0) + vom;
  auto wload = [&](int p, int rows, unsigned dst) {
    long off = (long)(rows * (p < 3 ? qstr : F_ * 2)) + (p == 1 ? dp1 : (p == 4 ? dgu : 0));
    if (p == 2) { asm volatile("" : "+s"(off)); GLDS_NC(mptr + off, dst); }
    else glds16_s((p < 2 ? qb : gb) + off, p < 2 ? voq : vog, dst);
  };
#pragma unroll
  for (int i = 0; i < 5; ++i) wload(i, 0, shm_base + wid * 1024 + 32768 + i * 8192);
  __builtin_amdgcn_sched_barrier(0);
  const int fr = lane & 15, fq = lane >> 4, q4 = fr >> 2, pp = lane & 3;
  const int ff = ((q4 >> 1) & 1) | ((fq & 1) << 1);
  int aoff[2], boff[2];
#pragma unroll
  for (int ks = 0; ks < 2; ++ks) aoff[ks] = (wm * 64 + fr) * 128 + (((ks * 4 + fq) ^ (fr & 7)) * 16);
#pragma unroll
  for (int j = 0; j < 2; ++j) boff[j] = 32768 + (8 * fq + q4) * 128 + (((2 * wc + j) ^ ff) * 32) + pp * 8;

  f32x4 acc[4][10];
#pragma unroll
  for (int a = 0; a < 4; ++a)
#pragma unroll
    for (int b = 0; b < 10; ++b) acc[a][b] = f32x4{0.f, 0.f, 0.f, 0.f};

  auto stage_part = [&](int buf, int kt, int i, int h) {
    if (i < 5) wload(i, kt * 64 + (h - 1) * 32, shm_base + buf * G1_STAGE + (wid - 4 + 4 * h) * 1024 + 32768 + i * 8192);
    else stage_a(buf, kt, i - 5, h);
  };
  constexpr int NT = D_ / 64;
  constexpr int DMP = 2 * G1_STAGE;
  constexpr int CF = AUX + 4096, QS = CF + 1280, CNT = CF + 3840;
  bf16x8 af0[4], af1[4];
  bf16x4 bl[10], bh[10];
#pragma unroll
  for (int i = 0; i < 4; ++i) { af1[i] = bf16x8{0, 0, 0, 0, 0, 0, 0, 0}; bl[i] = bf16x4{0, 0, 0, 0}; bh[i] = bf16x4{0, 0, 0, 0}; }
#define SB0() __builtin_amdgcn_sched_barrier(0)
  auto mm = [&](int n, int f, int ks, int mf) {
    const bf16x8 b = __builtin_shufflevector(bl[n % 10], bh[n % 10], 0, 1, 2, 3, 4, 5, 6, 7);
    if ((f >> 1) == 2) acc[mf][f] = __builtin_amdgcn_mfma_f32_16x16x32_bf16(ks ? af1[mf] : af0[mf], b, acc[mf][f], 0, 0, 0);
    else acc[mf][f] = __builtin_amdgcn_mfma_f32_16x16x32_bf16(b, ks ? af1[mf] : af0[mf], acc[mf][f], 0, 0, 0);
  };
  auto correct = [&]() {
    int l = threadIdx.x & 63;
    asm volatile("s_nop 7" : "+v"(l));
    const float* const tb = (const float*)(shm + DMP + wm * 2048);
    const float* const cf = (const float*)(shm + CF) + wc * 160;
    auto ld = [&](int f) { return f < 8 ? *(const f32x4*)(cf + (f < 4 ? f : f + 2) * 16 + (l >> 4) * 4) : *(const f32x4*)(tb + (f - 8) * 16 + (l >> 4) * 4); };
    float m[4];
    f32x4 cr[3];
#pragma unroll
    for (int mf = 0; mf < 4; ++mf) m[mf] = tb[mf * 16 + (l & 15)];
    cr[0] = ld(0);
    cr[1] = ld(1);
    const float c4 = cf[64 + (l & 15)], c5 = cf[80 + (l & 15)];
#pragma unroll
    for (int f = 0; f < 12; ++f) {
      if (f + 2 < 12) cr[(f + 2) % 3] = ld(f + 2);
      SB0();
      const f32x4 c = cr[f % 3];
#pragma unroll
      for (int i = 0; i < (f < 8 ? 4 : 2); ++i) {
        f32x4& a = f < 8 ? acc[i][f < 4 ? f : f + 2] : acc[f - 8][4 + i];
        float a0 = a[0], a1 = a[1], a2 = a[2], a3 = a[3];
        asm volatile("v_fmac_f32 %0, %4, %5\n\tv_fmac_f32 %1, %4, %6\n\tv_fmac_f32 %2, %4, %7\n\tv_fmac_f32 %3, %4, %8"
                     : "+v"(a0), "+v"(a1), "+v"(a2), "+v"(a3) : "v"(f < 8 ? m[i] : (i ? c5 : c4)), "v"(c[0]), "v"(c[1]), "v"(c[2]), "v"(c[3]));
        a = f32x4{a0, a1, a2, a3};
      }
      SB0();
    }
    asm volatile("s_nop 1");
  };
  auto kloop = [&](auto wmc) __attribute__((always_inline)) {
    constexpr bool LD = decltype(wmc)::value != 0;
    constexpr int grp = LD ? 1 : 0;
    const int WID = wid;
    const char* pa = nullptr;
    unsigned vob = 0;
    f32x4 ca = {0.f, 0.f, 0.f, 0.f}, cb = {0.f, 0.f, 0.f, 0.f};
    bf16x8 sel = {0, 0, 0, 0, 0, 0, 0, 0};
    int tro = 0;
    u64 tg = 0;
    const int psh = (wid == 2 && !tya) ? 1 : 0, pstr = wid < 3 ? qstr : F_ * 2;
    const char* const pbase = wid == 0 ? qb : (wid == 1 ? qb + dp1 : (wid == 2 ? mb0 : gb));
    if (!LD) {
      const int rs = mtile * 8 + (lane >> 3), vs = ((lane & 7) >> 1) ^ (((rs >> 1) & 1) | ((mtile & 1) << 1));
      pa = pbase + ((psh & vs) ? dgu : 0) + (rs * pstr + (vs >> psh) * 32 + (lane & 1) * 16);
      vob = rs * (F_ * 2) + vs * 32 + (lane & 1) * 16;
      const short one = fr == fq ? 0x3f80 : 0;
      sel = bf16x8{one, one, one, one, one, one, one, one};
      tro = q4 * 128 + ((fq ^ ((q4 >> 1) | ((mtile & 1) << 1))) * 32) + pp * 8;
      asm volatile("" : "+v"(sel), "+v"(tro), "+v"(ca), "+v"(cb));
    }
    auto share = [&](int t, unsigned dsta, unsigned dstb) {
      long off = (long)(t * 64 * pstr);
      asm volatile("" : "+s"(off));
      GLDS_NC(pa + off, shm_base + dsta);
      if (wid == 0) glds16_p(gb + dgu + (long)(t * 64 * F_ * 2), vob, shm_base + dstb);
    };
    auto accum = [&](int oa, int ob) {
      ca = __builtin_amdgcn_mfma_f32_16x16x32_bf16(tr_frag(shm3 + tro + oa), sel, ca, 0, 0, 0);
      if (wid == 0) cb = __builtin_amdgcn_mfma_f32_16x16x32_bf16(tr_frag(shm3 + tro + ob), sel, cb, 0, 0, 0);
    };
    auto cgather = [&]() {
      int l = threadIdx.x & 63;
      asm volatile("s_memrealtime %0\n\ts_waitcnt lgkmcnt(0)" : "=s"(tg), "+v"(l) :: "memory");
      const char* const p4 = l < 32 ? (const char*)(csum + (ntile * 5 + 4) * 512 + wid * 16) + (l >> 2) * 256
                                    : (const char*)(cstamp + (ntile * 4 + (l < 36 ? wid : 0)) * 8);
      if (l < 40) GLDS_SC1(p4 + (l & 3) * 16, shm_base + QS + wid * 640);
#pragma unroll
      for (int i = 0; i < 2; ++i) GLDS_SC1((const char*)(csum + (ntile * 5 + wid) * 512) + i * 1024 + l * 16, shm_base + DMP + wid * 2048 + i * 1024);
    };
    if (!LD) {
#pragma unroll
      for (int i = 0; i < ESH; ++i) share(2 + i, DMP + 7168, DMP + 7168);
      if (EROW) {
#pragma unroll
        for (int i = 0; i < 2; ++i) glds16_p(x + (size_t)(srow + i) * D_, lane * 16, shm_base + AUX + wid * 2048 + i * 1024);
      }
    }
    if (LD) {
#pragma unroll
      for (int i = 0; i < NP1; ++i) {
        stage_part(1, kz + 1, i >> 1, i & 1);
        asm volatile("" : "+v"(acc[0][i]), "+v"(acc[1][i]), "+v"(acc[2][i]), "+v"(acc[3][i]));
      }
    }
#pragma unroll
    for (int a = 0; a < 4; ++a)
      asm volatile("" : "+v"(acc[a][0]), "+v"(acc[a][1]), "+v"(acc[a][2]), "+v"(acc[a][3]), "+v"(acc[a][4]), "+v"(acc[a][5]), "+v"(acc[a][6]),
                   "+v"(acc[a][7]), "+v"(acc[a][8]), "+v"(acc[a][9]));
    asm volatile("" : "+v"(aoff[0]), "+v"(aoff[1]), "+v"(boff[0]), "+v"(boff[1]));
    TS1(3);
    if constexpr (LD) {
      TS1(4);
      WAIT_V(NP1);
      BAR();
    } else {
      TS1(4);
      if (wid) WAIT_V(ESH + 2 * EROW);
      WAIT_V(2 * ESH + 2 * EROW);
    }
    TS1(0);
#pragma clang loop unroll(disable)
    for (int kt = 0; kt < NT; ++kt) {
      int cur = kt & 1;
      if (!LD && kt == NT - 1) { correct(); __builtin_amdgcn_s_setprio(2); }
      if (!LD) {
        asm volatile("" : "+s"(cur));
        __builtin_amdgcn_s_waitcnt(0xC07F);
        BAR();
      }
      lds_cptr sb = shm3;
      if (LD) {
        unsigned k2 = kt - 1;
        asm volatile("" : "+s"(k2));
        if (k2 < NT - 2) {
#pragma unroll
          for (int i = 0; i < NP1; ++i) stage_part(cur ^ 1, kt + 1, i >> 1, i & 1);
        }
      }
      if (LD && kt + 1 < NT) {
#pragma unroll
        for (int i = NP1; i < 18; ++i) stage_part(cur ^ 1, kt + 1, i >> 1, i & 1);
      } else if (LD) {
        correct();
      }
      auto fadr = [&](int n) {
        const int f = n < 2 ? 8 + n : (n < 4 ? 11 - n : (n - 4) >> 1), ks = n < 2 ? 1 : (n < 4 ? 0 : n & 1);
        return sb + boff[f & 1] + (f >> 1) * 8192 + ks * 4096;
      };
      auto rdl = [&](int n) { bl[n % 10] = tr_read(fadr(n % 20)); };
      auto rdh = [&](int n) { bh[n % 10] = tr_read(fadr(n % 20) + 512); };
      auto rda = [&](bf16x8* af, int ks, int mf) { af[mf] = *(const __attribute__((address_space(3))) bf16x8*)(sb + aoff[ks] + mf * 2048); };
      SB0();
      if (LD) __builtin_amdgcn_s_waitcnt(0xC07F);
      sfor<80>([&](auto uc) __attribute__((always_inline)) {
        constexpr int u = decltype(uc)::value;
        SB0();
        if constexpr (u < 16) {
          const int n = u >> 2;
          mm(n, n < 2 ? 8 + n : 11 - n, n < 2 ? 1 : 0, (n & 1) ? 3 - (u & 3) : u & 3);
        } else {
          const int f = (u - 16) >> 3, i = (u >> 1) & 3, ks = (u ^ i) & 1;
          if ((u & 7) == 0) { asm volatile("" :: "v"(bl[(5 + 2 * f) % 10]), "v"(bh[(5 + 2 * f) % 10])); SB0(); }
          mm(4 + 2 * f + ks, f, ks, (f & 1) ? 3 - i : i);
        }
        SB0();
        if (u == 0) { rdl(2); rdh(2); }
        if (u == 1) { rda(af0, 0, 0); rda(af0, 0, 1); }
        if (u == 2) { rda(af0, 0, 2); rda(af0, 0, 3); }
        if (u == 3) { rdl(3); rdh(3); }
        if constexpr (u >= 4 && u < 8) rda(af1, 1, 7 - u);
        if (u >= 8 && u < 16) { if (u & 1) rdh(u >> 1); else rdl(u >> 1); }
        if (u >= RD0 && u < RD0 + 55 && (u & 1)) {
          const int n = 8 + ((u - RD0) >> 2);
          if ((u - RD0) & 2) rdh(n); else rdl(n);
        }
      });
      SB0();
      if (LD)
        asm volatile("s_cmp_eq_u32 %0, %1\n\ts_waitcnt vmcnt(0) lgkmcnt(0)\n\ts_cbranch_scc1 1f\n\ts_barrier\n1:" :: "s"(kt), "n"(NT - 1) : "memory", "scc");
      SB0();
      {
        const int d = cur ? -G1_STAGE : G1_STAGE;
        aoff[0] += d; aoff[1] += d; boff[0] += d; boff[1] += d;
        asm volatile("" : "+v"(aoff[0]), "+v"(aoff[1]), "+v"(boff[0]), "+v"(boff[1]));
      }
      if (!LD && kt <= 19 - SD) {
        int lane = threadIdx.x & 63;
        asm volatile("" : "+v"(lane));
        auto clock = [&]() { u64 now; asm volatile("s_memrealtime %0\n\ts_waitcnt lgkmcnt(0)" : "=s"(now) :: "memory"); return now; };
        WAIT_V(0);
        if (kt <= 16 - SD) {
          if (kt >= 1 + KS0) { accum(DMP + wid * 1024, DMP + 4096); WAIT_L(0); }
          if (kt <= 15 - SD) {
            if (kt >= KS0) share(kt + SD, DMP + wid * 1024, DMP + 4096);
          } else if ((lane & 15) < 4) {
            const __amdgpu_buffer_rsrc_t rc = __builtin_amdgcn_make_buffer_rsrc((void*)(csum + ntile * 5 * 512 + mtile * 64), 0, 5 * 2048, 0x00020000);
            const int co = (lane & 15) * 64 + (lane >> 4) * 16;
            __builtin_amdgcn_raw_buffer_store_b128(__builtin_bit_cast(u32x4, ca), rc, co + wid * 2048, 0, 16);
            if (wid == 0) __builtin_amdgcn_raw_buffer_store_b128(__builtin_bit_cast(u32x4, cb), rc, co + 4 * 2048, 0, 16);
          }
          if (kt < SD + KS0) {
            if (kt <= SD + KS0 - 3 - ESH) share(kt + 2 + ESH, DMP + 7168, DMP + 7168);
            const int sp = cur * G1_STAGE + 32768 + mtile * 1024;
            accum(sp + wid * 8192, sp + 4 * 8192);
            if (kt == 1 - EROW || kt == 2 - EROW) {
              const bf16x8 ones = {0x3f80, 0x3f80, 0x3f80, 0x3f80, 0x3f80, 0x3f80, 0x3f80, 0x3f80};
              const char* const xr = shm + AUX + wid * 2048 + (lane & 8) * 128 + (lane >> 4) * 128 + (lane & 7) * 16;
              const bf16x8 r0 = *(const bf16x8*)xr, r1 = *(const bf16x8*)(xr + 512);
              const f32x4 z = {0.f, 0.f, 0.f, 0.f};
              const f32x4 s1 = __builtin_amdgcn_mfma_f32_16x16x32_bf16(ones, r1, __builtin_amdgcn_mfma_f32_16x16x32_bf16(ones, r0, z, 0, 0, 0), 0, 0, 0);
              const f32x4 s2 = __builtin_amdgcn_mfma_f32_16x16x32_bf16(r1, r1, __builtin_amdgcn_mfma_f32_16x16x32_bf16(r0, r0, z, 0, 0, 0), 0, 0, 0);
              float t0 = __uint_as_float((unsigned)tg) + s1[0];
              asm volatile("" : "+v"(t0));
              const float t1 = __uint_as_float((unsigned)(tg >> 32)) + ((lane & 2) ? ((lane & 1) ? s2[3] : s2[2]) : ((lane & 1) ? s2[1] : s2[0]));
              tg = ((u64)__float_as_uint(t1) << 32) | __float_as_uint(t0);
            }
            if (kt < 2 - EROW) {
#pragma unroll
              for (int i = 0; i < 2; ++i) glds16_p(x + (size_t)(srow + i) * D_ + (kt + EROW) * 512, lane * 16, shm_base + AUX + wid * 2048 + i * 1024);
            }
            if (kt == PUB_K) {
              if (wid == 0 && lane < 8) *(unsigned*)(shm + CNT + lane * 4) = 0u;
              const bool dg = ((lane >> 2) & 3) == (lane >> 4);
              float sm = half_sum(dg ? __uint_as_float((unsigned)tg) : 0.f);
              asm volatile("" : "+v"(sm));
              const float sq = half_sum(dg ? __uint_as_float((unsigned)(tg >> 32)) : 0.f);
              const u64 now = clock();
              if ((lane & 31) == 0) {
                const __amdgpu_buffer_rsrc_t rs = __builtin_amdgcn_make_buffer_rsrc((void*)(rstat + srow), 0, 32, 0x00020000);
                __builtin_amdgcn_raw_buffer_store_b128(u32x4{__float_as_uint(sm), (unsigned)now, __float_as_uint(sq), (unsigned)now ^ (unsigned)(now >> 32)}, rs,
                                                       lane >> 1, 0, 16);
              }
            }
          }
          if (kt >= PF_K0 && kt < PF_K0 + 5) {
#pragma unroll
            for (int i = 0; i < 2; ++i) {
              const int c = (bid * 4 + wid) * 10 + (kt - PF_K0) * 2 + i;
              if (c < 9216) {
                const u16* src = (c < 7168 ? wd + (size_t)c * 512 : wo + (size_t)(c - 7168) * 512) + lane * 8;
                GLDS_NC(src, shm_base + DMP + 7168);
              }
            }
          }
          if (kt >= CP_K0 && kt <= CP_K0 + 4) {
            if (kt < CP_K0 + 3) {
              const int q1 = bid * 4 + WID + (kt - CP_K0) * 1024, q0 = q1 - 1024;
              if (q0 >= 0 && q0 < 1152) {
                const __amdgpu_buffer_rsrc_t rc = __builtin_amdgcn_make_buffer_rsrc((void*)(wcopy + (q0 & 1) * 589824 + (q0 >> 1) * 1024), 0, 1024, 0x00020000);
                __builtin_amdgcn_raw_buffer_store_b128(*(const u32x4*)(shm + AUX + WID * 1024 + lane * 16), rc, lane * 16, 0, 16);
                WAIT_L(0);
              }
              if (q1 < 1152) {
                const char* src = (const char*)((q1 >> 1) < 448 ? wd + (size_t)(q1 >> 1) * 8 * D_ : wo + (size_t)((q1 >> 1) - 448) * 8 * D_);
                src += ((3 - ((unsigned)(uintptr_t)src >> 7)) & 7) * 128 + (q1 & 1) * 1024;
                GLDS(src + (lane >> 3) * 2048 + (lane & 7) * 16, shm_base + AUX + WID * 1024);
              }
            } else if (kt == CP_K0 + 3) {
              GLDS_SC1(rstat + m0 + wid * 64 + (lane & 32) + 2 * (lane & 15) + ((lane >> 4) & 1), shm_base + AUX + wid * 1024);
            } else {
              if (WID == 0) {
                const u64 now = clock();
                if (lane == 0) __hip_atomic_store(&donec[bid], now, RLX_AGENT);
              }
              if (WID == 2) GLDS_SC1((const char*)(done2 + bid * 128) + lane * 16, shm_base + DMP + 7168);
            }
          }
        } else if (kt == 17 - SD) {
          const u64 now = clock();
          if (lane == 0) __hip_atomic_store(&cstamp[(ntile * 4 + wid) * 8 + mtile], now, RLX_AGENT);
          if (WID >= 2) GLDS_SC1((const char*)donec + (WID - 2) * 1024 + lane * 16, shm_base + DMP + WID * 2048);
          {
            const char* const wp = (const char*)wd + (size_t)(xcd < 4 ? 28 + 5 * (xcd & 1) : 5 - 5 * (xcd & 1)) * 131072 + (idx * 4 + wid) * 1024;
#pragma unroll
            for (int i = 0; i < NWPF; ++i) glds16_p(wp + (long)((i & 1) + ((i >> 1) & 1) * 14 + (i >> 2) * 7) * 131072, lane * 16, shm_base + DMP + wid * 2048 + 1024);
          }
          u32x4 gr = *(const u32x4*)(shm + AUX + wid * 1024 + lane * 16);
          bool late = false;
          if (__builtin_expect(!__all(fresh(((u64)(gr[1] ^ gr[3]) << 32) | gr[1], t_own)), 0)) {
            const __amdgpu_buffer_rsrc_t rs = __builtin_amdgcn_make_buffer_rsrc((void*)(rstat + m0), 0, 4096, 0x00020000);
            const int prow = wid * 64 + (lane & 32) + 2 * (lane & 15) + ((lane >> 4) & 1);
            for (;;) {
              gr = __builtin_bit_cast(u32x4, __builtin_amdgcn_raw_buffer_load_b128(rs, prow * 16, 0, 16));
              if (__all(fresh(((u64)(gr[1] ^ gr[3]) << 32) | gr[1], t_own))) break;
              if (__builtin_amdgcn_s_memrealtime() - t_own > TIMEOUT_) { gr[1] = row_stats_serial(x + (size_t)(m0 + prow) * D_); late = true; break; }
              __builtin_amdgcn_s_sleep(2);
            }
          }
          float mean = __uint_as_float(gr[0]) * (1.0f / D_);
          asm volatile("" : "+v"(mean));
          const unsigned ps = pack_stats(mean, fmaxf(__builtin_fmaf(-mean, mean, __uint_as_float(gr[2]) * (1.0f / D_)), 0.f));
          const unsigned g0 = late ? gr[1] : ps;
          const float nm = -lo_f(g0), dn = hi_f(g0), r = __builtin_amdgcn_rcpf(dn);
          float k1 = -2.3022081986f * r, d2 = dn * dn;
          asm volatile("" : "+v"(k1));
          float k2 = -0.10294324f * r;
          asm volatile("" : "+v"(d2), "+v"(k2));
          const f32x4 fc = {nm, k1, k2 * r * r, d2};
          *(f32x4*)(shm + AUX + wid * 1024 + lane * 16) = fc;
        } else if (kt == 18 - SD) {
          const u32x4 c4 = *(const u32x4*)(shm + DMP + WID * 2048 + lane * 16);
          asm volatile("" :: "v"(c4) : "memory");
          cgather();
          const u64 win = tg - t_own;
          auto frs = [&](unsigned lo, unsigned hi) { return (((u64)hi << 32) | lo) - t_own - 100ull < win; };
          if (WID >= 2) *(unsigned*)(shm + CNT - 4 + WID * 4) = __all(frs(c4[0], c4[1]) & frs(c4[2], c4[3])) ? 1u : 0u;
        } else {
          const char* const sc = shm + (lane < 32 ? DMP + wid * 2048 + lane * 8 : QS + wid * 640 + (lane & 7) * 8);
          const int st = lane < 32 ? 256 : 64;
          const f32x4 fc = *(const f32x4*)(shm + AUX + wid * 1024 + lane * 16);
          f32x2 w[8];
          bool late = false;
          for (;;) {
            const u64 sv = *(const u64*)(shm + QS + wid * 640 + 512 + (lane & 15) * 8);
#pragma unroll
            for (int r = 0; r < 8; ++r) w[r] = *(const f32x2*)(sc + r * st);
            if (__builtin_expect(__all(sv - t_own - 100ull < tg - t_own - 200ull), 1)) break;
            late = __builtin_amdgcn_s_memrealtime() - t_own > TIMEOUT_;
            if (late) break;
            __builtin_amdgcn_s_sleep(2);
            WAIT_L(0);
            cgather();
            WAIT_V(0);
          }
          float t0 = w[0][0], t1 = w[0][1];
#pragma unroll
          for (int r = 1; r < 8; ++r)
            asm("v_add_f32 %0, %0, %2\n\tv_add_f32 %1, %1, %3" : "+v"(t0), "+v"(t1) : "v"(w[r][0]), "v"(w[r][1]));
          if (__builtin_expect(late, 0)) {
            const int gq = (lane >> 3) & 3;
            const char* pc = (lane < 32 ? pbase + ((psh & gq) ? dgu : 0) + (gq >> psh) * 32 : gb + dgu + wid * 32) + (lane & 7) * 4;
            t0 = 0.f; t1 = 0.f;
#pragma unroll 1
            for (int k = 0; k < D_; ++k) { const unsigned v = *(const unsigned*)(pc + (size_t)k * (lane < 32 ? pstr : F_ * 2)); t0 += lo_f(v); t1 += hi_f(v); }
          }
          if (lane < 40)
            *(f32x2*)((float*)(shm + CF) + (lane < 32 ? (lane >> 4) * 160 + wid * 32 + (lane & 15) * 2 : (wid >> 1) * 160 + 128 + (wid & 1) * 16 + (lane & 7) * 2)) = f32x2{t0, t1};
          float* const tw = (float*)(shm + DMP + WID * 2048) + lane;
#pragma unroll
          for (int i = 0; i < 4; ++i) tw[i * 64] = fc[i];
          ((float*)(shm + QS + WID * 640))[lane] = fc[1] * (-1.0f / 2.3022081986f);
        }
      }
      SB0();
    }
#pragma unroll
    for (int u = 0; u < 8; ++u) mm(u >> 2, 8 + (u >> 2), 1, u & 3);
  TS1(1);
  __builtin_amdgcn_sched_barrier(0);
  int tz = threadIdx.x;
  asm volatile("s_nop 7" : "+v"(tz));
  __builtin_amdgcn_sched_barrier(0);
  const int li = tz & 15, lq = (tz >> 4) & 3, ln = tz & 63;
  const __amdgpu_buffer_rsrc_t ra = __builtin_amdgcn_make_buffer_rsrc((void*)abuf, 0, 16 * 72 * 16384, 0x00020000);
  float* const svr = (float*)(shm + QS + wm * 640);
  float* const tb = (float*)(shm + DMP + wm * 2048);
  typedef __attribute__((address_space(3))) unsigned lds_u32;
  lds_u32* const cnt = (lds_u32*)(shm3 + CNT);
  unsigned vcnt = shm_base + CNT;
  asm volatile("" : "+v"(vcnt));
  auto signal = [&](unsigned v) {
    unsigned t;
    asm volatile("v_mov_b32 %0, %2\n\ts_mov_b64 exec, 1\n\tds_add_u32 %1, %0\n\ts_mov_b64 exec, -1" : "=&v"(t) : "v"(vcnt), "s"(v) : "memory");
  };
  auto await = [&](unsigned mask, unsigned val) {
    unsigned t, u;
    asm volatile("1:\n\tds_read_b32 %0, %2\n\ts_waitcnt lgkmcnt(0)\n\tv_readfirstlane_b32 %1, %0\n\ts_and_b32 %1, %1, %3\n\ts_cmp_eq_u32 %1, %4\n\t"
                 "s_cbranch_scc1 2f\n\ts_sleep 1\n\ts_branch 1b\n\t2:" : "=&v"(t), "=&s"(u) : "v"(vcnt), "s"(mask), "s"(val) : "memory", "scc");
  };
  if (grp) __builtin_amdgcn_s_setprio(1);
  signal(1u << (8 * grp));
  f32x4 rf[4];
#pragma unroll
  for (int mf = 0; mf < 4; ++mf) rf[mf] = *(const f32x4*)(shm + AUX + (wm * 64 + mf * 16 + li) * 16);
  const int sbase = ((mtile * 2 + (wm >> 1)) * 72 * 8 + (wm & 1) * 4) * 2048;
  auto putf = [&](int kg, int mf, u32x4 v) {
    __builtin_amdgcn_raw_buffer_store_b128(v, ra, ln * 16 + sbase + ((kg >> 1) * 8 + mf) * 2048 + (kg & 1) * 1024, 0, 16);
  };
  auto gelp = [&](f32x2 tg, f32x2 tu, f32x2 k12, f32x2 k22, f32x2 d2) {
    const f32x2 arg = tg * __builtin_elementwise_fma(tg * tg, k22, k12);
    const f32x2 dn = __builtin_elementwise_fma(f32x2{__builtin_amdgcn_exp2f(arg[0]), __builtin_amdgcn_exp2f(arg[1])}, d2, d2);
    const f32x2 r = (tg * tu) * f32x2{__builtin_amdgcn_rcpf(dn[0]), __builtin_amdgcn_rcpf(dn[1])};
    return pack2(r[0], r[1]);
  };
  auto gels = [&](const f32x4& g, const f32x4& u, const f32x4& k) {
    float t0, t1, t2, t3, n0, n1, n2, n3;
    asm("v_mul_f32 %0, %8, %8\n\tv_mul_f32 %1, %9, %9\n\tv_mul_f32 %2, %10, %10\n\tv_mul_f32 %3, %11, %11\n\t"
        "v_fma_f32 %0, %0, %17, %16\n\tv_fma_f32 %1, %1, %17, %16\n\tv_fma_f32 %2, %2, %17, %16\n\tv_fma_f32 %3, %3, %17, %16\n\t"
        "v_mul_f32 %0, %8, %0\n\tv_mul_f32 %1, %9, %1\n\tv_mul_f32 %2, %10, %2\n\tv_mul_f32 %3, %11, %3\n\t"
        "v_exp_f32 %0, %0\n\tv_mul_f32 %4, %8, %12\n\tv_exp_f32 %1, %1\n\tv_mul_f32 %5, %9, %13\n\t"
        "v_exp_f32 %2, %2\n\tv_mul_f32 %6, %10, %14\n\tv_exp_f32 %3, %3\n\tv_mul_f32 %7, %11, %15\n\t"
        "v_fma_f32 %0, %0, %18, %18\n\tv_fma_f32 %1, %1, %18, %18\n\tv_rcp_f32 %0, %0\n\tv_fma_f32 %2, %2, %18, %18\n\t"
        "v_rcp_f32 %1, %1\n\tv_fma_f32 %3, %3, %18, %18\n\tv_rcp_f32 %2, %2\n\tv_mul_f32 %0, %4, %0\n\t"
        "v_rcp_f32 %3, %3\n\tv_mul_f32 %1, %5, %1\n\tv_mul_f32 %2, %6, %2\n\tv_mul_f32 %3, %7, %3\n\t"
        "v_cvt_pk_bf16_f32 %0, %0, %1\n\tv_cvt_pk_bf16_f32 %1, %2, %3"
        : "=&v"(t0), "=&v"(t1), "=&v"(t2), "=&v"(t3), "=&v"(n0), "=&v"(n1), "=&v"(n2), "=&v"(n3)
        : "v"(g[0]), "v"(g[1]), "v"(g[2]), "v"(g[3]), "v"(u[0]), "v"(u[1]), "v"(u[2]), "v"(u[3]), "v"(k[1]), "v"(k[2]), "v"(k[3]));
    return u32x2{__float_as_uint(t0), __float_as_uint(t1)};
  };
  auto gel2 = [&](int p, int kg, int mf0 = 0, int mf1 = 4, int nsc = 0, bool opq = false) {
#pragma unroll
    for (int mf = mf0; mf < mf1; ++mf) {
      const f32x2 k12 = {rf[mf][1], rf[mf][1]}, k22 = {rf[mf][2], rf[mf][2]}, d2 = {rf[mf][3], rf[mf][3]};
      u32x4 o;
      if (opq) asm volatile("" : "+v"(acc[mf][p * 2]), "+v"(acc[mf][p * 2 + 1]));
      if (mf < nsc) {
        const u32x2 a = gels(acc[mf][p * 2], acc[mf][p * 2 + 2], rf[mf]), b = gels(acc[mf][p * 2 + 1], acc[mf][p * 2 + 3], rf[mf]);
        o = u32x4{a[0], a[1], b[0], b[1]};
      } else {
#pragma unroll
        for (int i = 0; i < 4; ++i) {
          const int j = i >> 1, e = (i & 1) * 2;
          const f32x4 g = acc[mf][p * 2 + j], u = acc[mf][p * 2 + 2 + j];
          o[i] = gelp(f32x2{g[e], g[e + 1]}, f32x2{u[e], u[e + 1]}, k12, k22, d2);
        }
      }
      putf(kg, mf, o);
      if (mf < nsc) SB0();
    }
  };
  auto client = [&](int l) { return (((l & 7) * 4 + (mtile & 3)) * 8) + (((mtile >> 2) * 2 + ((l >> 4) & 1)) ^ ((l & 8) ? 5 : 0)); };
  auto notify = [&](u64 lanes) {
    int l = threadIdx.x & 63;
    asm volatile("" : "+v"(l));
    const unsigned off = (client(l) * 128 + (l & 32) + ntile) * 8;
    u64 now, vn;
    asm volatile("s_memrealtime %0\n\ts_nop 0\n\ts_waitcnt lgkmcnt(0)\n\tv_mov_b64 %1, %0\n\ts_mov_b64 exec, %4\n\tglobal_store_dwordx2 %2, %1, %3 sc1\n\ts_mov_b64 exec, -1"
                 : "=&s"(now), "=&v"(vn) : "v"(off), "s"(done1), "s"(lanes) : "memory");
  };
  const u64 myh = 0xffffull << ((wm >> 1) * 16);
  const u64 mrole = (xcd & 4) ? 0x00ff00ffull : 0xff00ff00ull;
  const u64 mall = (tya || (xcd & 3) == 3) ? 0xffffffffull : mrole;
  auto arrive = [&](auto pc) {
    constexpr int part = decltype(pc)::value;
    unsigned t;
    asm volatile("v_mov_b32 %0, 1\n\ts_mov_b64 exec, 1\n\tds_add_rtn_u32 %0, %1, %0 offset:%2\n\ts_mov_b64 exec, -1\n\ts_waitcnt lgkmcnt(0)"
                 : "=&v"(t) : "v"(vcnt), "n"(12 + 4 * part) : "memory");
    if (__builtin_amdgcn_readfirstlane(t) == 3u) notify(part ? (myh & mall) << 32 : myh & mrole);
  };
  const int role = 1 - (xcd >> 2);
  const int xb = role ? xcd : xcd ^ 5, par = xb & 1;
  const int mt2 = ((xb >> 1) * 4 + (idx & 3)) * 2 + par, nt2 = idx >> 2;
  auto rsrc = [](u64 b, unsigned n) { return u32x4{(unsigned)b, (unsigned)(b >> 32) & 0xffffu, n, 0x00020000u}; };
  u32x4 rw[2];
  int wsh[2];
  unsigned vb[2][2], cokw = 0;
  auto wsetup = [&](const u16* w, int rows, int crow0, int l) {
    const unsigned vr = (wid >> 1) * 32 + (wid & 1) * 8 + ((l >> 5) & 1) * 16 + ((l >> 3) & 3), vc = (((l >> 1) & 3) ^ ((l >> 4) & 1)) * 32 + (l & 1) * 16;
    u64 bs = (u64)(uintptr_t)(wcopy + (nt2 >> 2) * 589824 + crow0 * 128 - 1024), bf = (u64)(uintptr_t)(w + nt2 * 128) - 1024;
    asm volatile("" : "+s"(bs), "+s"(bf));
#pragma unroll
    for (int i = 0; i < 2; ++i) {
      const bool slow = ((((unsigned)bf >> 7) + i) & 7) == (cokw ? 3u : 8u);
      const int sh = slow ? 7 : 11;
      rw[i] = rsrc(slow ? bs : bf + i * 128, ((rows - 1) << sh) + 128 + 1024);
      wsh[i] = 6 + sh;
      vb[i][0] = (vr << sh) + vc + 1024;
      vb[i][1] = ((vr + 4) << sh) + (vc ^ 64);
    }
  };
#define G2_W "s_add_u32 m0, %[l], %[ow]\n\ts_lshl_b32 %[t], %[k], %[s0]\n\tbuffer_load_dwordx4 %[v0], %[w0], %[t] offen sc1 lds\n\t" \
             "buffer_load_dwordx4 %[v1], %[w0], %[t] offen offset:1024 sc1 lds\n\t" \
             "s_add_u32 m0, m0, 8192\n\ts_lshl_b32 %[t], %[k], %[s1]\n\tbuffer_load_dwordx4 %[v2], %[w1], %[t] offen sc1 lds\n\t" \
             "buffer_load_dwordx4 %[v3], %[w1], %[t] offen offset:1024 sc1 lds"
#define G2_WOP [ow] "s"(16384 + wid * 2048), [k] "s"(kt), [s0] "s"(wsh[0]), [s1] "s"(wsh[1]), [w0] "s"(rw[0]), [w1] "s"(rw[1]), \
               [v0] "v"(vb[0][0]), [v1] "v"(vb[0][1]), [v2] "v"(vb[1][0]), [v3] "v"(vb[1][1])
#define G2_A "s_add_u32 m0, %[l], %[oa]\n\ts_lshl_b32 %[t], %[k], 14\n\tbuffer_load_dwordx4 %[va], %[a], %[t] offen sc1 lds\n\t" \
             "buffer_load_dwordx4 %[va], %[a], %[t] offen offset:1024 sc1 lds\n\t"
#define G2_A2 "buffer_load_dwordx4 %[va], %[a], %[t] offen offset:2048 sc1 lds\n\tbuffer_load_dwordx4 %[va], %[a], %[t] offen offset:3072 sc1 lds\n\t"
  constexpr int PD = G2_NSTAGE - 1;
  constexpr int NY = 28;
  const int ny = (xcd & 1) ? NY - DLT : NY + DLT;
  const u32x4 ra2 = rsrc((u64)(uintptr_t)abuf + mt2 * (72 * 16384), 72 * 16384);
  unsigned va, tab, nlo, nhi, sl = shm_base + 4 * G2_STAGE;
  const int kfirst = 28 * role + 5 * par;
  int q = 3, kt = kfirst + 15;
  u64 okb = 0;
  auto adv = [&]() { ++q; kt = __builtin_amdgcn_readlane(tab, q); sl = sl == shm_base + (G2_NSTAGE - 1) * G2_STAGE ? shm_base : sl + G2_STAGE; };
  auto stage_y = [&]() {
    unsigned t;
    asm volatile(G2_A G2_A2 G2_W : [t] "=&s"(t) : [l] "s"(sl), [oa] "s"((wid ^ role) * 4096), [a] "s"(ra2), [va] "v"(va), G2_WOP : "memory", "scc", "m0");
    adv();
  };
  auto digest = [&](u64 v, u64 valid = ~0ull) {
    const u64 r = __ballot(fresh(v, t_own)) & valid;
    const unsigned hi = (unsigned)(r >> 32), lo = (unsigned)r | hi;
    okb |= __ballot(((nlo & ~lo) | (nhi & ~hi)) == 0u);
  };
  auto aload = [&](const u64* base, unsigned off) {
    asm volatile("s_nop 4\n\tglobal_load_dwordx2 v[254:255], %0, %1 sc1" :: "v"(off), "s"(base) : "memory", "v254", "v255");
    SB0();
  };
  auto aget = [&]() { u64 v; asm volatile("v_mov_b64 %0, v[254:255]" : "=v"(v) :: "memory"); return v; };
  auto snapshot = [&]() { aload(done1 + bid * 128, ln * 8); };
  auto peek = [&]() { digest(aget()); };
  auto snapshot1 = [&]() { aload(done1 + bid * 128 + role * 16, (ln & 15) * 8); };
  auto peek1 = [&]() { digest(aget(), 0xffffull << (16 * role)); };
  auto wpair = [&](int g, int i) {
    unsigned t;
    asm volatile("s_mov_b32 m0, %1\n\ts_lshl_b32 %0, %2, %3\n\tbuffer_load_dwordx4 %5, %4, %0 offen sc1 lds\n\tbuffer_load_dwordx4 %6, %4, %0 offen offset:1024 sc1 lds"
                 : "=&s"(t) : "s"(shm_base + (g + 1) * G2_STAGE + 16384 + i * 8192 + wid * 2048), "s"(kfirst + (g == 2 ? 14 : g)), "s"(wsh[i]), "s"(rw[i]),
                   "v"(vb[i][0]), "v"(vb[i][1]) : "memory", "scc", "m0");
    SB0();
  };
  auto aquad = [&](int g) {
    unsigned t;
    asm volatile(G2_A G2_A2 : [t] "=&s"(t) : [l] "s"(shm_base + (g + 1) * G2_STAGE), [oa] "s"((wid ^ role) * 4096), [a] "s"(ra2), [va] "v"(va),
                 [k] "s"(((okb >> g) & 1) ? kfirst + (g == 2 ? 14 : g) : 100) : "memory", "scc", "m0");
    SB0();
  };
  auto mid = [&]() {
    cokw = __builtin_amdgcn_readfirstlane(cnt[1] & cnt[2]);
    wsetup(wd, F_, 0, ln);
    va = ln * 16 + wid * 4096;
    {
      const int ext = ln >= NY ? 1 : 0, qq = ln - ext * DLT, so = ln - ny;
      int j0 = 5 * par, j1 = 5 - 5 * par;
      asm volatile("" : "+s"(j1));
      const int j = (qq < 8 ? j0 : (qq < 16 ? j1 : 2)) + ((unsigned)(0x92492444444444ull >> (2 * qq)) & 3);
      const int xs = ((role ^ ext) << 2) | ((unsigned)(0xfd5a80f5a0f5a0ull >> (2 * qq)) & 3);
      unsigned bits = ((0x41a8282u >> (4 * j)) & 15u) << (4 * xs), ob = (so < 16 ? 1u : 0u) << (2 * so), ty = 7 * xs + j;
      asm volatile("" : "+v"(bits), "+v"(ob), "+v"(ty));
      tab = so < 0 ? ty : so;
      nlo = ln < 8 ? bits : 0u;
      nhi = so < 0 ? bits - nlo : ob;
    }
    asm volatile("" :: "s"(rw[0]), "s"(rw[1]), "s"(wsh[0]), "s"(wsh[1]), "s"(ra2));
    asm volatile("" : "+v"(vb[0][0]), "+v"(vb[0][1]), "+v"(vb[1][0]), "+v"(vb[1][1]), "+v"(va), "+v"(tab), "+v"(nlo), "+v"(nhi));
    await(0xffffu, 0x404u);
    wpair(0, 0);
    wpair(0, 1);
    WAIT_V(4);
    arrive(std::integral_constant<int, 0>{});
    __builtin_amdgcn_s_setprio(PRB);
  };
  int tsel = ntile & 1;
  asm volatile("" : "+s"(tsel));
  if (tsel != 0) {
    if (grp == 0) gel2(0, 2 * (kt0 + he) + wc, 0, NSC, NSC);
    else gel2(0, 2 * (kt0 + he) + wc, 0, NSC);
    gel2(0, 2 * (kt0 + he) + wc, NSC, 4);
    if (grp == 0) mid();
#pragma unroll
    for (int mf = 0; mf < 4; ++mf) {
      f32x4 t[4];
#pragma unroll
      for (int i = 1; i < 4; ++i) t[i] = *(const f32x4*)(tb + i * 64 + mf * 16 + lq * 4);
      const f32x4 g = acc[mf][4], u = acc[mf][5];
      u32x2 o;
#pragma unroll
      for (int e = 0; e < 4; e += 2)
        o[e >> 1] = gelp(f32x2{g[e], g[e + 1]}, f32x2{u[e], u[e + 1]}, f32x2{t[1][e], t[1][e + 1]}, f32x2{t[2][e], t[2][e + 1]}, f32x2{t[3][e], t[3][e + 1]});
      *(u32x2*)(shm + wm * 4096 + mf * 1024 + wc * 512 + li * 32 + lq * 8) = o;
      if (mf == 1 || grp == 0) SB0();
      if (grp == 0) {
        wpair(1 + (mf >> 1), mf & 1);
        if (mf == SNP) snapshot1();
      }
    }
    signal(0x10000u << vid);
    TS1(5);
    if (grp) {
      gel2(3, 2 * (kt0 + 2 + he) + wc, 0, 4);
    } else {
#pragma unroll
      for (int mf = 0; mf < 4; ++mf) {
        gel2(3, 2 * (kt0 + 2 + he) + wc, mf, mf + 1);
        SB0();
        if (mf == 0) { WAIT_V(7 - 2 * SNP); peek1(); }
        if (mf < 3) aquad(mf);
        if (mf == 2) snapshot1();
      }
    }
    await(0x110000u << wm, 0x110000u << wm);
#pragma unroll
    for (int i = 0; i < 2; ++i) putf(2 * (kt0 + 4) + he, 2 * wc + i, __builtin_bit_cast(u32x4, tr_frag(shm3 + wm * 4096 + wc * 2048 + i * 1024 + ln * 8)));
    if (grp == 0) WAIT_V(9);
    TS1(6);
  } else {
    if (grp) __builtin_amdgcn_s_setprio(PRL0);
    float rd[4];
#pragma unroll
    for (int mf = 0; mf < 4; ++mf) rd[mf] = svr[mf * 16 + li];
    auto cvs = [&](const f32x4& a, const f32x4& b, const f32x4& r) {
      float t0, t1, t2, t3, t4, t5, t6, t7;
      asm("v_mul_f32 %0, %8, %16\n\tv_mul_f32 %4, %9, %17\n\tv_mul_f32 %1, %10, %18\n\tv_mul_f32 %5, %11, %19\n\t"
          "v_mul_f32 %2, %12, %16\n\tv_mul_f32 %6, %13, %17\n\tv_mul_f32 %3, %14, %18\n\tv_mul_f32 %7, %15, %19\n\t"
          "v_cvt_pk_bf16_f32 %0, %0, %4\n\tv_cvt_pk_bf16_f32 %1, %1, %5\n\tv_cvt_pk_bf16_f32 %2, %2, %6\n\tv_cvt_pk_bf16_f32 %3, %3, %7"
          : "=&v"(t0), "=&v"(t1), "=&v"(t2), "=&v"(t3), "=&v"(t4), "=&v"(t5), "=&v"(t6), "=&v"(t7)
          : "v"(a[0]), "v"(a[1]), "v"(a[2]), "v"(a[3]), "v"(b[0]), "v"(b[1]), "v"(b[2]), "v"(b[3]), "v"(r[0]), "v"(r[1]), "v"(r[2]), "v"(r[3]));
      return u32x4{__float_as_uint(t0), __float_as_uint(t1), __float_as_uint(t2), __float_as_uint(t3)};
    };
    auto cvp = [&](const f32x4& a, const f32x4& b) { return u32x4{pack2(a[0], a[1]), pack2(a[2], a[3]), pack2(b[0], b[1]), pack2(b[2], b[3])}; };
    auto cvk = [&](int mf, bool sc) {
      const f32x4 r4 = {rd[mf], rd[mf], rd[mf], rd[mf]};
      return sc ? cvs(acc[mf][2], acc[mf][3], r4) : cvp(acc[mf][2] * r4, acc[mf][3] * r4);
    };
    auto cvv = [&](int mf, bool sc) {
      const f32x4 r4 = *(const f32x4*)(svr + mf * 16 + lq * 4);
      return sc ? cvs(acc[mf][4], acc[mf][5], r4) : cvp(acc[mf][4] * r4, acc[mf][5] * r4);
    };
    u32x4 kx[4], vx[4], qx[4], hx[4];
    const bf16x8 ones = {0x3f80, 0x3f80, 0x3f80, 0x3f80, 0x3f80, 0x3f80, 0x3f80, 0x3f80};
    kx[2] = cvk(2, true); kx[3] = cvk(3, true);
    vx[2] = cvv(2, true); vx[3] = cvv(3, true);
    const bool giveh = !grp && wm == 3 && mtile < 7, needh = grp && wm == 0 && mtile > 0;
    char* const hptr = (char*)halo + (size_t)((needh ? mtile - 1 : mtile) * 32 + ntile + wc) * 16384;
    if (grp || wm < 3) {
      char* const ex = shm + (vid - wc) * 4096 + ln * 16;
      *(u32x4*)ex = kx[2]; *(u32x4*)(ex + 1024) = kx[3]; *(u32x4*)(ex + 2048) = vx[2]; *(u32x4*)(ex + 3072) = vx[3];
      signal(0x10000u << vid);
    } else if (giveh) {
      const __amdgpu_buffer_rsrc_t rh = __builtin_amdgcn_make_buffer_rsrc((void*)hptr, 0, 9216, 0x00020000);
      const u64 now = __builtin_amdgcn_s_memrealtime();
      const unsigned tag = (unsigned)now;
#pragma unroll
      for (int r = 0; r < 8; ++r) {
        const u32x4 w = r < 4 ? kx[2 + (r >> 1)] : vx[r >> 1];
        __builtin_amdgcn_raw_buffer_store_b128(u32x4{w[2 * (r & 1)], tag, w[2 * (r & 1) + 1], tag}, rh, ln * 16 + r * 1024, 0, 16);
      }
      __builtin_amdgcn_raw_buffer_store_b128(u32x4{tag, (unsigned)(now >> 32), tag, (unsigned)(now >> 32)}, rh, ln * 16 + 8192, 0, 16);
    }
    if (grp == 0) { gel2(3, 2 * (kt0 + 5 + he) + wc, 0, 4, NSC - 1, true); mid(); }
    kx[0] = cvk(0, false); kx[1] = cvk(1, false);
    vx[0] = cvv(0, false); vx[1] = cvv(1, false);
#pragma unroll
    for (int mf = 0; mf < 4; ++mf) qx[mf] = cvp(acc[mf][0], acc[mf][1]);
    f32x4 mk0, mk1;
#pragma unroll
    for (int j = 0; j < 4; ++j) {
      const int k0 = 4 * lq + j, k1 = 16 + 4 * lq + j;
      mk0[j] = (k0 >= li + 9 && k0 <= li + 16) ? 0.f : -1.0e30f;
      mk1[j] = (k1 >= li + 9 && k1 <= li + 16) ? 0.f : -1.0e30f;
    }
    auto max3 = [](float a, float b, float c) { float r; asm("v_max3_f32 %0, %1, %2, %3" : "=v"(r) : "v"(a), "v"(b), "v"(c)); return r; };
    auto max2 = [](float a, float b) { float r; asm("v_max_f32 %0, %1, %2" : "=v"(r) : "v"(a), "v"(b)); return r; };
    auto tiles = [&](int t0, const u32x4* kp, const u32x4* vp, bool first_rows) {
      const f32x4 z = {0.f, 0.f, 0.f, 0.f};
      const f32x4 mz = {-1.0e30f, -1.0e30f, -1.0e30f, -1.0e30f};
      f32x4 s0[2], s1[2];
#pragma unroll
      for (int n = 0; n < 2; ++n) {
        const bf16x8 qf = __builtin_bit_cast(bf16x8, qx[t0 + n]);
        s0[n] = __builtin_amdgcn_mfma_f32_16x16x32_bf16(__builtin_bit_cast(bf16x8, kp[n]), qf, first_rows ? mz : mk0, 0, 0, 0);
        s1[n] = __builtin_amdgcn_mfma_f32_16x16x32_bf16(__builtin_bit_cast(bf16x8, kx[t0 + n]), qf, mk1, 0, 0, 0);
      }
      SB0();
      if (grp == 0) { if (t0) wpair(1, 1); else aquad(1); }
      asm volatile("s_nop 7\n\ts_nop 3" : "+v"(s0[0]), "+v"(s1[0]), "+v"(s0[1]), "+v"(s1[1]));
      float mx[2];
#pragma unroll
      for (int n = 0; n < 2; ++n) mx[n] = max3(max3(s0[n][0], s0[n][1], s0[n][2]), max3(s0[n][3], s1[n][0], s1[n][1]), max2(s1[n][2], s1[n][3]));
#pragma unroll
      for (int n = 0; n < 2; ++n) { auto r = __builtin_amdgcn_permlane16_swap(__float_as_uint(mx[n]), __float_as_uint(mx[n]), false, false);
        mx[n] = max2(__uint_as_float(r[0]), __uint_as_float(r[1])); }
#pragma unroll
      for (int n = 0; n < 2; ++n) { auto r = __builtin_amdgcn_permlane32_swap(__float_as_uint(mx[n]), __float_as_uint(mx[n]), false, false);
        mx[n] = max2(__uint_as_float(r[0]), __uint_as_float(r[1])); }
      SB0();
      if (grp == 0) {
        if (t0) wpair(2, 0); else aquad(2);
        snapshot1();
      }
      constexpr float cs2 = 0.17677669529663687f * 1.4426950408889634f;
      f32x4 os[2], o0[2], o1[2];
#pragma unroll
      for (int n = 0; n < 2; ++n) {
        const float c = cs2 * rd[t0 + n], nm = -mx[n] * c;
        float e8[8];
#pragma unroll
        for (int j = 0; j < 4; ++j) {
          e8[j] = __builtin_amdgcn_exp2f(__builtin_fmaf(s0[n][j], c, nm));
          e8[4 + j] = __builtin_amdgcn_exp2f(__builtin_fmaf(s1[n][j], c, nm));
        }
        u32x4 pk;
#pragma unroll
        for (int j = 0; j < 4; ++j) pk[j] = pack2(e8[2 * j], e8[2 * j + 1]);
        const bf16x8 pf = __builtin_bit_cast(bf16x8, pk);
        const u32x4 va = {vp[n][0], vp[n][1], vx[t0 + n][0], vx[t0 + n][1]}, vb = {vp[n][2], vp[n][3], vx[t0 + n][2], vx[t0 + n][3]};
        os[n] = __builtin_amdgcn_mfma_f32_16x16x32_bf16(ones, pf, z, 0, 0, 0);
        o0[n] = __builtin_amdgcn_mfma_f32_16x16x32_bf16(__builtin_bit_cast(bf16x8, va), pf, z, 0, 0, 0);
        o1[n] = __builtin_amdgcn_mfma_f32_16x16x32_bf16(__builtin_bit_cast(bf16x8, vb), pf, z, 0, 0, 0);
      }
      if (grp == 0 && t0) { SB0(); wpair(2, 1); }
#pragma unroll
      for (int n = 0; n < 2; ++n) {
        const float inv = __builtin_amdgcn_rcpf(os[n][0]);
        const f32x4 n0 = o0[n] * inv, n1 = o1[n] * inv;
        putf(112 + ntile + wc, t0 + n, u32x4{pack2(n0[0], n0[1]), pack2(n0[2], n0[3]), pack2(n1[0], n1[1]), pack2(n1[2], n1[3])});
      }
    };
    TS1(5);
    if (grp == 0) wpair(1, 0);
    tiles(2, &kx[0], &vx[0], false);
    constexpr int HL = 131072, HS = DMP + 1024;
    auto hload = [&]() {
      asm volatile("s_mov_b32 m0, %2\n\ts_nop 0\n\tglobal_load_lds_dwordx4 %0, %1 offset:-4096 sc1\n\tglobal_load_lds_dwordx4 %0, %1 offset:-3072 sc1\n\t"
                   "global_load_lds_dwordx4 %0, %1 offset:-2048 sc1\n\tglobal_load_lds_dwordx4 %0, %1 offset:-1024 sc1\n\tglobal_load_lds_dwordx4 %0, %1 sc1\n\t"
                   "global_load_lds_dwordx4 %0, %1 offset:1024 sc1\n\tglobal_load_lds_dwordx4 %0, %1 offset:2048 sc1\n\tglobal_load_lds_dwordx4 %0, %1 offset:3072 sc1\n\t"
                   "s_mov_b32 m0, %4\n\ts_nop 0\n\tglobal_load_lds_dwordx4 %0, %3 sc1"
                   :: "v"(ln * 16), "s"(hptr + 4096), "s"(shm_base + HL + wc * 8192 + 4096), "s"(hptr + 8192), "s"(shm_base + HS + wc * 2048) : "memory", "m0");
    };
    if (needh) { await(0xffffu, 0x404u); hload(); }
    if (grp) { __builtin_amdgcn_s_setprio(PRL1); gel2(3, 2 * (kt0 + 5 + he) + wc, 0, 2, 0, true); __builtin_amdgcn_s_setprio(PRL0); }
    else {
      WAIT_V(4);
      peek1();
      aquad(0);
    }
    TS1(6);
    if (!grp || wm > 0) {
      await(0x8000u << vid, 0x8000u << vid);
#pragma unroll
      for (int i = 0; i < 4; ++i) hx[i] = *(const u32x4*)(shm + (vid - wc - 1) * 4096 + i * 1024 + ln * 16);
    } else if (!needh) {
#pragma unroll
      for (int i = 0; i < 4; ++i) hx[i] = u32x4{0u, 0u, 0u, 0u};
    } else {
      bool late = false;
      WAIT_V(2);
      for (;;) {
        const char* const hl = shm + HL + wc * 8192 + ln * 16;
        const u32x4 sp = *(const u32x4*)(shm + HS + wc * 2048 + ln * 16);
        unsigned df = 0;
#pragma unroll
        for (int i = 0; i < 4; ++i) {
          const u32x4 g0 = *(const u32x4*)(hl + i * 2048), g1 = *(const u32x4*)(hl + i * 2048 + 1024);
          df |= (g0[1] ^ sp[0]) | (g0[3] ^ sp[0]) | (g1[1] ^ sp[0]) | (g1[3] ^ sp[0]);
          hx[i] = u32x4{g0[0], g0[2], g1[0], g1[2]};
        }
        if (__builtin_expect(__all((df == 0) & fresh(((u64)sp[1] << 32) | sp[0], t_own)), 1)) break;
        late = __builtin_amdgcn_s_memrealtime() - t_own > TIMEOUT_;
        if (late) break;
        __builtin_amdgcn_s_sleep(1);
        WAIT_L(0);
        hload();
        WAIT_V(0);
      }
      if (__builtin_expect(late, 0)) {
        char* const fb = shm + HL + wc * 8192 + ln * 16;
#pragma unroll 1
        for (int i = 0; i < 32; ++i) {
          const int rec = i >> 3, e = i & 7;
          const int row = m0 - 32 + 2 * (rec < 2 ? li : lq * 4 + (e & 3)) + (rec & 1);
          const int col = (rec < 2 ? D_ + lq * 4 + (e & 3) : 2 * D_ + li) + (ntile + wc) * 32 + (e >> 2) * 16;
          const u16* xr = x + (size_t)row * D_;
          const unsigned stt = row_stats_serial(xr);
          const float mb = lo_f(stt);
          float a = 0.f;
#pragma unroll 1
          for (int k = 0; k < D_; ++k) a += (bf2f(xr[k]) - mb) * bf2f(wqkv[(size_t)k * 3072 + col]);
          *(u16*)(fb + rec * 1024 + e * 2) = f2bf(a / hi_f(stt));
        }
#pragma unroll
        for (int i = 0; i < 4; ++i) hx[i] = *(const u32x4*)(fb + i * 1024);
      }
    }
    tiles(0, &hx[0], &hx[2], grp && mtile == 0 && wm == 0);
    if (grp) { __builtin_amdgcn_s_setprio(PRL1); gel2(3, 2 * (kt0 + 5 + he) + wc, 2, 4, 0, true); }
    else WAIT_V(7);
  }
  TS1(2);
  auto stamp = [&](u64* p) {
    WAIT_V(0);
    u64 now;
    asm volatile("s_memrealtime %0\n\ts_waitcnt lgkmcnt(0)" : "=s"(now) :: "memory");
    if ((threadIdx.x & 63) == 0) __hip_atomic_store(p, now, RLX_AGENT);
  };
  const bool cok = cokw != 0u;
  {
  __builtin_amdgcn_sched_barrier(0);
  int tid = threadIdx.x & 255;
  asm volatile("" : "+v"(tid));
  __builtin_amdgcn_sched_barrier(0);
  const int lane = tid & 63;
  const int wm = (wid >> 1) & 1, wn = wid & 1;
  const int mtile = mt2, ntile = nt2;
  const int m0 = mtile * 128, n0 = ntile * 128;
  TS(0);
  constexpr int EP = 288;
  char* const img = shm + ((ny + 13) % G2_NSTAGE) * G2_STAGE;
  if constexpr (!LD) {
    const int p = wid;
    const u64* const mbox = &done1[bid * 128 + lane];
    auto stage_s = [&]() {
      unsigned t;
      asm volatile(G2_A G2_W : [t] "=&s"(t) : [l] "s"(sl), [oa] "s"((p >> 1) * 8192 + (p & 1) * 2048), [a] "s"(ra2), [va] "v"(va), G2_WOP : "memory", "scc", "m0");
      adv();
    };
    u64 ren = 0;
    auto need = [&]() {
      while (!((okb >> q) & 1)) {
        digest(__hip_atomic_load(mbox, RLX_AGENT));
        if (!((okb >> q) & 1)) {
          __builtin_amdgcn_s_sleep(PSLEEP);
          if (__builtin_expect(!cok, 0)) notify(ren);
          if (GIVEUP()) break;
        }
      }
      asm volatile("" ::: "memory");
    };
    __builtin_amdgcn_s_setprio(0);
    if ((okb & 7) != 7) {
      const unsigned done = (unsigned)okb;
#pragma unroll 1
      for (q = 0; q < 3; ++q)
        if (!((done >> q) & 1)) {
          need();
          aquad(q);
        }
      if (done & 4) WAIT_V(0);
      WAIT_V(4);
    }
    TS(4);
    BARL();
    need();
    stage_y();
    WAIT_V(8);
    peek1();
    if (p == 0) snapshot();
    need();
    stage_y();
    if (p) okb = ~0ull;
    auto check = [&]() {
      if (__builtin_expect(okb != ~0ull, 0)) {
        if (!(q & 1)) {
          peek();
          if (okb != ~0ull) snapshot();
        }
        need();
      }
    };
#pragma unroll 1
    while (q < ny) {
      WAIT_V(16);
      check();
      BAR();
      if (__builtin_expect(q == PD + 1, 0)) {
        ren = 0xffffffff00000000ull;
        if (p == 1) notify((myh & mall) << 32);
      }
      stage_y();
    }
    va = lane * 16 + (p >> 1) * 8192 + role * 4096 + (p & 1) * 2048 + (56 << 14);
    wsetup(wo, D_, F_, lane);
    const u64* const pf = done2 + (bid ^ 5) * 128 + (lane & 3);
#pragma unroll 1
    for (int g = 0; g < 16; ++g) {
      WAIT_V(12);
      check();
      BAR();
      if (__builtin_expect(g == 14 && p == 0, 0)) {
        ++q;
        need();
        --q;
        aload(done2 + (bid ^ 5) * 128, (lane & 3) * 8);
      }
      stage_s();
    }
    WAIT_V(12);
    if (p == 0) {
      u64 f2 = aget();
      while (!__all(fresh(f2, t_own))) {
        if (__builtin_expect(!cok, 0)) { notify(ren); if (lane < 4) __hip_atomic_store(done2 + bid * 128 + lane, __builtin_amdgcn_s_memrealtime(), RLX_AGENT); }
        __builtin_amdgcn_s_sleep(1);
        f2 = __hip_atomic_load(pf, RLX_AGENT);
        if (GIVEUP()) break;
      }
    }
    BAR();
    {
      const char* ps = (const char*)slab + (size_t)(bid ^ 5) * 16384 + p * 4096 + lane * 16;
#pragma unroll
      for (int i = 0; i < 4; ++i) GLDS_SC1(ps + i * 1024, sl + p * 4096 + i * 1024);
    }
    WAIT_V(10);
    BAR();
    WAIT_V(4);
    BAR();
    WAIT_V(0);
    BAR();
    u32x4 xv[4];
#pragma unroll
    for (int i = 0; i < 4; ++i) {
      const int c = i * 256 + tid, r = c >> 4;
      xv[i] = *(const u32x4*)(x + (size_t)(m0 + (r >> 5) * 64 + role * 32 + (r & 31)) * D_ + n0 + (c & 15) * 8);
    }
    BAR();
#pragma unroll
    for (int i = 0; i < 4; ++i) {
      const int c = i * 256 + tid, r = c >> 4;
      const u32x4 tv = *(const u32x4*)(img + ((r & 32) + (r & 1) * 16 + ((r >> 1) & 15)) * EP + (c & 15) * 16);
      u32x4 ov;
#pragma unroll
      for (int e = 0; e < 4; ++e) ov[e] = pack2(lo_f(xv[i][e]) + lo_f(tv[e]), hi_f(xv[i][e]) + hi_f(tv[e]));
      __builtin_nontemporal_store(ov, (u32x4*)(out + (size_t)(m0 + (r >> 5) * 64 + role * 32 + (r & 31)) * D_ + n0 + (c & 15) * 8));
    }
    TS(5);
    if (tid == 0) __hip_atomic_store(&acks[bid], __builtin_amdgcn_s_memrealtime(), RLX_AGENT);
    if (__builtin_expect(!cok, 0) && p == 0) {
      const bool mine = lane <= 32;
      const u64* const pa = &acks[lane < 32 ? client(lane) : bid ^ 5];
      while (!__all(!mine || fresh(__hip_atomic_load(pa, RLX_AGENT), t_own))) {
        notify(ren);
        if (lane < 4) __hip_atomic_store(done2 + bid * 128 + lane, __builtin_amdgcn_s_memrealtime(), RLX_AGENT);
        __builtin_amdgcn_s_sleep(32);
        if (GIVEUP()) break;
      }
    }
  } else {
    TS(6);
    f32x4 acc[4][4], sacc[2][4];
    const int fr = lane & 15, fq = lane >> 4, q4 = fr >> 2, pp = lane & 3;
    const int ff = ((q4 >> 1) & 1) | ((fq & 1) << 1);
    int aof = G2_STAGE + wm * 8192 + lane * 16;
    int boff[4];
#pragma unroll
    for (int j = 0; j < 4; ++j) boff[j] = G2_STAGE + 16384 + wn * 8192 + (8 * fq + q4) * 128 + ((j ^ ff) * 32) + pp * 8;
#pragma unroll
    for (int b = 0; b < 4; ++b) {
#pragma unroll
      for (int a = 0; a < 4; ++a) acc[a][b] = f32x4{0.f, 0.f, 0.f, 0.f};
      sacc[0][b] = f32x4{0.f, 0.f, 0.f, 0.f}; sacc[1][b] = f32x4{0.f, 0.f, 0.f, 0.f};
    }
#define SB() __builtin_amdgcn_sched_barrier(0)
    struct Fr { bf16x8 a[4]; bf16x4 bl[4], bh[4]; };
    Fr f0[2], f1[2];
    auto rd = [&](Fr& f, lds_cptr sb, int ks, int t, bool sp) {
      constexpr int ord[12] = {0, 1, 8, 2, 3, 4, 5, 6, 7, 9, 10, 11};
      const int o = ord[t];
      if (o >= 8) f.a[o - 8] = *(const __attribute__((address_space(3))) bf16x8*)(sb + aof + (o - 8) * 2048 + ks * 1024);
      else if (o & 1) f.bh[o >> 1] = tr_read(sb + boff[o >> 1] + ks * 4096 + 512);
      else f.bl[o >> 1] = tr_read(sb + boff[o >> 1] + ks * 4096);
    };
    auto bfr = [&](const Fr& c, int j) { return __builtin_shufflevector(c.bl[j], c.bh[j], 0, 1, 2, 3, 4, 5, 6, 7); };
    auto ystg = [&](const Fr* c, Fr* n, int kind) {
      lds_cptr sb = shm3;
#pragma unroll
      for (int t = 0; t < 16; ++t) {
        const int mf = (t >> 2) ^ kind, j = (t & 4) ? 3 - (t & 3) : t & 3, ks = t & 1;
        SB();
        asm volatile("v_mfma_f32_16x16x32_bf16 %0, %1, %2, %0\n\tv_mfma_f32_16x16x32_bf16 %0, %3, %4, %0"
                     : "+v"(acc[mf][j]) : "v"(bfr(c[ks], j)), "v"(c[ks].a[mf]), "v"(bfr(c[ks ^ 1], j)), "v"(c[ks ^ 1].a[mf]));
        SB();
        const int h = kind == 2 ? 5 : 6;
        if (t < 2 * h) { rd(n[t / h], sb, t / h, 2 * (t % h), true); rd(n[t / h], sb, t / h, 2 * (t % h) + 1, true); }
      }
      SB();
      __builtin_amdgcn_s_waitcnt(0xC07F);
    };
    auto rds = [&](Fr* n, lds_cptr sb, int t) {
      if (t < 10) { rd(n[t / 5], sb, t / 5, 2 * (t % 5), true); rd(n[t / 5], sb, t / 5, 2 * (t % 5) + 1, true); }
    };
    auto stg = [&](const Fr* c, Fr* n, bool load = true) {
      lds_cptr sb = shm3;
#pragma unroll
      for (int t = 0; t < 16; ++t) {
        const int ks = t >> 3, mf = (t >> 2) & 1, j = (t & 4) ? 3 - (t & 3) : t & 3;
        SB();
        sacc[mf][j] = __builtin_amdgcn_mfma_f32_16x16x32_bf16(bfr(c[ks], j), c[ks].a[mf], sacc[mf][j], 0, 0, 0);
        SB();
        if (load) rds(n, sb, t);
      }
      SB();
    };
    asm volatile("" : "+v"(aof), "+v"(boff[0]), "+v"(boff[1]), "+v"(boff[2]), "+v"(boff[3]));
    asm volatile("" : "+v"(sacc[0][0]), "+v"(sacc[0][1]), "+v"(sacc[0][2]), "+v"(sacc[0][3]), "+v"(sacc[1][0]), "+v"(sacc[1][1]), "+v"(sacc[1][2]), "+v"(sacc[1][3]));
#pragma unroll
    for (int a = 0; a < 4; ++a) asm volatile("" : "+v"(acc[a][0]), "+v"(acc[a][1]), "+v"(acc[a][2]), "+v"(acc[a][3]));
    WAIT_V(0);
    arrive(std::integral_constant<int, 1>{});
    BARL();
    TS(7);
    __builtin_amdgcn_s_waitcnt(0xC07F);
    SB();
#pragma unroll
    for (int t = 0; t < 24; ++t) rd(f0[t & 1], shm3, t & 1, t >> 1, false);
    int so = G2_STAGE;
    auto nxt = [&]() {
      const int d = so == (G2_NSTAGE - 1) * G2_STAGE ? -(G2_NSTAGE - 1) * G2_STAGE : G2_STAGE;
      so += d; aof += d; boff[0] += d; boff[1] += d; boff[2] += d; boff[3] += d;
      asm volatile("" : "+v"(aof), "+v"(boff[0]), "+v"(boff[1]), "+v"(boff[2]), "+v"(boff[3]));
    };
    nxt();
    __builtin_amdgcn_s_waitcnt(0xC07F);
#pragma unroll 1
    for (int g = 0; g < ny - 2; g += 2) {
      ystg(f0, f1, 0);
      nxt();
      BAR();
      ystg(f1, f0, 0);
      nxt();
      BAR();
    }
    if (!(DLT & 1)) { ystg(f0, f1, 0); nxt(); BAR(); }
    Fr sa[2], sb2[2];
    ystg((DLT & 1) ? f0 : f1, sa, 2);
    asm volatile("" : "+v"(acc[0][0]), "+v"(acc[0][1]), "+v"(acc[0][2]), "+v"(acc[0][3]), "+v"(acc[1][0]), "+v"(acc[1][1]), "+v"(acc[1][2]), "+v"(acc[1][3]));
    TS(2);
    {
      const __amdgpu_buffer_rsrc_t rs = __builtin_amdgcn_make_buffer_rsrc((void*)((char*)slab + (size_t)bid * 16384), 0, 16384, 0x00020000);
#pragma unroll
      for (int f = 0; f < 4; ++f) {
        const f32x4 a = acc[2 + (f >> 1)][(f & 1) * 2], b = acc[2 + (f >> 1)][(f & 1) * 2 + 1];
        __builtin_amdgcn_raw_buffer_store_b128(u32x4{pack2(a[0], a[1]), pack2(a[2], a[3]), pack2(b[0], b[1]), pack2(b[2], b[3])}, rs,
                                               (f * 256 + tid) * 16, 0, 16);
      }
    }
    SB();
#pragma unroll 1
    for (int i = 0; i < 7; ++i) {
      nxt();
      BAR();
      stg(sa, sb2);
      nxt();
      BAR();
      stg(sb2, sa);
      if (i == 2) {
        stamp(done2 + bid * 128 + (wid & 3));
      }
    }
    nxt();
    BAR();
    stg(sa, sb2);
    BAR();
    u32x4 pv[4];
    {
      nxt();
      lds_cptr sb = shm3 + so + tid * 16;
#pragma unroll
      for (int f = 0; f < 4; ++f) pv[f] = *(const __attribute__((address_space(3))) u32x4*)(sb + f * 4096);
    }
    stg(sb2, sa, false);
    TS(1);
#pragma unroll
    for (int mf = 0; mf < 2; ++mf)
#pragma unroll
      for (int j = 0; j < 4; ++j) {
        const u32x4 pw = pv[mf * 2 + (j >> 1)];
        const f32x4 sv = sacc[mf][j];
        const f32x4 ya = acc[mf][j] + f32x4{lo_f(pw[2 * (j & 1)]), hi_f(pw[2 * (j & 1)]), lo_f(pw[2 * (j & 1) + 1]), hi_f(pw[2 * (j & 1) + 1])};
        u32x2 o;
#pragma unroll
        for (int e = 0; e < 2; ++e) {
          const f32x2 y2 = {ya[2 * e], ya[2 * e + 1]}, s2 = {sv[2 * e], sv[2 * e + 1]};
          const f32x2 ar = y2 * f32x2{-1.4426950408889634f, -1.4426950408889634f};
          const f32x2 dn = f32x2{__builtin_amdgcn_exp2f(ar[0]), __builtin_amdgcn_exp2f(ar[1])} + f32x2{1.0f, 1.0f};
          const f32x2 t = (s2 * y2) * f32x2{__builtin_amdgcn_rcpf(dn[0]), __builtin_amdgcn_rcpf(dn[1])};
          o[e] = pack2(t[0], t[1]);
        }
        *(u32x2*)(img + (wm * 32 + mf * 16 + fr) * EP + (wn * 64 + j * 16 + fq * 4) * 2) = o;
      }
    BARL();
    TS(3);
  }
  TSFLUSH();
  }
  };
  if (wid < 4) kloop(std::integral_constant<int, 0>{});
  else kloop(std::integral_constant<int, 1>{});
  asm volatile(".Lcode_end:");
}

extern "C" void kernel_launch(void* const* d_in, const int* in_sizes, int n_in, void* d_out, int out_size, void* d_ws,
                              size_t ws_size, hipStream_t stream) {
  const u16* A_x = (const u16*)d_in[0];
  const u16* A_wqkv = (const u16*)d_in[1];
  const u16* A_wo = (const u16*)d_in[2];
  const u16* B_wg = (const u16*)d_in[4];
  const u16* B_wu = (const u16*)d_in[5];
  const u16* B_wd = (const u16*)d_in[6];
  u16* out = (u16*)d_out;
  fused_kernel<<<256, 512, 0, stream>>>(A_x, A_wqkv, B_wg, B_wu, B_wd, A_wo, out, (char*)d_ws);
}
```

```cpp
#include <hip/hip_runtime.h>
#include <hip/hip_bf16.h>
#include <stdint.h>
#include <type_traits>
#include <utility>

typedef unsigned short u16;
using bf16x8 = __attribute__((ext_vector_type(8))) short;
using bf16x4 = __attribute__((ext_vector_type(4))) short;
using f32x4 = __attribute__((ext_vector_type(4))) float;
using u32x2 = __attribute__((ext_vector_type(2))) unsigned;
using u32x4 = __attribute__((ext_vector_type(4))) unsigned;
typedef __attribute__((address_space(3))) const char* lds_cptr;

template <class F, int... I> __device__ __forceinline__ void sfor_(F&& f, std::integer_sequence<int, I...>) { (f(std::integral_constant<int, I>{}), ...); }
template <int N, class F> __device__ __forceinline__ void sfor(F&& f) { sfor_(f, std::make_integer_sequence<int, N>{}); }

constexpr int S_ = 2048, D_ = 1024, F_ = 3584;

__device__ __forceinline__ float bf2f(u16 h) { return __uint_as_float((unsigned)h << 16); }
__device__ __forceinline__ u16 f2bf(float f) {
  unsigned u = __float_as_uint(f);
  u += 0x7fffu + ((u >> 16) & 1u);
  return (u16)(u >> 16);
}
__device__ __forceinline__ float rbf(float f) { return bf2f(f2bf(f)); }
typedef __attribute__((ext_vector_type(2))) __bf16 bf16x2_t;
typedef __attribute__((ext_vector_type(2))) float f32x2;
__device__ __forceinline__ unsigned pack2(float a, float b) {
  bf16x2_t r = __builtin_convertvector((f32x2){a, b}, bf16x2_t);
  return __builtin_bit_cast(unsigned, r);
}
__device__ __forceinline__ float lo_f(unsigned p) { return __uint_as_float(p << 16); }
__device__ __forceinline__ float hi_f(unsigned p) { return __uint_as_float(p & 0xffff0000u); }

#define TSDECL
#define TSS(i) do {} while (0)
#define TSFLUSH() do {} while (0)
#define TS1(i) TSS(i)
#define TS(i) TSS(8 + (i))
#define WAIT_V(n) asm volatile("s_waitcnt vmcnt(%0)" ::"n"(n) : "memory")
#define WAIT_L(n) asm volatile("s_waitcnt lgkmcnt(%0)" ::"n"(n) : "memory")
#define BARL() do { WAIT_L(0); BAR(); } while (0)
#define BAR() do { asm volatile("" ::: "memory"); __builtin_amdgcn_s_barrier(); asm volatile("" ::: "memory"); } while (0)

__device__ __forceinline__ bf16x4 tr_read(lds_cptr p) {
  return __builtin_amdgcn_ds_read_tr16_b64_v4i16((__attribute__((address_space(3))) bf16x4*)p);
}
__device__ __forceinline__ bf16x8 tr_frag(lds_cptr p) {
  bf16x4 lo = tr_read(p), hi = tr_read(p + 512);
  bf16x8 r;
  r[0] = lo[0]; r[1] = lo[1]; r[2] = lo[2]; r[3] = lo[3];
  r[4] = hi[0]; r[5] = hi[1]; r[6] = hi[2]; r[7] = hi[3];
  return r;
}
__device__ __forceinline__ void glds16(const void* gsrc, unsigned lds_dst) {
  unsigned keep;
  asm volatile("s_mov_b32 %0, m0\n\ts_mov_b32 m0, %2\n\ts_nop 0\n\tglobal_load_lds_dwordx4 %1, off\n\ts_mov_b32 m0, %0"
               : "=&s"(keep) : "v"(gsrc), "s"(lds_dst) : "memory");
}
#define GLDS(gptr, laddr) glds16((const void*)(gptr), (unsigned)(laddr))
__device__ __forceinline__ void glds16_sc1(const void* gsrc, unsigned lds_dst) {
  unsigned keep;
  asm volatile("s_mov_b32 %0, m0\n\ts_mov_b32 m0, %2\n\ts_nop 0\n\tglobal_load_lds_dwordx4 %1, off sc1\n\ts_mov_b32 m0, %0"
               : "=&s"(keep) : "v"(gsrc), "s"(lds_dst) : "memory");
}
#define GLDS_SC1(gptr, laddr) glds16_sc1((const void*)(gptr), (unsigned)(laddr))
__device__ __forceinline__ void glds16_nc(const void* gsrc, unsigned lds_dst) {
  unsigned keep;
  asm volatile("s_mov_b32 %0, m0\n\ts_mov_b32 m0, %2\n\ts_nop 0\n\tglobal_load_lds_dwordx4 %1, off\n\ts_mov_b32 m0, %0"
               : "=&s"(keep) : "v"(gsrc), "s"(lds_dst));
}
#define GLDS_NC(gptr, laddr) glds16_nc((const void*)(gptr), (unsigned)(laddr))
__device__ __forceinline__ void glds16_s(const void* sbase, unsigned voff, unsigned lds_dst) {
  asm volatile("s_mov_b32 m0, %2\n\ts_nop 0\n\tglobal_load_lds_dwordx4 %0, %1" :: "v"(voff), "s"(sbase), "s"(lds_dst) : "m0");
}

__device__ __forceinline__ void glds16_p(const void* sbase, unsigned voff, unsigned lds_dst) {
  asm volatile("s_mov_b32 m0, %2\n\ts_nop 3\n\tglobal_load_lds_dwordx4 %0, %1" :: "v"(voff), "s"(sbase), "s"(lds_dst) : "m0");
}

typedef unsigned long long u64;
__device__ __forceinline__ bool fresh(u64 stamp, u64 t_own) {
  return stamp - t_own - 100ull < __builtin_amdgcn_s_memrealtime() - t_own;
}
constexpr u64 TIMEOUT_ = 200000;
#define GIVEUP() (__builtin_amdgcn_s_memrealtime() - t_own > 100000000ull)
__device__ __forceinline__ float half_sum(float v) {
  v += __builtin_bit_cast(float, __builtin_amdgcn_update_dpp(0, __builtin_bit_cast(int, v), 0xB1, 0xf, 0xf, false));
  v += __builtin_bit_cast(float, __builtin_amdgcn_update_dpp(0, __builtin_bit_cast(int, v), 0x4E, 0xf, 0xf, false));
  v += __builtin_bit_cast(float, __builtin_amdgcn_update_dpp(0, __builtin_bit_cast(int, v), 0x141, 0xf, 0xf, false));
  v += __builtin_bit_cast(float, __builtin_amdgcn_update_dpp(0, __builtin_bit_cast(int, v), 0x140, 0xf, 0xf, false));
  { auto r = __builtin_amdgcn_permlane16_swap(__float_as_uint(v), __float_as_uint(v), false, false); v = __uint_as_float(r[0]) + __uint_as_float(r[1]); }
  return v;
}
__device__ __forceinline__ unsigned pack_stats(float mean, float var) {
  return (unsigned)f2bf(mean) | ((unsigned)f2bf(sqrtf(rbf(rbf(var) + 1e-5f))) << 16);
}
__device__ __forceinline__ unsigned row_stats_serial(const u16* xr) {
  float s = 0.f, q = 0.f;
#pragma unroll 1
  for (int k = 0; k < D_; ++k) s += bf2f(xr[k]);
  const float mean = s * (1.0f / D_);
#pragma unroll 1
  for (int k = 0; k < D_; ++k) { const float d = bf2f(xr[k]) - mean; q += d * d; }
  return pack_stats(mean, q * (1.0f / D_));
}
constexpr int G1_STAGE = 32768 + 5 * 8192;
#define PF_K0 6
#define CP_K0 3
#define DLT 1
#define PSLEEP 2
#define SD 6
#define NSC 2
#define PRB 0
#define PRL0 3
#define PRL1 1
#define NWPF 4
#define SNP 3
#define RD0 21
#define NP1 8
#define ESH 1
#define EROW 0
#define KS0 2
#define PADE 0
#define PADL 2
#define STI 2
#define PUB_K 5

#define RLX_AGENT __ATOMIC_RELAXED, __HIP_MEMORY_SCOPE_AGENT

constexpr int G2_STAGE = 16384 + 2 * 8192;
constexpr int G2_NSTAGE = 5;
constexpr size_t MB_ = 1024 * 1024;
__global__ __launch_bounds__(512, 2) __attribute__((amdgpu_num_vgpr(127))) void fused_kernel(const u16* __restrict__ x, const u16* __restrict__ wqkv,
                                                       const u16* __restrict__ wg, const u16* __restrict__ wu,
                                                       const u16* __restrict__ wd, const u16* __restrict__ wo,
                                                       u16* __restrict__ out, char* __restrict__ ws) {
  u16* const halo = (u16*)(ws + 4 * MB_);
  u16* const abuf = (u16*)(ws + 16 * MB_);
  char* const slab = ws + 64 * MB_;
  unsigned* const flags = (unsigned*)(ws + 96 * MB_);
  u32x4* const rstat = (u32x4*)(ws + 97 * MB_);
  char* const wcopy = ws + 104 * MB_;
  u64* const done1 = (u64*)(ws + 102 * MB_ + 512);
  u64* const done2 = (u64*)(ws + 102 * MB_);
  u64* const donec = (u64*)(ws + 100 * MB_ + 196608);
  u64* const acks = (u64*)(ws + 100 * MB_ + 262144);
  u64* const cstamp = (u64*)(ws + 100 * MB_ + 327680);
  float* const csum = (float*)(ws + 101 * MB_);
  __shared__ __attribute__((aligned(1024))) char shm[163840];
  constexpr int AUX = 2 * G1_STAGE + 8192;
  {
    u64 pc; int len;
    asm volatile("s_getpc_b64 %0\n.Lcode_beg:\n\ts_mov_b32 %1, .Lcode_end-.Lcode_beg" : "=s"(pc), "=s"(len));
    const int ol = ((blockIdx.x >> 3) + 32 * (threadIdx.x >> 6)) * 1024 + (threadIdx.x & 63) * 16;
    asm volatile("s_mov_b32 m0, %2\n\ts_nop 0\n\tglobal_load_lds_dwordx4 %0, %1"
                 :: "v"(ol < len - 16 ? ol : 0), "s"(pc & ~15ull), "s"((unsigned)(uintptr_t)(lds_cptr)shm + 2 * G1_STAGE + 7168) : "m0");
    __builtin_amdgcn_sched_barrier(0);
  }
  const u64 t_own = __builtin_amdgcn_s_memrealtime();
  __builtin_amdgcn_sched_barrier(0);
  TSDECL;
  const int tid = threadIdx.x, lane = tid & 63;
  const int wid = __builtin_amdgcn_readfirstlane(tid >> 6);
  const int bid = blockIdx.x, xcd = bid & 7, idx = bid >> 3;
  const int ntile = xcd * 4 + (idx & 3), mtile = idx >> 2;
  const int m0 = mtile * 256;
  const int hg = ntile >> 1;
  const bool tya = (ntile & 1) == 0;
  const int grp = wid >> 2, wc = (wid >> 1) & 1, wm = (((grp ^ ntile ^ 1) & 1) << 1) | (wid & 1), vid = wc * 4 + wm;
  const int srow = m0 + ntile * 8 + (wid & 3) * 2;
  lds_cptr shm3 = (lds_cptr)shm;
  const unsigned shm_base = (unsigned)(uintptr_t)shm3;
  const int r8 = tid >> 3, pos = tid & 7;
  const int phys = pos >> 1, half = pos & 1;
  const int fk = ((r8 >> 1) & 1) | (((r8 >> 3) & 1) << 1);
  const int vsrc = phys ^ fk;
  const int qstr = tya ? 3072 * 2 : F_ * 2;
  unsigned voa = (((r8 & 32) + 2 * (r8 & 15) + ((r8 >> 4) & 1)) * D_ + ((pos ^ (r8 & 7)) * 8)) * 2;
  unsigned vog = r8 * (F_ * 2) + vsrc * 32 + half * 16, voq = r8 * qstr + vsrc * 32 + half * 16;
  unsigned vom = r8 * qstr + (vsrc >> (tya ? 0 : 1)) * 32 + half * 16;
  asm volatile("" : "+v"(voa), "+v"(vog), "+v"(voq), "+v"(vom));
  asm volatile("" :: "s"(x), "s"(wqkv), "s"(wg), "s"(wu), "s"(wd), "s"(wo), "s"(out), "s"(ws));
  const char* const ab = (const char*)(x + (size_t)m0 * D_);
  int kz = 0;
  asm volatile("" : "+s"(kz));
  auto stage_a = [&](int buf, int kt, int i, int h) {
    glds16_s(ab + (long)(((i * 64 + (h - 1) * 32) * D_ + kt * 64) * 2), voa, shm_base + buf * G1_STAGE + (wid - 4 + 4 * h) * 1024 + i * 8192);
  };
#pragma unroll
  for (int i = 0; i < 4; ++i) stage_a(0, 0, i, 1);
  const int kt0 = (hg >> 1) * 7, he = hg & 1;
  const char* const gb = (const char*)(wg + (kt0 + (tya ? 5 + he : 2 + he)) * 64);
  const char* const qb = tya ? (const char*)(wqkv + 64 * hg) : gb - 256;
  const long dgu = (const char*)wu - (const char*)wg, dp1 = tya ? 2048 : dgu;
  const char* const mb0 = tya ? qb + 4096 : (const char*)(wg + (kt0 + 4) * 64 + 32 * he);
  const char* mptr = mb0 + (((vsrc & 1) && !tya) ? dgu : 0) + vom;
  auto wload = [&](int p, int rows, unsigned dst) {
    long off = (long)(rows * (p < 3 ? qstr : F_ * 2)) + (p == 1 ? dp1 : (p == 4 ? dgu : 0));
    if (p == 2) { asm volatile("" : "+s"(off)); GLDS_NC(mptr + off, dst); }
    else glds16_s((p < 2 ? qb : gb) + off, p < 2 ? voq : vog, dst);
  };
#pragma unroll
  for (int i = 0; i < 5; ++i) wload(i, 0, shm_base + wid * 1024 + 32768 + i * 8192);
  __builtin_amdgcn_sched_barrier(0);
  const int fr = lane & 15, fq = lane >> 4, q4 = fr >> 2, pp = lane & 3;
  const int ff = ((q4 >> 1) & 1) | ((fq & 1) << 1);
  int aoff[2], boff[2];
#pragma unroll
  for (int ks = 0; ks < 2; ++ks) aoff[ks] = (wm * 64 + fr) * 128 + (((ks * 4 + fq) ^ (fr & 7)) * 16);
#pragma unroll
  for (int j = 0; j < 2; ++j) boff[j] = 32768 + (8 * fq + q4) * 128 + (((2 * wc + j) ^ ff) * 32) + pp * 8;

  f32x4 acc[4][10];
#pragma unroll
  for (int a = 0; a < 4; ++a)
#pragma unroll
    for (int b = 0; b < 10; ++b) acc[a][b] = f32x4{0.f, 0.f, 0.f, 0.f};

  auto stage_part = [&](int buf, int kt, int i, int h) {
    if (i < 5) wload(i, kt * 64 + (h - 1) * 32, shm_base + buf * G1_STAGE + (wid - 4 + 4 * h) * 1024 + 32768 + i * 8192);
    else stage_a(buf, kt, i - 5, h);
  };
  constexpr int NT = D_ / 64;
  constexpr int DMP = 2 * G1_STAGE;
  constexpr int CF = AUX + 4096, QS = CF + 1280, CNT = CF + 3840;
  bf16x8 af0[4], af1[4];
  bf16x4 bl[10], bh[10];
#pragma unroll
  for (int i = 0; i < 4; ++i) { af1[i] = bf16x8{0, 0, 0, 0, 0, 0, 0, 0}; bl[i] = bf16x4{0, 0, 0, 0}; bh[i] = bf16x4{0, 0, 0, 0}; }
#define SB0() __builtin_amdgcn_sched_barrier(0)
  auto mm = [&](int n, int f, int ks, int mf) {
    const bf16x8 b = __builtin_shufflevector(bl[n % 10], bh[n % 10], 0, 1, 2, 3, 4, 5, 6, 7);
    if ((f >> 1) == 2) acc[mf][f] = __builtin_amdgcn_mfma_f32_16x16x32_bf16(ks ? af1[mf] : af0[mf], b, acc[mf][f], 0, 0, 0);
    else acc[mf][f] = __builtin_amdgcn_mfma_f32_16x16x32_bf16(b, ks ? af1[mf] : af0[mf], acc[mf][f], 0, 0, 0);
  };
  auto correct = [&]() {
    int l = threadIdx.x & 63;
    asm volatile("s_nop 7" : "+v"(l));
    const float* const tb = (const float*)(shm + DMP + wm * 2048);
    const float* const cf = (const float*)(shm + CF) + wc * 160;
    auto ld = [&](int f) { return f < 8 ? *(const f32x4*)(cf + (f < 4 ? f : f + 2) * 16 + (l >> 4) * 4) : *(const f32x4*)(tb + (f - 8) * 16 + (l >> 4) * 4); };
    float m[4];
    f32x4 cr[3];
#pragma unroll
    for (int mf = 0; mf < 4; ++mf) m[mf] = tb[mf * 16 + (l & 15)];
    cr[0] = ld(0);
    cr[1] = ld(1);
    const float c4 = cf[64 + (l & 15)], c5 = cf[80 + (l & 15)];
#pragma unroll
    for (int f = 0; f < 12; ++f) {
      if (f + 2 < 12) cr[(f + 2) % 3] = ld(f + 2);
      SB0();
      const f32x4 c = cr[f % 3];
#pragma unroll
      for (int i = 0; i < (f < 8 ? 4 : 2); ++i) {
        f32x4& a = f < 8 ? acc[i][f < 4 ? f : f + 2] : acc[f - 8][4 + i];
        float a0 = a[0], a1 = a[1], a2 = a[2], a3 = a[3];
        asm volatile("v_fmac_f32 %0, %4, %5\n\tv_fmac_f32 %1, %4, %6\n\tv_fmac_f32 %2, %4, %7\n\tv_fmac_f32 %3, %4, %8"
                     : "+v"(a0), "+v"(a1), "+v"(a2), "+v"(a3) : "v"(f < 8 ? m[i] : (i ? c5 : c4)), "v"(c[0]), "v"(c[1]), "v"(c[2]), "v"(c[3]));
        a = f32x4{a0, a1, a2, a3};
      }
      SB0();
    }
    asm volatile("s_nop 1");
  };
  auto kloop = [&](auto wmc) __attribute__((always_inline)) {
    constexpr bool LD = decltype(wmc)::value != 0;
    constexpr int grp = LD ? 1 : 0;
    const int WID = wid;
    const char* pa = nullptr;
    unsigned vob = 0;
    f32x4 ca = {0.f, 0.f, 0.f, 0.f}, cb = {0.f, 0.f, 0.f, 0.f};
    bf16x8 sel = {0, 0, 0, 0, 0, 0, 0, 0};
    int tro = 0;
    u64 tg = 0;
    const int psh = (wid == 2 && !tya) ? 1 : 0, pstr = wid < 3 ? qstr : F_ * 2;
    const char* const pbase = wid == 0 ? qb : (wid == 1 ? qb + dp1 : (wid == 2 ? mb0 : gb));
    if (!LD) {
      const int rs = mtile * 8 + (lane >> 3), vs = ((lane & 7) >> 1) ^ (((rs >> 1) & 1) | ((mtile & 1) << 1));
      pa = pbase + ((psh & vs) ? dgu : 0) + (rs * pstr + (vs >> psh) * 32 + (lane & 1) * 16);
      vob = rs * (F_ * 2) + vs * 32 + (lane & 1) * 16;
      const short one = fr == fq ? 0x3f80 : 0;
      sel = bf16x8{one, one, one, one, one, one, one, one};
      tro = q4 * 128 + ((fq ^ ((q4 >> 1) | ((mtile & 1) << 1))) * 32) + pp * 8;
      asm volatile("" : "+v"(sel), "+v"(tro), "+v"(ca), "+v"(cb));
    }
    auto share = [&](int t, unsigned dsta, unsigned dstb) {
      long off = (long)(t * 64 * pstr);
      asm volatile("" : "+s"(off));
      GLDS_NC(pa + off, shm_base + dsta);
      if (wid == 0) glds16_p(gb + dgu + (long)(t * 64 * F_ * 2), vob, shm_base + dstb);
    };
    auto accum = [&](int oa, int ob) {
      ca = __builtin_amdgcn_mfma_f32_16x16x32_bf16(tr_frag(shm3 + tro + oa), sel, ca, 0, 0, 0);
      if (wid == 0) cb = __builtin_amdgcn_mfma_f32_16x16x32_bf16(tr_frag(shm3 + tro + ob), sel, cb, 0, 0, 0);
    };
    auto cgather = [&]() {
      int l = threadIdx.x & 63;
      asm volatile("s_memrealtime %0\n\ts_waitcnt lgkmcnt(0)" : "=s"(tg), "+v"(l) :: "memory");
      const char* const p4 = l < 32 ? (const char*)(csum + (ntile * 5 + 4) * 512 + wid * 16) + (l >> 2) * 256
                                    : (const char*)(cstamp + (ntile * 4 + (l < 36 ? wid : 0)) * 8);
      if (l < 40) GLDS_SC1(p4 + (l & 3) * 16, shm_base + QS + wid * 640);
#pragma unroll
      for (int i = 0; i < 2; ++i) GLDS_SC1((const char*)(csum + (ntile * 5 + wid) * 512) + i * 1024 + l * 16, shm_base + DMP + wid * 2048 + i * 1024);
    };
    if (!LD) {
#pragma unroll
      for (int i = 0; i < ESH; ++i) share(2 + i, DMP + 7168, DMP + 7168);
      if (EROW) {
#pragma unroll
        for (int i = 0; i < 2; ++i) glds16_p(x + (size_t)(srow + i) * D_, lane * 16, shm_base + AUX + wid * 2048 + i * 1024);
      }
    }
    if (LD) {
#pragma unroll
      for (int i = 0; i < NP1; ++i) {
        stage_part(1, kz + 1, i >> 1, i & 1);
        asm volatile("" : "+v"(acc[0][i]), "+v"(acc[1][i]), "+v"(acc[2][i]), "+v"(acc[3][i]));
      }
    }
#pragma unroll
    for (int a = 0; a < 4; ++a)
      asm volatile("" : "+v"(acc[a][0]), "+v"(acc[a][1]), "+v"(acc[a][2]), "+v"(acc[a][3]), "+v"(acc[a][4]), "+v"(acc[a][5]), "+v"(acc[a][6]),
                   "+v"(acc[a][7]), "+v"(acc[a][8]), "+v"(acc[a][9]));
    asm volatile("" : "+v"(aoff[0]), "+v"(aoff[1]), "+v"(boff[0]), "+v"(boff[1]));
    TS1(3);
    if constexpr (LD) {
      TS1(4);
      WAIT_V(NP1);
      BAR();
    } else {
      TS1(4);
      if (wid) WAIT_V(ESH + 2 * EROW);
      WAIT_V(2 * ESH + 2 * EROW);
    }
    TS1(0);
#pragma clang loop unroll(disable)
    for (int kt = 0; kt < NT; ++kt) {
      int cur = kt & 1;
      if (!LD && kt == NT - 1) { correct(); __builtin_amdgcn_s_setprio(2); }
      if (!LD) {
        asm volatile("" : "+s"(cur));
        __builtin_amdgcn_s_waitcnt(0xC07F);
        asm volatile(".p2align 5\n\t.fill %0, 4, 0xBF800000" :: "n"(PADE));
        BAR();
      }
      lds_cptr sb = shm3;
      if (LD) {
        unsigned k2 = kt - 1;
        asm volatile("" : "+s"(k2));
        if (k2 < NT - 2) {
#pragma unroll
          for (int i = 0; i < NP1; ++i) stage_part(cur ^ 1, kt + 1, i >> 1, i & 1);
        }
      }
      if (LD && kt + 1 < NT) {
#pragma unroll
        for (int i = NP1; i < 18; ++i) stage_part(cur ^ 1, kt + 1, i >> 1, i & 1);
      } else if (LD) {
        correct();
      }
      auto fadr = [&](int n) {
        const int f = n < 2 ? 8 + n : (n < 4 ? 11 - n : (n - 4) >> 1), ks = n < 2 ? 1 : (n < 4 ? 0 : n & 1);
        return sb + boff[f & 1] + (f >> 1) * 8192 + ks * 4096;
      };
      auto rdl = [&](int n) { bl[n % 10] = tr_read(fadr(n % 20)); };
      auto rdh = [&](int n) { bh[n % 10] = tr_read(fadr(n % 20) + 512); };
      auto rda = [&](bf16x8* af, int ks, int mf) { af[mf] = *(const __attribute__((address_space(3))) bf16x8*)(sb + aoff[ks] + mf * 2048); };
      SB0();
      if (LD) __builtin_amdgcn_s_waitcnt(0xC07F);
      if (LD) asm volatile(".p2align 5\n\t.fill %0, 4, 0xBF800000" :: "n"(PADL));
      sfor<80>([&](auto uc) __attribute__((always_inline)) {
        constexpr int u = decltype(uc)::value;
        SB0();
        if constexpr (u < 16) {
          const int n = u >> 2;
          mm(n, n < 2 ? 8 + n : 11 - n, n < 2 ? 1 : 0, (n & 1) ? 3 - (u & 3) : u & 3);
        } else {
          const int f = (u - 16) >> 3, i = (u >> 1) & 3, ks = (u ^ i) & 1;
          if ((u & 7) == 0) { asm volatile("" :: "v"(bl[(5 + 2 * f) % 10]), "v"(bh[(5 + 2 * f) % 10])); SB0(); }
          mm(4 + 2 * f + ks, f, ks, (f & 1) ? 3 - i : i);
        }
        SB0();
        if (u == 0) { rdl(2); rdh(2); }
        if (u == 1) { rda(af0, 0, 0); rda(af0, 0, 1); }
        if (u == 2) { rda(af0, 0, 2); rda(af0, 0, 3); }
        if (u == 3) { rdl(3); rdh(3); }
        if constexpr (u >= 4 && u < 8) rda(af1, 1, 7 - u);
        if (u >= 8 && u < 16) { if (u & 1) rdh(u >> 1); else rdl(u >> 1); }
        if (u >= RD0 && u < RD0 + 55 && (u & 1)) {
          const int n = 8 + ((u - RD0) >> 2);
          if ((u - RD0) & 2) rdh(n); else rdl(n);
        }
      });
      SB0();
      if (LD)
        asm volatile("s_cmp_eq_u32 %0, %1\n\ts_waitcnt vmcnt(0) lgkmcnt(0)\n\ts_cbranch_scc1 1f\n\ts_barrier\n1:" :: "s"(kt), "n"(NT - 1) : "memory", "scc");
      SB0();
      {
        const int d = cur ? -G1_STAGE : G1_STAGE;
        aoff[0] += d; aoff[1] += d; boff[0] += d; boff[1] += d;
        asm volatile("" : "+v"(aoff[0]), "+v"(aoff[1]), "+v"(boff[0]), "+v"(boff[1]));
      }
      if (!LD && kt <= 19 - SD) {
        int lane = threadIdx.x & 63;
        asm volatile("" : "+v"(lane));
        auto clock = [&]() { u64 now; asm volatile("s_memrealtime %0\n\ts_waitcnt lgkmcnt(0)" : "=s"(now) :: "memory"); return now; };
        WAIT_V(0);
        if (kt <= 16 - SD) {
          if (kt >= 1 + KS0) { accum(DMP + wid * 1024, DMP + 4096); WAIT_L(0); }
          if (kt <= 15 - SD) {
            if (kt >= KS0) share(kt + SD, DMP + wid * 1024, DMP + 4096);
          } else {
            GLDS_SC1(rstat + m0 + wid * 64 + (lane & 32) + 2 * (lane & 15) + ((lane >> 4) & 1), shm_base + AUX + wid * 1024);
          }
          if (kt == 16 - SD && (lane & 15) < 4) {
            const __amdgpu_buffer_rsrc_t rc = __builtin_amdgcn_make_buffer_rsrc((void*)(csum + ntile * 5 * 512 + mtile * 64), 0, 5 * 2048, 0x00020000);
            const int co = (lane & 15) * 64 + (lane >> 4) * 16;
            __builtin_amdgcn_raw_buffer_store_b128(__builtin_bit_cast(u32x4, ca), rc, co + wid * 2048, 0, 16);
            if (wid == 0) __builtin_amdgcn_raw_buffer_store_b128(__builtin_bit_cast(u32x4, cb), rc, co + 4 * 2048, 0, 16);
          }
          if (kt < SD + KS0) {
            if (kt <= SD + KS0 - 3 - ESH) share(kt + 2 + ESH, DMP + 7168, DMP + 7168);
            const int sp = cur * G1_STAGE + 32768 + mtile * 1024;
            accum(sp + wid * 8192, sp + 4 * 8192);
            if (kt == 1 - EROW || kt == 2 - EROW) {
              const bf16x8 ones = {0x3f80, 0x3f80, 0x3f80, 0x3f80, 0x3f80, 0x3f80, 0x3f80, 0x3f80};
              const char* const xr = shm + AUX + wid * 2048 + (lane & 8) * 128 + (lane >> 4) * 128 + (lane & 7) * 16;
              const bf16x8 r0 = *(const bf16x8*)xr, r1 = *(const bf16x8*)(xr + 512);
              const f32x4 z = {0.f, 0.f, 0.f, 0.f};
              const f32x4 s1 = __builtin_amdgcn_mfma_f32_16x16x32_bf16(ones, r1, __builtin_amdgcn_mfma_f32_16x16x32_bf16(ones, r0, z, 0, 0, 0), 0, 0, 0);
              const f32x4 s2 = __builtin_amdgcn_mfma_f32_16x16x32_bf16(r1, r1, __builtin_amdgcn_mfma_f32_16x16x32_bf16(r0, r0, z, 0, 0, 0), 0, 0, 0);
              float t0 = __uint_as_float((unsigned)tg) + s1[0];
              asm volatile("" : "+v"(t0));
              const float t1 = __uint_as_float((unsigned)(tg >> 32)) + ((lane & 2) ? ((lane & 1) ? s2[3] : s2[2]) : ((lane & 1) ? s2[1] : s2[0]));
              tg = ((u64)__float_as_uint(t1) << 32) | __float_as_uint(t0);
            }
            if (kt < 2 - EROW) {
#pragma unroll
              for (int i = 0; i < 2; ++i) glds16_p(x + (size_t)(srow + i) * D_ + (kt + EROW) * 512, lane * 16, shm_base + AUX + wid * 2048 + i * 1024);
            }
            if (kt == PUB_K) {
              if (wid == 0 && lane < 8) *(unsigned*)(shm + CNT + lane * 4) = 0u;
              const bool dg = ((lane >> 2) & 3) == (lane >> 4);
              float sm = half_sum(dg ? __uint_as_float((unsigned)tg) : 0.f);
              asm volatile("" : "+v"(sm));
              const float sq = half_sum(dg ? __uint_as_float((unsigned)(tg >> 32)) : 0.f);
              const u64 now = clock();
              if ((lane & 31) == 0) {
                const __amdgpu_buffer_rsrc_t rs = __builtin_amdgcn_make_buffer_rsrc((void*)(rstat + srow), 0, 32, 0x00020000);
                __builtin_amdgcn_raw_buffer_store_b128(u32x4{__float_as_uint(sm), (unsigned)now, __float_as_uint(sq), (unsigned)now ^ (unsigned)(now >> 32)}, rs,
                                                       lane >> 1, 0, 16);
              }
            }
          }
          if (kt >= PF_K0 && kt < PF_K0 + 5) {
#pragma unroll
            for (int i = 0; i < 2; ++i) {
              const int c = (bid * 4 + wid) * 10 + (kt - PF_K0) * 2 + i;
              if (c < 9216) {
                const u16* src = (c < 7168 ? wd + (size_t)c * 512 : wo + (size_t)(c - 7168) * 512) + lane * 8;
                GLDS_NC(src, shm_base + DMP + 7168);
              }
            }
          }
          if (kt >= CP_K0 && kt <= CP_K0 + 4) {
            if (kt < CP_K0 + 3) {
              const int q1 = bid * 4 + WID + (kt - CP_K0) * 1024, q0 = q1 - 1024;
              if (q0 >= 0 && q0 < 1152) {
                const __amdgpu_buffer_rsrc_t rc = __builtin_amdgcn_make_buffer_rsrc((void*)(wcopy + (q0 & 1) * 1179648 + (q0 >> 1) * 2048), 0, 2048, 0x00020000);
                __builtin_amdgcn_raw_buffer_store_b128(*(const u32x4*)(shm + AUX + WID * 1024 + lane * 16), rc, (lane >> 3) * 256 + (lane & 7) * 16, 0, 16);
                WAIT_L(0);
              }
              if (q1 < 1152) {
                const char* src = (const char*)((q1 >> 1) < 448 ? wd + (size_t)(q1 >> 1) * 8 * D_ : wo + (size_t)((q1 >> 1) - 448) * 8 * D_);
                src += ((3 - ((unsigned)(uintptr_t)src >> 7)) & 7) * 128 + (q1 & 1) * 1024;
                GLDS(src + (lane >> 3) * 2048 + (lane & 7) * 16, shm_base + AUX + WID * 1024);
              }
            } else if (kt == CP_K0 + 4) {
              if (WID == 0) {
                const u64 now = clock();
                if (lane == 0) __hip_atomic_store(&donec[bid], now, RLX_AGENT);
              }
              if (WID == 2) GLDS_SC1((const char*)(done2 + bid * 128) + lane * 16, shm_base + DMP + 7168);
            }
          }
        } else if (kt == 17 - SD) {
          const u64 now = clock();
          if (lane == 0) __hip_atomic_store(&cstamp[(ntile * 4 + wid) * 8 + mtile], now, RLX_AGENT);
          if (WID >= 2) GLDS_SC1((const char*)donec + (WID - 2) * 1024 + lane * 16, shm_base + DMP + WID * 2048);
          {
            const char* const wp = (const char*)wd + (size_t)(xcd < 4 ? 28 + 5 * (xcd & 1) : 5 - 5 * (xcd & 1)) * 131072 + (idx * 4 + wid) * 1024;
#pragma unroll
            for (int i = 0; i < NWPF; ++i) glds16_p(wp + (long)((i & 1) + ((i >> 1) & 1) * 14 + (i >> 2) * 7) * 131072, lane * 16, shm_base + DMP + wid * 2048 + 1024);
          }
          u32x4 gr = *(const u32x4*)(shm + AUX + wid * 1024 + lane * 16);
          bool late = false;
          if (__builtin_expect(!__all(fresh(((u64)(gr[1] ^ gr[3]) << 32) | gr[1], t_own)), 0)) {
            const __amdgpu_buffer_rsrc_t rs = __builtin_amdgcn_make_buffer_rsrc((void*)(rstat + m0), 0, 4096, 0x00020000);
            const int prow = wid * 64 + (lane & 32) + 2 * (lane & 15) + ((lane >> 4) & 1);
            for (;;) {
              gr = __builtin_bit_cast(u32x4, __builtin_amdgcn_raw_buffer_load_b128(rs, prow * 16, 0, 16));
              if (__all(fresh(((u64)(gr[1] ^ gr[3]) << 32) | gr[1], t_own))) break;
              if (__builtin_amdgcn_s_memrealtime() - t_own > TIMEOUT_) { gr[1] = row_stats_serial(x + (size_t)(m0 + prow) * D_); late = true; break; }
              __builtin_amdgcn_s_sleep(2);
            }
          }
          float mean = __uint_as_float(gr[0]) * (1.0f / D_);
          asm volatile("" : "+v"(mean));
          const unsigned ps = pack_stats(mean, fmaxf(__builtin_fmaf(-mean, mean, __uint_as_float(gr[2]) * (1.0f / D_)), 0.f));
          const unsigned g0 = late ? gr[1] : ps;
          const float nm = -lo_f(g0), dn = hi_f(g0), r = __builtin_amdgcn_rcpf(dn);
          float k1 = -2.3022081986f * r, d2 = dn * dn;
          asm volatile("" : "+v"(k1));
          float k2 = -0.10294324f * r;
          asm volatile("" : "+v"(d2), "+v"(k2));
          const f32x4 fc = {nm, k1, k2 * r * r, d2};
          *(f32x4*)(shm + AUX + wid * 1024 + lane * 16) = fc;
        } else if (kt == 18 - SD) {
          const u32x4 c4 = *(const u32x4*)(shm + DMP + WID * 2048 + lane * 16);
          asm volatile("" :: "v"(c4) : "memory");
          cgather();
          const u64 win = tg - t_own;
          auto frs = [&](unsigned lo, unsigned hi) { return (((u64)hi << 32) | lo) - t_own - 100ull < win; };
          if (WID >= 2) *(unsigned*)(shm + CNT - 4 + WID * 4) = __all(frs(c4[0], c4[1]) & frs(c4[2], c4[3])) ? 1u : 0u;
        } else {
          const char* const sc = shm + (lane < 32 ? DMP + wid * 2048 + lane * 8 : QS + wid * 640 + (lane & 7) * 8);
          const int st = lane < 32 ? 256 : 64;
          const f32x4 fc = *(const f32x4*)(shm + AUX + wid * 1024 + lane * 16);
          f32x2 w[8];
          bool late = false;
          for (;;) {
            const u64 sv = *(const u64*)(shm + QS + wid * 640 + 512 + (lane & 15) * 8);
#pragma unroll
            for (int r = 0; r < 8; ++r) w[r] = *(const f32x2*)(sc + r * st);
            if (__builtin_expect(__all(sv - t_own - 100ull < tg - t_own - 200ull), 1)) break;
            late = __builtin_amdgcn_s_memrealtime() - t_own > TIMEOUT_;
            if (late) break;
            __builtin_amdgcn_s_sleep(2);
            WAIT_L(0);
            cgather();
            WAIT_V(0);
          }
          float t0 = w[0][0], t1 = w[0][1];
#pragma unroll
          for (int r = 1; r < 8; ++r)
            asm("v_add_f32 %0, %0, %2\n\tv_add_f32 %1, %1, %3" : "+v"(t0), "+v"(t1) : "v"(w[r][0]), "v"(w[r][1]));
          if (__builtin_expect(late, 0)) {
            const int gq = (lane >> 3) & 3;
            const char* pc = (lane < 32 ? pbase + ((psh & gq) ? dgu : 0) + (gq >> psh) * 32 : gb + dgu + wid * 32) + (lane & 7) * 4;
            t0 = 0.f; t1 = 0.f;
#pragma unroll 1
            for (int k = 0; k < D_; ++k) { const unsigned v = *(const unsigned*)(pc + (size_t)k * (lane < 32 ? pstr : F_ * 2)); t0 += lo_f(v); t1 += hi_f(v); }
          }
          if (lane < 40)
            *(f32x2*)((float*)(shm + CF) + (lane < 32 ? (lane >> 4) * 160 + wid * 32 + (lane & 15) * 2 : (wid >> 1) * 160 + 128 + (wid & 1) * 16 + (lane & 7) * 2)) = f32x2{t0, t1};
          float* const tw = (float*)(shm + DMP + WID * 2048) + lane;
#pragma unroll
          for (int i = 0; i < 4; ++i) tw[i * 64] = fc[i];
          ((float*)(shm + QS + WID * 640))[lane] = fc[1] * (-1.0f / 2.3022081986f);
        }
      }
      SB0();
    }
#pragma unroll
    for (int u = 0; u < 8; ++u) mm(u >> 2, 8 + (u >> 2), 1, u & 3);
  TS1(1);
  __builtin_amdgcn_sched_barrier(0);
  int tz = threadIdx.x;
  asm volatile("s_nop 7" : "+v"(tz));
  __builtin_amdgcn_sched_barrier(0);
  const int li = tz & 15, lq = (tz >> 4) & 3, ln = tz & 63;
  const __amdgpu_buffer_rsrc_t ra = __builtin_amdgcn_make_buffer_rsrc((void*)abuf, 0, 16 * 72 * 16384, 0x00020000);
  float* const svr = (float*)(shm + QS + wm * 640);
  float* const tb = (float*)(shm + DMP + wm * 2048);
  typedef __attribute__((address_space(3))) unsigned lds_u32;
  lds_u32* const cnt = (lds_u32*)(shm3 + CNT);
  unsigned vcnt = shm_base + CNT;
  asm volatile("" : "+v"(vcnt));
  auto signal = [&](unsigned v) {
    unsigned t;
    asm volatile("v_mov_b32 %0, %2\n\ts_mov_b64 exec, 1\n\tds_add_u32 %1, %0\n\ts_mov_b64 exec, -1" : "=&v"(t) : "v"(vcnt), "s"(v) : "memory");
  };
  auto await = [&](unsigned mask, unsigned val) {
    unsigned t, u;
    asm volatile("1:\n\tds_read_b32 %0, %2\n\ts_waitcnt lgkmcnt(0)\n\tv_readfirstlane_b32 %1, %0\n\ts_and_b32 %1, %1, %3\n\ts_cmp_eq_u32 %1, %4\n\t"
                 "s_cbranch_scc1 2f\n\ts_sleep 1\n\ts_branch 1b\n\t2:" : "=&v"(t), "=&s"(u) : "v"(vcnt), "s"(mask), "s"(val) : "memory", "scc");
  };
  if (grp) __builtin_amdgcn_s_setprio(1);
  signal(1u << (8 * grp));
  f32x4 rf[4];
#pragma unroll
  for (int mf = 0; mf < 4; ++mf) rf[mf] = *(const f32x4*)(shm + AUX + (wm * 64 + mf * 16 + li) * 16);
  const int sbase = ((mtile * 2 + (wm >> 1)) * 72 * 8 + (wm & 1) * 4) * 2048;
  auto putf = [&](int kg, int mf, u32x4 v) {
    __builtin_amdgcn_raw_buffer_store_b128(v, ra, ln * 16 + sbase + ((kg >> 1) * 8 + mf) * 2048 + (kg & 1) * 1024, 0, 16);
  };
  auto gelp = [&](f32x2 tg, f32x2 tu, f32x2 k12, f32x2 k22, f32x2 d2) {
    const f32x2 arg = tg * __builtin_elementwise_fma(tg * tg, k22, k12);
    const f32x2 dn = __builtin_elementwise_fma(f32x2{__builtin_amdgcn_exp2f(arg[0]), __builtin_amdgcn_exp2f(arg[1])}, d2, d2);
    const f32x2 r = (tg * tu) * f32x2{__builtin_amdgcn_rcpf(dn[0]), __builtin_amdgcn_rcpf(dn[1])};
    return pack2(r[0], r[1]);
  };
  auto gels = [&](const f32x4& g, const f32x4& u, const f32x4& k) {
    float t0, t1, t2, t3, n0, n1, n2, n3;
    asm("v_mul_f32 %0, %8, %8\n\tv_mul_f32 %1, %9, %9\n\tv_mul_f32 %2, %10, %10\n\tv_mul_f32 %3, %11, %11\n\t"
        "v_fma_f32 %0, %0, %17, %16\n\tv_fma_f32 %1, %1, %17, %16\n\tv_fma_f32 %2, %2, %17, %16\n\tv_fma_f32 %3, %3, %17, %16\n\t"
        "v_mul_f32 %0, %8, %0\n\tv_mul_f32 %1, %9, %1\n\tv_mul_f32 %2, %10, %2\n\tv_mul_f32 %3, %11, %3\n\t"
        "v_exp_f32 %0, %0\n\tv_mul_f32 %4, %8, %12\n\tv_exp_f32 %1, %1\n\tv_mul_f32 %5, %9, %13\n\t"
        "v_exp_f32 %2, %2\n\tv_mul_f32 %6, %10, %14\n\tv_exp_f32 %3, %3\n\tv_mul_f32 %7, %11, %15\n\t"
        "v_fma_f32 %0, %0, %18, %18\n\tv_fma_f32 %1, %1, %18, %18\n\tv_rcp_f32 %0, %0\n\tv_fma_f32 %2, %2, %18, %18\n\t"
        "v_rcp_f32 %1, %1\n\tv_fma_f32 %3, %3, %18, %18\n\tv_rcp_f32 %2, %2\n\tv_mul_f32 %0, %4, %0\n\t"
        "v_rcp_f32 %3, %3\n\tv_mul_f32 %1, %5, %1\n\tv_mul_f32 %2, %6, %2\n\tv_mul_f32 %3, %7, %3\n\t"
        "v_cvt_pk_bf16_f32 %0, %0, %1\n\tv_cvt_pk_bf16_f32 %1, %2, %3"
        : "=&v"(t0), "=&v"(t1), "=&v"(t2), "=&v"(t3), "=&v"(n0), "=&v"(n1), "=&v"(n2), "=&v"(n3)
        : "v"(g[0]), "v"(g[1]), "v"(g[2]), "v"(g[3]), "v"(u[0]), "v"(u[1]), "v"(u[2]), "v"(u[3]), "v"(k[1]), "v"(k[2]), "v"(k[3]));
    return u32x2{__float_as_uint(t0), __float_as_uint(t1)};
  };
  auto gel2 = [&](int p, int kg, int mf0 = 0, int mf1 = 4, int nsc = 0, bool opq = false) {
#pragma unroll
    for (int mf = mf0; mf < mf1; ++mf) {
      const f32x2 k12 = {rf[mf][1], rf[mf][1]}, k22 = {rf[mf][2], rf[mf][2]}, d2 = {rf[mf][3], rf[mf][3]};
      u32x4 o;
      if (opq) asm volatile("" : "+v"(acc[mf][p * 2]), "+v"(acc[mf][p * 2 + 1]));
      if (mf < nsc) {
        const u32x2 a = gels(acc[mf][p * 2], acc[mf][p * 2 + 2], rf[mf]), b = gels(acc[mf][p * 2 + 1], acc[mf][p * 2 + 3], rf[mf]);
        o = u32x4{a[0], a[1], b[0], b[1]};
      } else {
#pragma unroll
        for (int i = 0; i < 4; ++i) {
          const int j = i >> 1, e = (i & 1) * 2;
          const f32x4 g = acc[mf][p * 2 + j], u = acc[mf][p * 2 + 2 + j];
          o[i] = gelp(f32x2{g[e], g[e + 1]}, f32x2{u[e], u[e + 1]}, k12, k22, d2);
        }
      }
      putf(kg, mf, o);
      if (mf < nsc) SB0();
    }
  };
  auto client = [&](int l) { return (((l & 7) * 4 + (mtile & 3)) * 8) + (((mtile >> 2) * 2 + ((l >> 4) & 1)) ^ ((l & 8) ? 5 : 0)); };
  auto notify = [&](u64 lanes) {
    int l = threadIdx.x & 63;
    asm volatile("" : "+v"(l));
    const unsigned off = (client(l) * 128 + (l & 32) + ntile) * 8;
    u64 now, vn;
    asm volatile("s_memrealtime %0\n\ts_nop 0\n\ts_waitcnt lgkmcnt(0)\n\tv_mov_b64 %1, %0\n\ts_mov_b64 exec, %4\n\tglobal_store_dwordx2 %2, %1, %3 sc1\n\ts_mov_b64 exec, -1"
                 : "=&s"(now), "=&v"(vn) : "v"(off), "s"(done1), "s"(lanes) : "memory");
  };
  const u64 myh = 0xffffull << ((wm >> 1) * 16);
  const u64 mrole = (xcd & 4) ? 0x00ff00ffull : 0xff00ff00ull;
  const u64 mall = (tya || (xcd & 3) == 3) ? 0xffffffffull : mrole;
  auto arrive = [&](auto pc) {
    constexpr int part = decltype(pc)::value;
    unsigned t;
    asm volatile("v_mov_b32 %0, 1\n\ts_mov_b64 exec, 1\n\tds_add_rtn_u32 %0, %1, %0 offset:%2\n\ts_mov_b64 exec, -1\n\ts_waitcnt lgkmcnt(0)"
                 : "=&v"(t) : "v"(vcnt), "n"(12 + 4 * part) : "memory");
    if (__builtin_amdgcn_readfirstlane(t) == 3u) notify(part ? (myh & mall) << 32 : myh & mrole);
  };
  const int role = 1 - (xcd >> 2);
  const int xb = role ? xcd : xcd ^ 5, par = xb & 1;
  const int mt2 = ((xb >> 1) * 4 + (idx & 3)) * 2 + par, nt2 = idx >> 2;
  auto rsrc = [](u64 b, unsigned n) { return u32x4{(unsigned)b, (unsigned)(b >> 32) & 0xffffu, n, 0x00020000u}; };
  u32x4 rw[2];
  int wsh[2];
  unsigned vb[2][2], cokw = 0;
  auto wsetup = [&](const u16* w, int rows, int crow0, int l) {
    const unsigned vr = (wid >> 1) * 32 + (wid & 1) * 8 + ((l >> 5) & 1) * 16 + ((l >> 3) & 3), vc = (((l >> 1) & 3) ^ ((l >> 4) & 1)) * 32 + (l & 1) * 16;
    u64 bs = (u64)(uintptr_t)(wcopy + (nt2 >> 2) * 1179648 + crow0 * 256 - 1024), bf = (u64)(uintptr_t)(w + nt2 * 128) - 1024;
    asm volatile("" : "+s"(bs), "+s"(bf));
#pragma unroll
    for (int i = 0; i < 2; ++i) {
      const bool slow = ((((unsigned)bf >> 7) + i) & 7) == (cokw ? 3u : 8u);
      const int sh = slow ? 8 : 11;
      rw[i] = rsrc(slow ? bs : bf + i * 128, ((rows - 1) << sh) + 128 + 1024);
      wsh[i] = 6 + sh;
      vb[i][0] = (vr << sh) + vc + 1024;
      vb[i][1] = ((vr + 4) << sh) + (vc ^ 64);
    }
  };
#define G2_W "s_add_u32 m0, %[l], %[ow]\n\ts_lshl_b32 %[t], %[k], %[s0]\n\tbuffer_load_dwordx4 %[v0], %[w0], %[t] offen sc1 lds\n\t" \
             "buffer_load_dwordx4 %[v1], %[w0], %[t] offen offset:1024 sc1 lds\n\t" \
             "s_add_u32 m0, m0, 8192\n\ts_lshl_b32 %[t], %[k], %[s1]\n\tbuffer_load_dwordx4 %[v2], %[w1], %[t] offen sc1 lds\n\t" \
             "buffer_load_dwordx4 %[v3], %[w1], %[t] offen offset:1024 sc1 lds"
#define G2_WOP [ow] "s"(16384 + wid * 2048), [k] "s"(kt), [s0] "s"(wsh[0]), [s1] "s"(wsh[1]), [w0] "s"(rw[0]), [w1] "s"(rw[1]), \
               [v0] "v"(vb[0][0]), [v1] "v"(vb[0][1]), [v2] "v"(vb[1][0]), [v3] "v"(vb[1][1])
#define G2_A "s_add_u32 m0, %[l], %[oa]\n\ts_lshl_b32 %[t], %[k], 14\n\tbuffer_load_dwordx4 %[va], %[a], %[t] offen sc1 lds\n\t" \
             "buffer_load_dwordx4 %[va], %[a], %[t] offen offset:1024 sc1 lds\n\t"
#define G2_A2 "buffer_load_dwordx4 %[va], %[a], %[t] offen offset:2048 sc1 lds\n\tbuffer_load_dwordx4 %[va], %[a], %[t] offen offset:3072 sc1 lds\n\t"
  constexpr int PD = G2_NSTAGE - 1;
  constexpr int NY = 28;
  const int ny = (xcd & 1) ? NY - DLT : NY + DLT;
  const u32x4 ra2 = rsrc((u64)(uintptr_t)abuf + mt2 * (72 * 16384), 72 * 16384);
  unsigned va, tab, nlo, nhi, sl = shm_base + 4 * G2_STAGE;
  const int kfirst = 28 * role + 5 * par;
  int q = 3, kt = kfirst + 15;
  u64 okb = 0;
  auto adv = [&]() { ++q; kt = __builtin_amdgcn_readlane(tab, q); sl = sl == shm_base + (G2_NSTAGE - 1) * G2_STAGE ? shm_base : sl + G2_STAGE; };
  auto stage_y = [&]() {
    unsigned t;
    asm volatile(G2_A G2_A2 G2_W : [t] "=&s"(t) : [l] "s"(sl), [oa] "s"((wid ^ role) * 4096), [a] "s"(ra2), [va] "v"(va), G2_WOP : "memory", "scc", "m0");
    adv();
  };
  auto digest = [&](u64 v, u64 valid = ~0ull) {
    const u64 r = __ballot(fresh(v, t_own)) & valid;
    const unsigned hi = (unsigned)(r >> 32), lo = (unsigned)r | hi;
    okb |= __ballot(((nlo & ~lo) | (nhi & ~hi)) == 0u);
  };
  auto aload = [&](const u64* base, unsigned off) {
    asm volatile("s_nop 4\n\tglobal_load_dwordx2 v[254:255], %0, %1 sc1" :: "v"(off), "s"(base) : "memory", "v254", "v255");
    SB0();
  };
  auto aget = [&]() { u64 v; asm volatile("v_mov_b64 %0, v[254:255]" : "=v"(v) :: "memory"); return v; };
  auto snapshot = [&]() { aload(done1 + bid * 128, ln * 8); };
  auto peek = [&]() { digest(aget()); };
  auto snapshot1 = [&]() { aload(done1 + bid * 128 + role * 16, (ln & 15) * 8); };
  auto peek1 = [&]() { digest(aget(), 0xffffull << (16 * role)); };
  auto wpair = [&](int g, int i) {
    unsigned t;
    asm volatile("s_mov_b32 m0, %1\n\ts_lshl_b32 %0, %2, %3\n\tbuffer_load_dwordx4 %5, %4, %0 offen sc1 lds\n\tbuffer_load_dwordx4 %6, %4, %0 offen offset:1024 sc1 lds"
                 : "=&s"(t) : "s"(shm_base + (g + 1) * G2_STAGE + 16384 + i * 8192 + wid * 2048), "s"(kfirst + (g == 2 ? 14 : g)), "s"(wsh[i]), "s"(rw[i]),
                   "v"(vb[i][0]), "v"(vb[i][1]) : "memory", "scc", "m0");
    SB0();
  };
  auto aquad = [&](int g) {
    unsigned t;
    asm volatile(G2_A G2_A2 : [t] "=&s"(t) : [l] "s"(shm_base + (g + 1) * G2_STAGE), [oa] "s"((wid ^ role) * 4096), [a] "s"(ra2), [va] "v"(va),
                 [k] "s"(((okb >> g) & 1) ? kfirst + (g == 2 ? 14 : g) : 100) : "memory", "scc", "m0");
    SB0();
  };
  auto mid = [&]() {
    cokw = __builtin_amdgcn_readfirstlane(cnt[1] & cnt[2]);
    wsetup(wd, F_, 0, ln);
    va = ln * 16 + wid * 4096;
    {
      const int ext = ln >= NY ? 1 : 0, qq = ln - ext * DLT, so = ln - ny;
      int j0 = 5 * par, j1 = 5 - 5 * par;
      asm volatile("" : "+s"(j1));
      const int j = (qq < 8 ? j0 : (qq < 16 ? j1 : 2)) + ((unsigned)(0x92492444444444ull >> (2 * qq)) & 3);
      const int xs = ((role ^ ext) << 2) | ((unsigned)(0xfd5a80f5a0f5a0ull >> (2 * qq)) & 3);
      unsigned bits = ((0x41a8282u >> (4 * j)) & 15u) << (4 * xs), ob = (so < 16 ? 1u : 0u) << (2 * so), ty = 7 * xs + j;
      asm volatile("" : "+v"(bits), "+v"(ob), "+v"(ty));
      tab = so < 0 ? ty : so;
      nlo = ln < 8 ? bits : 0u;
      nhi = so < 0 ? bits - nlo : ob;
    }
    asm volatile("" :: "s"(rw[0]), "s"(rw[1]), "s"(wsh[0]), "s"(wsh[1]), "s"(ra2));
    asm volatile("" : "+v"(vb[0][0]), "+v"(vb[0][1]), "+v"(vb[1][0]), "+v"(vb[1][1]), "+v"(va), "+v"(tab), "+v"(nlo), "+v"(nhi));
    await(0xffffu, 0x404u);
    wpair(0, 0);
    wpair(0, 1);
    WAIT_V(4);
    arrive(std::integral_constant<int, 0>{});
    __builtin_amdgcn_s_setprio(PRB);
  };
  int tsel = ntile & 1;
  asm volatile("" : "+s"(tsel));
  if (tsel != 0) {
    if (grp == 0) gel2(0, 2 * (kt0 + he) + wc, 0, NSC, NSC);
    else gel2(0, 2 * (kt0 + he) + wc, 0, NSC);
    gel2(0, 2 * (kt0 + he) + wc, NSC, 4);
    if (grp == 0) mid();
#pragma unroll
    for (int mf = 0; mf < 4; ++mf) {
      f32x4 t[4];
#pragma unroll
      for (int i = 1; i < 4; ++i) t[i] = *(const f32x4*)(tb + i * 64 + mf * 16 + lq * 4);
      const f32x4 g = acc[mf][4], u = acc[mf][5];
      u32x2 o;
#pragma unroll
      for (int e = 0; e < 4; e += 2)
        o[e >> 1] = gelp(f32x2{g[e], g[e + 1]}, f32x2{u[e], u[e + 1]}, f32x2{t[1][e], t[1][e + 1]}, f32x2{t[2][e], t[2][e + 1]}, f32x2{t[3][e], t[3][e + 1]});
      *(u32x2*)(shm + wm * 4096 + mf * 1024 + wc * 512 + li * 32 + lq * 8) = o;
      if (mf == 1 || grp == 0) SB0();
      if (grp == 0) {
        wpair(1 + (mf >> 1), mf & 1);
        if (mf == SNP) snapshot1();
      }
    }
    signal(0x10000u << vid);
    TS1(5);
    if (grp) {
      gel2(3, 2 * (kt0 + 2 + he) + wc, 0, 4);
    } else {
#pragma unroll
      for (int mf = 0; mf < 4; ++mf) {
        gel2(3, 2 * (kt0 + 2 + he) + wc, mf, mf + 1);
        SB0();
        if (mf == 0) { WAIT_V(7 - 2 * SNP); peek1(); }
        if (mf < 3) aquad(mf);
        if (mf == 2) snapshot1();
      }
    }
    await(0x110000u << wm, 0x110000u << wm);
#pragma unroll
    for (int i = 0; i < 2; ++i) putf(2 * (kt0 + 4) + he, 2 * wc + i, __builtin_bit_cast(u32x4, tr_frag(shm3 + wm * 4096 + wc * 2048 + i * 1024 + ln * 8)));
    if (grp == 0) WAIT_V(9);
    TS1(6);
  } else {
    if (grp) __builtin_amdgcn_s_setprio(PRL0);
    float rd[4];
#pragma unroll
    for (int mf = 0; mf < 4; ++mf) rd[mf] = svr[mf * 16 + li];
    auto cvs = [&](const f32x4& a, const f32x4& b, const f32x4& r) {
      float t0, t1, t2, t3, t4, t5, t6, t7;
      asm("v_mul_f32 %0, %8, %16\n\tv_mul_f32 %4, %9, %17\n\tv_mul_f32 %1, %10, %18\n\tv_mul_f32 %5, %11, %19\n\t"
          "v_mul_f32 %2, %12, %16\n\tv_mul_f32 %6, %13, %17\n\tv_mul_f32 %3, %14, %18\n\tv_mul_f32 %7, %15, %19\n\t"
          "v_cvt_pk_bf16_f32 %0, %0, %4\n\tv_cvt_pk_bf16_f32 %1, %1, %5\n\tv_cvt_pk_bf16_f32 %2, %2, %6\n\tv_cvt_pk_bf16_f32 %3, %3, %7"
          : "=&v"(t0), "=&v"(t1), "=&v"(t2), "=&v"(t3), "=&v"(t4), "=&v"(t5), "=&v"(t6), "=&v"(t7)
          : "v"(a[0]), "v"(a[1]), "v"(a[2]), "v"(a[3]), "v"(b[0]), "v"(b[1]), "v"(b[2]), "v"(b[3]), "v"(r[0]), "v"(r[1]), "v"(r[2]), "v"(r[3]));
      return u32x4{__float_as_uint(t0), __float_as_uint(t1), __float_as_uint(t2), __float_as_uint(t3)};
    };
    auto cvp = [&](const f32x4& a, const f32x4& b) { return u32x4{pack2(a[0], a[1]), pack2(a[2], a[3]), pack2(b[0], b[1]), pack2(b[2], b[3])}; };
    auto cvk = [&](int mf, bool sc) {
      const f32x4 r4 = {rd[mf], rd[mf], rd[mf], rd[mf]};
      return sc ? cvs(acc[mf][2], acc[mf][3], r4) : cvp(acc[mf][2] * r4, acc[mf][3] * r4);
    };
    auto cvv = [&](int mf, bool sc) {
      const f32x4 r4 = *(const f32x4*)(svr + mf * 16 + lq * 4);
      return sc ? cvs(acc[mf][4], acc[mf][5], r4) : cvp(acc[mf][4] * r4, acc[mf][5] * r4);
    };
    u32x4 kx[4], vx[4], qx[4], hx[4];
    const bf16x8 ones = {0x3f80, 0x3f80, 0x3f80, 0x3f80, 0x3f80, 0x3f80, 0x3f80, 0x3f80};
    kx[2] = cvk(2, true); kx[3] = cvk(3, true);
    vx[2] = cvv(2, true); vx[3] = cvv(3, true);
    const bool giveh = !grp && wm == 3 && mtile < 7, needh = grp && wm == 0 && mtile > 0;
    char* const hptr = (char*)halo + (size_t)((needh ? mtile - 1 : mtile) * 32 + ntile + wc) * 16384;
    if (grp || wm < 3) {
      char* const ex = shm + (vid - wc) * 4096 + ln * 16;
      *(u32x4*)ex = kx[2]; *(u32x4*)(ex + 1024) = kx[3]; *(u32x4*)(ex + 2048) = vx[2]; *(u32x4*)(ex + 3072) = vx[3];
      signal(0x10000u << vid);
    } else if (giveh) {
      const __amdgpu_buffer_rsrc_t rh = __builtin_amdgcn_make_buffer_rsrc((void*)hptr, 0, 9216, 0x00020000);
      const u64 now = __builtin_amdgcn_s_memrealtime();
      const unsigned tag = (unsigned)now;
#pragma unroll
      for (int r = 0; r < 8; ++r) {
        const u32x4 w = r < 4 ? kx[2 + (r >> 1)] : vx[r >> 1];
        __builtin_amdgcn_raw_buffer_store_b128(u32x4{w[2 * (r & 1)], tag, w[2 * (r & 1) + 1], tag}, rh, ln * 16 + r * 1024, 0, 16);
      }
      __builtin_amdgcn_raw_buffer_store_b128(u32x4{tag, (unsigned)(now >> 32), tag, (unsigned)(now >> 32)}, rh, ln * 16 + 8192, 0, 16);
    }
    if (grp == 0) { gel2(3, 2 * (kt0 + 5 + he) + wc, 0, 4, NSC - 1, true); mid(); }
    kx[0] = cvk(0, false); kx[1] = cvk(1, false);
    vx[0] = cvv(0, false); vx[1] = cvv(1, false);
#pragma unroll
    for (int mf = 0; mf < 4; ++mf) qx[mf] = cvp(acc[mf][0], acc[mf][1]);
    f32x4 mk0, mk1;
#pragma unroll
    for (int j = 0; j < 4; ++j) {
      const int k0 = 4 * lq + j, k1 = 16 + 4 * lq + j;
      mk0[j] = (k0 >= li + 9 && k0 <= li + 16) ? 0.f : -1.0e30f;
      mk1[j] = (k1 >= li + 9 && k1 <= li + 16) ? 0.f : -1.0e30f;
    }
    auto max3 = [](float a, float b, float c) { float r; asm("v_max3_f32 %0, %1, %2, %3" : "=v"(r) : "v"(a), "v"(b), "v"(c)); return r; };
    auto max2 = [](float a, float b) { float r; asm("v_max_f32 %0, %1, %2" : "=v"(r) : "v"(a), "v"(b)); return r; };
    auto tiles = [&](int t0, const u32x4* kp, const u32x4* vp, bool first_rows) {
      const f32x4 z = {0.f, 0.f, 0.f, 0.f};
      const f32x4 mz = {-1.0e30f, -1.0e30f, -1.0e30f, -1.0e30f};
      f32x4 s0[2], s1[2];
#pragma unroll
      for (int n = 0; n < 2; ++n) {
        const bf16x8 qf = __builtin_bit_cast(bf16x8, qx[t0 + n]);
        s0[n] = __builtin_amdgcn_mfma_f32_16x16x32_bf16(__builtin_bit_cast(bf16x8, kp[n]), qf, first_rows ? mz : mk0, 0, 0, 0);
        s1[n] = __builtin_amdgcn_mfma_f32_16x16x32_bf16(__builtin_bit_cast(bf16x8, kx[t0 + n]), qf, mk1, 0, 0, 0);
      }
      SB0();
      if (grp == 0) { if (t0) wpair(1, 1); else aquad(1); }
      asm volatile("s_nop 7\n\ts_nop 3" : "+v"(s0[0]), "+v"(s1[0]), "+v"(s0[1]), "+v"(s1[1]));
      float mx[2];
#pragma unroll
      for (int n = 0; n < 2; ++n) mx[n] = max3(max3(s0[n][0], s0[n][1], s0[n][2]), max3(s0[n][3], s1[n][0], s1[n][1]), max2(s1[n][2], s1[n][3]));
#pragma unroll
      for (int n = 0; n < 2; ++n) { auto r = __builtin_amdgcn_permlane16_swap(__float_as_uint(mx[n]), __float_as_uint(mx[n]), false, false);
        mx[n] = max2(__uint_as_float(r[0]), __uint_as_float(r[1])); }
#pragma unroll
      for (int n = 0; n < 2; ++n) { auto r = __builtin_amdgcn_permlane32_swap(__float_as_uint(mx[n]), __float_as_uint(mx[n]), false, false);
        mx[n] = max2(__uint_as_float(r[0]), __uint_as_float(r[1])); }
      SB0();
      if (grp == 0) {
        if (t0) wpair(2, 0); else aquad(2);
        snapshot1();
      }
      constexpr float cs2 = 0.17677669529663687f * 1.4426950408889634f;
      f32x4 os[2], o0[2], o1[2];
#pragma unroll
      for (int n = 0; n < 2; ++n) {
        const float c = cs2 * rd[t0 + n], nm = -mx[n] * c;
        float e8[8];
#pragma unroll
        for (int j = 0; j < 4; ++j) {
          e8[j] = __builtin_amdgcn_exp2f(__builtin_fmaf(s0[n][j], c, nm));
          e8[4 + j] = __builtin_amdgcn_exp2f(__builtin_fmaf(s1[n][j], c, nm));
        }
        u32x4 pk;
#pragma unroll
        for (int j = 0; j < 4; ++j) pk[j] = pack2(e8[2 * j], e8[2 * j + 1]);
        const bf16x8 pf = __builtin_bit_cast(bf16x8, pk);
        const u32x4 va = {vp[n][0], vp[n][1], vx[t0 + n][0], vx[t0 + n][1]}, vb = {vp[n][2], vp[n][3], vx[t0 + n][2], vx[t0 + n][3]};
        os[n] = __builtin_amdgcn_mfma_f32_16x16x32_bf16(ones, pf, z, 0, 0, 0);
        o0[n] = __builtin_amdgcn_mfma_f32_16x16x32_bf16(__builtin_bit_cast(bf16x8, va), pf, z, 0, 0, 0);
        o1[n] = __builtin_amdgcn_mfma_f32_16x16x32_bf16(__builtin_bit_cast(bf16x8, vb), pf, z, 0, 0, 0);
      }
      if (grp == 0 && t0) { SB0(); wpair(2, 1); }
#pragma unroll
      for (int n = 0; n < 2; ++n) {
        const float inv = __builtin_amdgcn_rcpf(os[n][0]);
        const f32x4 n0 = o0[n] * inv, n1 = o1[n] * inv;
        putf(112 + ntile + wc, t0 + n, u32x4{pack2(n0[0], n0[1]), pack2(n0[2], n0[3]), pack2(n1[0], n1[1]), pack2(n1[2], n1[3])});
      }
    };
    TS1(5);
    if (grp == 0) wpair(1, 0);
    tiles(2, &kx[0], &vx[0], false);
    constexpr int HL = 131072, HS = DMP + 1024;
    auto hload = [&]() {
      asm volatile("s_mov_b32 m0, %2\n\ts_nop 0\n\tglobal_load_lds_dwordx4 %0, %1 offset:-4096 sc1\n\tglobal_load_lds_dwordx4 %0, %1 offset:-3072 sc1\n\t"
                   "global_load_lds_dwordx4 %0, %1 offset:-2048 sc1\n\tglobal_load_lds_dwordx4 %0, %1 offset:-1024 sc1\n\tglobal_load_lds_dwordx4 %0, %1 sc1\n\t"
                   "global_load_lds_dwordx4 %0, %1 offset:1024 sc1\n\tglobal_load_lds_dwordx4 %0, %1 offset:2048 sc1\n\tglobal_load_lds_dwordx4 %0, %1 offset:3072 sc1\n\t"
                   "s_mov_b32 m0, %4\n\ts_nop 0\n\tglobal_load_lds_dwordx4 %0, %3 sc1"
                   :: "v"(ln * 16), "s"(hptr + 4096), "s"(shm_base + HL + wc * 8192 + 4096), "s"(hptr + 8192), "s"(shm_base + HS + wc * 2048) : "memory", "m0");
    };
    if (needh) { await(0xffffu, 0x404u); hload(); }
    if (grp) { __builtin_amdgcn_s_setprio(PRL1); gel2(3, 2 * (kt0 + 5 + he) + wc, 0, 2, 0, true); __builtin_amdgcn_s_setprio(PRL0); }
    else {
      WAIT_V(4);
      peek1();
      aquad(0);
    }
    TS1(6);
    if (!grp || wm > 0) {
      await(0x8000u << vid, 0x8000u << vid);
#pragma unroll
      for (int i = 0; i < 4; ++i) hx[i] = *(const u32x4*)(shm + (vid - wc - 1) * 4096 + i * 1024 + ln * 16);
    } else if (!needh) {
#pragma unroll
      for (int i = 0; i < 4; ++i) hx[i] = u32x4{0u, 0u, 0u, 0u};
    } else {
      bool late = false;
      WAIT_V(2);
      for (;;) {
        const char* const hl = shm + HL + wc * 8192 + ln * 16;
        const u32x4 sp = *(const u32x4*)(shm + HS + wc * 2048 + ln * 16);
        unsigned df = 0;
#pragma unroll
        for (int i = 0; i < 4; ++i) {
          const u32x4 g0 = *(const u32x4*)(hl + i * 2048), g1 = *(const u32x4*)(hl + i * 2048 + 1024);
          df |= (g0[1] ^ sp[0]) | (g0[3] ^ sp[0]) | (g1[1] ^ sp[0]) | (g1[3] ^ sp[0]);
          hx[i] = u32x4{g0[0], g0[2], g1[0], g1[2]};
        }
        if (__builtin_expect(__all((df == 0) & fresh(((u64)sp[1] << 32) | sp[0], t_own)), 1)) break;
        late = __builtin_amdgcn_s_memrealtime() - t_own > TIMEOUT_;
        if (late) break;
        __builtin_amdgcn_s_sleep(1);
        WAIT_L(0);
        hload();
        WAIT_V(0);
      }
      if (__builtin_expect(late, 0)) {
        char* const fb = shm + HL + wc * 8192 + ln * 16;
#pragma unroll 1
        for (int i = 0; i < 32; ++i) {
          const int rec = i >> 3, e = i & 7;
          const int row = m0 - 32 + 2 * (rec < 2 ? li : lq * 4 + (e & 3)) + (rec & 1);
          const int col = (rec < 2 ? D_ + lq * 4 + (e & 3) : 2 * D_ + li) + (ntile + wc) * 32 + (e >> 2) * 16;
          const u16* xr = x + (size_t)row * D_;
          const unsigned stt = row_stats_serial(xr);
          const float mb = lo_f(stt);
          float a = 0.f;
#pragma unroll 1
          for (int k = 0; k < D_; ++k) a += (bf2f(xr[k]) - mb) * bf2f(wqkv[(size_t)k * 3072 + col]);
          *(u16*)(fb + rec * 1024 + e * 2) = f2bf(a / hi_f(stt));
        }
#pragma unroll
        for (int i = 0; i < 4; ++i) hx[i] = *(const u32x4*)(fb + i * 1024);
      }
    }
    tiles(0, &hx[0], &hx[2], grp && mtile == 0 && wm == 0);
    if (grp) { __builtin_amdgcn_s_setprio(PRL1); gel2(3, 2 * (kt0 + 5 + he) + wc, 2, 4, 0, true); }
    else WAIT_V(7);
  }
  TS1(2);
  auto stamp = [&](u64* p) {
    WAIT_V(0);
    u64 now;
    asm volatile("s_memrealtime %0\n\ts_waitcnt lgkmcnt(0)" : "=s"(now) :: "memory");
    if ((threadIdx.x & 63) == 0) __hip_atomic_store(p, now, RLX_AGENT);
  };
  const bool cok = cokw != 0u;
  {
  __builtin_amdgcn_sched_barrier(0);
  int tid = threadIdx.x & 255;
  asm volatile("" : "+v"(tid));
  __builtin_amdgcn_sched_barrier(0);
  const int lane = tid & 63;
  const int wm = (wid >> 1) & 1, wn = wid & 1;
  const int mtile = mt2, ntile = nt2;
  const int m0 = mtile * 128, n0 = ntile * 128;
  TS(0);
  constexpr int EP = 288;
  char* const img = shm + ((ny + 13) % G2_NSTAGE) * G2_STAGE;
  if constexpr (!LD) {
    const int p = wid;
    const u64* const mbox = &done1[bid * 128 + lane];
    auto stage_s = [&]() {
      unsigned t;
      asm volatile(G2_A G2_W : [t] "=&s"(t) : [l] "s"(sl), [oa] "s"((p >> 1) * 8192 + (p & 1) * 2048), [a] "s"(ra2), [va] "v"(va), G2_WOP : "memory", "scc", "m0");
      adv();
    };
    u64 ren = 0;
    auto need = [&]() {
      while (__builtin_expect(!((okb >> q) & 1), 0)) {
        digest(__hip_atomic_load(mbox, RLX_AGENT));
        if (!((okb >> q) & 1)) {
          __builtin_amdgcn_s_sleep(PSLEEP);
          if (__builtin_expect(!cok, 0)) notify(ren);
          if (GIVEUP()) break;
        }
      }
      asm volatile("" ::: "memory");
    };
    __builtin_amdgcn_s_setprio(0);
    if ((okb & 7) != 7) {
      const unsigned done = (unsigned)okb;
#pragma unroll 1
      for (q = 0; q < 3; ++q)
        if (!((done >> q) & 1)) {
          need();
          aquad(q);
        }
      if (done & 4) WAIT_V(0);
      WAIT_V(4);
    }
    TS(4);
    BARL();
    need();
    stage_y();
    WAIT_V(8);
    peek1();
    if (p == 0) snapshot();
    need();
    stage_y();
    if (p) okb = ~0ull;
    auto check = [&]() {
      if (__builtin_expect(okb != ~0ull && !(q & 1), 0)) {
        peek();
        if (okb != ~0ull) snapshot();
      }
    };
#pragma unroll 1
    while (q < ny) {
      WAIT_V(16);
      need();
      BAR();
      check();
      if (__builtin_expect(q == PD + 1, 0)) {
        ren = 0xffffffff00000000ull;
        if (p == 1) notify((myh & mall) << 32);
      }
      stage_y();
    }
    va = lane * 16 + (p >> 1) * 8192 + role * 4096 + (p & 1) * 2048 + (56 << 14);
    wsetup(wo, D_, F_, lane);
#pragma unroll 1
    for (int g = 0; g < 16; ++g) {
      WAIT_V(12);
      need();
      BAR();
      check();
      if (__builtin_expect(g == 14 && p == 0, 0)) {
        ++q;
        need();
        --q;
        aload(done2 + (bid ^ 5) * 128, (lane & 3) * 8);
      }
      stage_s();
    }
    WAIT_V(12);
    if (p == 0) {
      while (__builtin_expect(!__all(fresh(aget(), t_own)), 0)) {
        if (__builtin_expect(!cok, 0)) { notify(ren); if (lane < 4) __hip_atomic_store(done2 + bid * 128 + lane, __builtin_amdgcn_s_memrealtime(), RLX_AGENT); }
        __builtin_amdgcn_s_sleep(1);
        aload(done2 + (bid ^ 5) * 128, (lane & 3) * 8);
        WAIT_V(0);
        if (GIVEUP()) break;
      }
    }
    BAR();
    {
      const char* ps = (const char*)slab + (size_t)(bid ^ 5) * 16384 + p * 4096 + lane * 16;
#pragma unroll
      for (int i = 0; i < 4; ++i) GLDS_SC1(ps + i * 1024, sl + p * 4096 + i * 1024);
    }
    WAIT_V(10);
    BAR();
    WAIT_V(4);
    BAR();
    WAIT_V(0);
    BAR();
    u32x4 xv[4];
#pragma unroll
    for (int i = 0; i < 4; ++i) {
      const int c = i * 256 + tid, r = c >> 4;
      xv[i] = *(const u32x4*)(x + (size_t)(m0 + (r >> 5) * 64 + role * 32 + (r & 31)) * D_ + n0 + (c & 15) * 8);
    }
    BAR();
#pragma unroll
    for (int i = 0; i < 4; ++i) {
      const int c = i * 256 + tid, r = c >> 4;
      const u32x4 tv = *(const u32x4*)(img + ((r & 32) + (r & 1) * 16 + ((r >> 1) & 15)) * EP + (c & 15) * 16);
      u32x4 ov;
#pragma unroll
      for (int e = 0; e < 4; ++e) ov[e] = pack2(lo_f(xv[i][e]) + lo_f(tv[e]), hi_f(xv[i][e]) + hi_f(tv[e]));
      __builtin_nontemporal_store(ov, (u32x4*)(out + (size_t)(m0 + (r >> 5) * 64 + role * 32 + (r & 31)) * D_ + n0 + (c & 15) * 8));
    }
    TS(5);
    if (tid == 0) __hip_atomic_store(&acks[bid], __builtin_amdgcn_s_memrealtime(), RLX_AGENT);
    if (__builtin_expect(!cok, 0) && p == 0) {
      const bool mine = lane <= 32;
      const u64* const pa = &acks[lane < 32 ? client(lane) : bid ^ 5];
      while (!__all(!mine || fresh(__hip_atomic_load(pa, RLX_AGENT), t_own))) {
        notify(ren);
        if (lane < 4) __hip_atomic_store(done2 + bid * 128 + lane, __builtin_amdgcn_s_memrealtime(), RLX_AGENT);
        __builtin_amdgcn_s_sleep(32);
        if (GIVEUP()) break;
      }
    }
  } else {
    TS(6);
    f32x4 acc[4][4], sacc[2][4];
    const int fr = lane & 15, fq = lane >> 4, q4 = fr >> 2, pp = lane & 3;
    const int ff = ((q4 >> 1) & 1) | ((fq & 1) << 1);
    int aof = G2_STAGE + wm * 8192 + lane * 16;
    int boff[4];
#pragma unroll
    for (int j = 0; j < 4; ++j) boff[j] = G2_STAGE + 16384 + wn * 8192 + (8 * fq + q4) * 128 + ((j ^ ff) * 32) + pp * 8;
#pragma unroll
    for (int b = 0; b < 4; ++b) {
#pragma unroll
      for (int a = 0; a < 4; ++a) acc[a][b] = f32x4{0.f, 0.f, 0.f, 0.f};
      sacc[0][b] = f32x4{0.f, 0.f, 0.f, 0.f}; sacc[1][b] = f32x4{0.f, 0.f, 0.f, 0.f};
    }
#define SB() __builtin_amdgcn_sched_barrier(0)
    struct Fr { bf16x8 a[4]; bf16x4 bl[4], bh[4]; };
    Fr f0[2], f1[2];
    auto rd = [&](Fr& f, lds_cptr sb, int ks, int t, bool sp) {
      constexpr int ord[12] = {0, 1, 8, 2, 3, 4, 5, 6, 7, 9, 10, 11};
      const int o = ord[t];
      if (o >= 8) f.a[o - 8] = *(const __attribute__((address_space(3))) bf16x8*)(sb + aof + (o - 8) * 2048 + ks * 1024);
      else if (o & 1) f.bh[o >> 1] = tr_read(sb + boff[o >> 1] + ks * 4096 + 512);
      else f.bl[o >> 1] = tr_read(sb + boff[o >> 1] + ks * 4096);
    };
    auto bfr = [&](const Fr& c, int j) { return __builtin_shufflevector(c.bl[j], c.bh[j], 0, 1, 2, 3, 4, 5, 6, 7); };
    auto ystg = [&](const Fr* c, Fr* n, int kind) {
      lds_cptr sb = shm3;
#pragma unroll
      for (int t = 0; t < 16; ++t) {
        const int mf = (t >> 2) ^ kind, j = (t & 4) ? 3 - (t & 3) : t & 3, ks = t & 1;
        SB();
        asm volatile("v_mfma_f32_16x16x32_bf16 %0, %1, %2, %0\n\tv_mfma_f32_16x16x32_bf16 %0, %3, %4, %0"
                     : "+v"(acc[mf][j]) : "v"(bfr(c[ks], j)), "v"(c[ks].a[mf]), "v"(bfr(c[ks ^ 1], j)), "v"(c[ks ^ 1].a[mf]));
        SB();
        const int h = kind == 2 ? 5 : 6;
        if (t < 2 * h) { rd(n[t / h], sb, t / h, 2 * (t % h), true); rd(n[t / h], sb, t / h, 2 * (t % h) + 1, true); }
      }
      SB();
      __builtin_amdgcn_s_waitcnt(0xC07F);
    };
    auto rds = [&](Fr* n, lds_cptr sb, int t) {
      if (t < 10) { rd(n[t / 5], sb, t / 5, 2 * (t % 5), true); rd(n[t / 5], sb, t / 5, 2 * (t % 5) + 1, true); }
    };
    auto stg = [&](const Fr* c, Fr* n, bool load = true) {
      lds_cptr sb = shm3;
#pragma unroll
      for (int t = 0; t < 16; ++t) {
        const int ks = t >> 3, mf = (t >> 2) & 1, j = (t & 4) ? 3 - (t & 3) : t & 3;
        SB();
        sacc[mf][j] = __builtin_amdgcn_mfma_f32_16x16x32_bf16(bfr(c[ks], j), c[ks].a[mf], sacc[mf][j], 0, 0, 0);
        SB();
        if (load) rds(n, sb, t);
      }
      SB();
    };
    asm volatile("" : "+v"(aof), "+v"(boff[0]), "+v"(boff[1]), "+v"(boff[2]), "+v"(boff[3]));
    asm volatile("" : "+v"(sacc[0][0]), "+v"(sacc[0][1]), "+v"(sacc[0][2]), "+v"(sacc[0][3]), "+v"(sacc[1][0]), "+v"(sacc[1][1]), "+v"(sacc[1][2]), "+v"(sacc[1][3]));
#pragma unroll
    for (int a = 0; a < 4; ++a) asm volatile("" : "+v"(acc[a][0]), "+v"(acc[a][1]), "+v"(acc[a][2]), "+v"(acc[a][3]));
    WAIT_V(0);
    arrive(std::integral_constant<int, 1>{});
    BARL();
    TS(7);
    __builtin_amdgcn_s_waitcnt(0xC07F);
    SB();
#pragma unroll
    for (int t = 0; t < 24; ++t) rd(f0[t & 1], shm3, t & 1, t >> 1, false);
    int so = G2_STAGE;
    auto nxt = [&]() {
      const int d = so == (G2_NSTAGE - 1) * G2_STAGE ? -(G2_NSTAGE - 1) * G2_STAGE : G2_STAGE;
      so += d; aof += d; boff[0] += d; boff[1] += d; boff[2] += d; boff[3] += d;
      asm volatile("" : "+v"(aof), "+v"(boff[0]), "+v"(boff[1]), "+v"(boff[2]), "+v"(boff[3]));
    };
    nxt();
    __builtin_amdgcn_s_waitcnt(0xC07F);
#pragma unroll 1
    for (int g = 0; g < ny - 2; g += 2) {
      ystg(f0, f1, 0);
      nxt();
      BAR();
      ystg(f1, f0, 0);
      nxt();
      BAR();
    }
    if (!(DLT & 1)) { ystg(f0, f1, 0); nxt(); BAR(); }
    Fr sa[2], sb2[2];
    ystg((DLT & 1) ? f0 : f1, sa, 2);
    asm volatile("" : "+v"(acc[0][0]), "+v"(acc[0][1]), "+v"(acc[0][2]), "+v"(acc[0][3]), "+v"(acc[1][0]), "+v"(acc[1][1]), "+v"(acc[1][2]), "+v"(acc[1][3]));
    TS(2);
    {
      const __amdgpu_buffer_rsrc_t rs = __builtin_amdgcn_make_buffer_rsrc((void*)((char*)slab + (size_t)bid * 16384), 0, 16384, 0x00020000);
#pragma unroll
      for (int f = 0; f < 4; ++f) {
        nxt();
        BAR();
        if (f & 1) stg(sb2, sa); else stg(sa, sb2);
        asm volatile("" : "+v"(acc[2 + (f >> 1)][(f & 1) * 2]), "+v"(acc[2 + (f >> 1)][(f & 1) * 2 + 1]));
        const f32x4 a = acc[2 + (f >> 1)][(f & 1) * 2], b = acc[2 + (f >> 1)][(f & 1) * 2 + 1];
        __builtin_amdgcn_raw_buffer_store_b128(u32x4{pack2(a[0], a[1]), pack2(a[2], a[3]), pack2(b[0], b[1]), pack2(b[2], b[3])}, rs,
                                               (f * 256 + tid) * 16, 0, 16);
        SB();
      }
    }
#pragma unroll 1
    for (int i = 2; i < 7; ++i) {
      nxt();
      BAR();
      stg(sa, sb2);
      nxt();
      BAR();
      stg(sb2, sa);
      if (i == STI) {
        stamp(done2 + bid * 128 + (wid & 3));
      }
    }
    nxt();
    BAR();
    stg(sa, sb2);
    BAR();
    u32x4 pv[4];
    {
      nxt();
      lds_cptr sb = shm3 + so + tid * 16;
#pragma unroll
      for (int f = 0; f < 4; ++f) pv[f] = *(const __attribute__((address_space(3))) u32x4*)(sb + f * 4096);
    }
    stg(sb2, sa, false);
    TS(1);
#pragma unroll
    for (int mf = 0; mf < 2; ++mf)
#pragma unroll
      for (int j = 0; j < 4; ++j) {
        const u32x4 pw = pv[mf * 2 + (j >> 1)];
        const f32x4 sv = sacc[mf][j];
        const f32x4 ya = acc[mf][j] + f32x4{lo_f(pw[2 * (j & 1)]), hi_f(pw[2 * (j & 1)]), lo_f(pw[2 * (j & 1) + 1]), hi_f(pw[2 * (j & 1) + 1])};
        u32x2 o;
#pragma unroll
        for (int e = 0; e < 2; ++e) {
          const f32x2 y2 = {ya[2 * e], ya[2 * e + 1]}, s2 = {sv[2 * e], sv[2 * e + 1]};
          const f32x2 ar = y2 * f32x2{-1.4426950408889634f, -1.4426950408889634f};
          const f32x2 dn = f32x2{__builtin_amdgcn_exp2f(ar[0]), __builtin_amdgcn_exp2f(ar[1])} + f32x2{1.0f, 1.0f};
          const f32x2 t = (s2 * y2) * f32x2{__builtin_amdgcn_rcpf(dn[0]), __builtin_amdgcn_rcpf(dn[1])};
          o[e] = pack2(t[0], t[1]);
        }
        *(u32x2*)(img + (wm * 32 + mf * 16 + fr) * EP + (wn * 64 + j * 16 + fq * 4) * 2) = o;
      }
    BARL();
    TS(3);
  }
  TSFLUSH();
  }
  };
  if (wid < 4) kloop(std::integral_constant<int, 0>{});
  else kloop(std::integral_constant<int, 1>{});
  asm volatile(".Lcode_end:");
}

extern "C" void kernel_launch(void* const* d_in, const int* in_sizes, int n_in, void* d_out, int out_size, void* d_ws,
                              size_t ws_size, hipStream_t stream) {
  const u16* A_x = (const u16*)d_in[0];
  const u16* A_wqkv = (const u16*)d_in[1];
  const u16* A_wo = (const u16*)d_in[2];
  const u16* B_wg = (const u16*)d_in[4];
  const u16* B_wu = (const u16*)d_in[5];
  const u16* B_wd = (const u16*)d_in[6];
  u16* out = (u16*)d_out;
  fused_kernel<<<256, 512, 0, stream>>>(A_x, A_wqkv, B_wg, B_wu, B_wd, A_wo, out, (char*)d_ws);
}
```

```cpp
#include <hip/hip_runtime.h>
#include <hip/hip_bf16.h>
#include <stdint.h>
#include <type_traits>
#include <utility>

typedef unsigned short u16;
using bf16x8 = __attribute__((ext_vector_type(8))) short;
using bf16x4 = __attribute__((ext_vector_type(4))) short;
using f32x4 = __attribute__((ext_vector_type(4))) float;
using u32x2 = __attribute__((ext_vector_type(2))) unsigned;
using u32x4 = __attribute__((ext_vector_type(4))) unsigned;
typedef __attribute__((address_space(3))) const char* lds_cptr;

template <class F, int... I> __device__ __forceinline__ void sfor_(F&& f, std::integer_sequence<int, I...>) { (f(std::integral_constant<int, I>{}), ...); }
template <int N, class F> __device__ __forceinline__ void sfor(F&& f) { sfor_(f, std::make_integer_sequence<int, N>{}); }

constexpr int S_ = 2048, D_ = 1024, F_ = 3584;

__device__ __forceinline__ float bf2f(u16 h) { return __uint_as_float((unsigned)h << 16); }
__device__ __forceinline__ u16 f2bf(float f) {
  unsigned u = __float_as_uint(f);
  u += 0x7fffu + ((u >> 16) & 1u);
  return (u16)(u >> 16);
}
__device__ __forceinline__ float rbf(float f) { return bf2f(f2bf(f)); }
typedef __attribute__((ext_vector_type(2))) __bf16 bf16x2_t;
typedef __attribute__((ext_vector_type(2))) float f32x2;
__device__ __forceinline__ unsigned pack2(float a, float b) {
  bf16x2_t r = __builtin_convertvector((f32x2){a, b}, bf16x2_t);
  return __builtin_bit_cast(unsigned, r);
}
__device__ __forceinline__ float lo_f(unsigned p) { return __uint_as_float(p << 16); }
__device__ __forceinline__ float hi_f(unsigned p) { return __uint_as_float(p & 0xffff0000u); }

#define TSDECL
#define TSS(i) do {} while (0)
#define TSFLUSH() do {} while (0)
#define TS1(i) TSS(i)
#define TS(i) TSS(8 + (i))
#define WAIT_V(n) asm volatile("s_waitcnt vmcnt(%0)" ::"n"(n) : "memory")
#define WAIT_L(n) asm volatile("s_waitcnt lgkmcnt(%0)" ::"n"(n) : "memory")
#define BARL() do { WAIT_L(0); BAR(); } while (0)
#define BAR() do { asm volatile("" ::: "memory"); __builtin_amdgcn_s_barrier(); asm volatile("" ::: "memory"); } while (0)

__device__ __forceinline__ bf16x4 tr_read(lds_cptr p) {
  return __builtin_amdgcn_ds_read_tr16_b64_v4i16((__attribute__((address_space(3))) bf16x4*)p);
}
__device__ __forceinline__ bf16x8 tr_frag(lds_cptr p) {
  bf16x4 lo = tr_read(p), hi = tr_read(p + 512);
  bf16x8 r;
  r[0] = lo[0]; r[1] = lo[1]; r[2] = lo[2]; r[3] = lo[3];
  r[4] = hi[0]; r[5] = hi[1]; r[6] = hi[2]; r[7] = hi[3];
  return r;
}
__device__ __forceinline__ void glds16(const void* gsrc, unsigned lds_dst) {
  unsigned keep;
  asm volatile("s_mov_b32 %0, m0\n\ts_mov_b32 m0, %2\n\ts_nop 0\n\tglobal_load_lds_dwordx4 %1, off\n\ts_mov_b32 m0, %0"
               : "=&s"(keep) : "v"(gsrc), "s"(lds_dst) : "memory");
}
#define GLDS(gptr, laddr) glds16((const void*)(gptr), (unsigned)(laddr))
__device__ __forceinline__ void glds16_sc1(const void* gsrc, unsigned lds_dst) {
  unsigned keep;
  asm volatile("s_mov_b32 %0, m0\n\ts_mov_b32 m0, %2\n\ts_nop 0\n\tglobal_load_lds_dwordx4 %1, off sc1\n\ts_mov_b32 m0, %0"
               : "=&s"(keep) : "v"(gsrc), "s"(lds_dst) : "memory");
}
#define GLDS_SC1(gptr, laddr) glds16_sc1((const void*)(gptr), (unsigned)(laddr))
__device__ __forceinline__ void glds16_nc(const void* gsrc, unsigned lds_dst) {
  unsigned keep;
  asm volatile("s_mov_b32 %0, m0\n\ts_mov_b32 m0, %2\n\ts_nop 0\n\tglobal_load_lds_dwordx4 %1, off\n\ts_mov_b32 m0, %0"
               : "=&s"(keep) : "v"(gsrc), "s"(lds_dst));
}
#define GLDS_NC(gptr, laddr) glds16_nc((const void*)(gptr), (unsigned)(laddr))
__device__ __forceinline__ void glds16_s(const void* sbase, unsigned voff, unsigned lds_dst) {
  asm volatile("s_mov_b32 m0, %2\n\ts_nop 0\n\tglobal_load_lds_dwordx4 %0, %1" :: "v"(voff), "s"(sbase), "s"(lds_dst) : "m0");
}

__device__ __forceinline__ void glds16_p(const void* sbase, unsigned voff, unsigned lds_dst) {
  asm volatile("s_mov_b32 m0, %2\n\ts_nop 3\n\tglobal_load_lds_dwordx4 %0, %1" :: "v"(voff), "s"(sbase), "s"(lds_dst) : "m0");
}

typedef unsigned long long u64;
__device__ __forceinline__ bool fresh(u64 stamp, u64 t_own) {
  return stamp - t_own - 100ull < __builtin_amdgcn_s_memrealtime() - t_own;
}
constexpr u64 TIMEOUT_ = 200000;
#define GIVEUP() (__builtin_amdgcn_s_memrealtime() - t_own > 100000000ull)
__device__ __forceinline__ float half_sum(float v) {
  v += __builtin_bit_cast(float, __builtin_amdgcn_update_dpp(0, __builtin_bit_cast(int, v), 0xB1, 0xf, 0xf, false));
  v += __builtin_bit_cast(float, __builtin_amdgcn_update_dpp(0, __builtin_bit_cast(int, v), 0x4E, 0xf, 0xf, false));
  v += __builtin_bit_cast(float, __builtin_amdgcn_update_dpp(0, __builtin_bit_cast(int, v), 0x141, 0xf, 0xf, false));
  v += __builtin_bit_cast(float, __builtin_amdgcn_update_dpp(0, __builtin_bit_cast(int, v), 0x140, 0xf, 0xf, false));
  { auto r = __builtin_amdgcn_permlane16_swap(__float_as_uint(v), __float_as_uint(v), false, false); v = __uint_as_float(r[0]) + __uint_as_float(r[1]); }
  return v;
}
__device__ __forceinline__ unsigned pack_stats(float mean, float var) {
  return (unsigned)f2bf(mean) | ((unsigned)f2bf(sqrtf(rbf(rbf(var) + 1e-5f))) << 16);
}
__device__ __forceinline__ unsigned row_stats_serial(const u16* xr) {
  float s = 0.f, q = 0.f;
#pragma unroll 1
  for (int k = 0; k < D_; ++k) s += bf2f(xr[k]);
  const float mean = s * (1.0f / D_);
#pragma unroll 1
  for (int k = 0; k < D_; ++k) { const float d = bf2f(xr[k]) - mean; q += d * d; }
  return pack_stats(mean, q * (1.0f / D_));
}
constexpr int G1_STAGE = 32768 + 5 * 8192;
#define PF_K0 6
#define CP_K0 3
#define DLT 1
#define PSLEEP 2
#define SD 6
#define NSC 2
#define PRB 0
#define PRL0 3
#define PRL1 1
#define NWPF 4
#define SNP 3
#define RD0 21
#define NP1 8
#define ESH 1
#define EROW 0
#define KS0 2
#define PADE 0
#define PADL 2
#define STI 2
#define PUB_K 5

#define RLX_AGENT __ATOMIC_RELAXED, __HIP_MEMORY_SCOPE_AGENT

constexpr int G2_STAGE = 16384 + 2 * 8192;
constexpr int G2_NSTAGE = 5;
constexpr size_t MB_ = 1024 * 1024;
__global__ __launch_bounds__(512, 2) __attribute__((amdgpu_num_vgpr(127))) void fused_kernel(const u16* __restrict__ x, const u16* __restrict__ wqkv,
                                                       const u16* __restrict__ wg, const u16* __restrict__ wu,
                                                       const u16* __restrict__ wd, const u16* __restrict__ wo,
                                                       u16* __restrict__ out, char* __restrict__ ws) {
  u16* const halo = (u16*)(ws + 4 * MB_);
  u16* const abuf = (u16*)(ws + 16 * MB_);
  char* const slab = ws + 64 * MB_;
  unsigned* const flags = (unsigned*)(ws + 96 * MB_);
  u32x4* const rstat = (u32x4*)(ws + 97 * MB_);
  char* const wcopy = ws + 104 * MB_;
  u64* const done1 = (u64*)(ws + 102 * MB_ + 512);
  u64* const done2 = (u64*)(ws + 102 * MB_);
  u64* const donec = (u64*)(ws + 100 * MB_ + 196608);
  u64* const acks = (u64*)(ws + 100 * MB_ + 262144);
  u64* const cstamp = (u64*)(ws + 100 * MB_ + 327680);
  float* const csum = (float*)(ws + 101 * MB_);
  __shared__ __attribute__((aligned(1024))) char shm[163840];
  constexpr int AUX = 2 * G1_STAGE + 8192;
  {
    u64 pc; int len;
    asm volatile("s_getpc_b64 %0\n.Lcode_beg:\n\ts_mov_b32 %1, .Lcode_end-.Lcode_beg" : "=s"(pc), "=s"(len));
    const int ol = ((blockIdx.x >> 3) + 32 * (threadIdx.x >> 6)) * 1024 + (threadIdx.x & 63) * 16;
    asm volatile("s_mov_b32 m0, %2\n\ts_nop 0\n\tglobal_load_lds_dwordx4 %0, %1"
                 :: "v"(ol < len - 16 ? ol : 0), "s"(pc & ~15ull), "s"((unsigned)(uintptr_t)(lds_cptr)shm + 2 * G1_STAGE + 7168) : "m0");
    __builtin_amdgcn_sched_barrier(0);
  }
  const u64 t_own = __builtin_amdgcn_s_memrealtime();
  __builtin_amdgcn_sched_barrier(0);
  TSDECL;
  const int tid = threadIdx.x, lane = tid & 63;
  const int wid = __builtin_amdgcn_readfirstlane(tid >> 6);
  const int bid = blockIdx.x, xcd = bid & 7, idx = bid >> 3;
  const int ntile = xcd * 4 + (idx & 3), mtile = idx >> 2;
  const int m0 = mtile * 256;
  const int hg = ntile >> 1;
  const bool tya = (ntile & 1) == 0;
  const int grp = wid >> 2, wc = (wid >> 1) & 1, wm = (((grp ^ ntile ^ 1) & 1) << 1) | (wid & 1), vid = wc * 4 + wm;
  const int srow = m0 + ntile * 8 + (wid & 3) * 2;
  lds_cptr shm3 = (lds_cptr)shm;
  const unsigned shm_base = (unsigned)(uintptr_t)shm3;
  const int r8 = tid >> 3, pos = tid & 7;
  const int phys = pos >> 1, half = pos & 1;
  const int fk = ((r8 >> 1) & 1) | (((r8 >> 3) & 1) << 1);
  const int vsrc = phys ^ fk;
  const int qstr = tya ? 3072 * 2 : F_ * 2;
  unsigned voa = (((r8 & 32) + 2 * (r8 & 15) + ((r8 >> 4) & 1)) * D_ + ((pos ^ (r8 & 7)) * 8)) * 2;
  unsigned vog = r8 * (F_ * 2) + vsrc * 32 + half * 16, voq = r8 * qstr + vsrc * 32 + half * 16;
  unsigned vom = r8 * qstr + (vsrc >> (tya ? 0 : 1)) * 32 + half * 16;
  asm volatile("" : "+v"(voa), "+v"(vog), "+v"(voq), "+v"(vom));
  asm volatile("" :: "s"(x), "s"(wqkv), "s"(wg), "s"(wu), "s"(wd), "s"(wo), "s"(out), "s"(ws));
  const char* const ab = (const char*)(x + (size_t)m0 * D_);
  int kz = 0;
  asm volatile("" : "+s"(kz));
  auto stage_a = [&](int buf, int kt, int i, int h) {
    glds16_s(ab + (long)(((i * 64 + (h - 1) * 32) * D_ + kt * 64) * 2), voa, shm_base + buf * G1_STAGE + (wid - 4 + 4 * h) * 1024 + i * 8192);
  };
#pragma unroll
  for (int i = 0; i < 4; ++i) stage_a(0, 0, i, 1);
  const int kt0 = (hg >> 1) * 7, he = hg & 1;
  const char* const gb = (const char*)(wg + (kt0 + (tya ? 5 + he : 2 + he)) * 64);
  const char* const qb = tya ? (const char*)(wqkv + 64 * hg) : gb - 256;
  const long dgu = (const char*)wu - (const char*)wg, dp1 = tya ? 2048 : dgu;
  const char* const mb0 = tya ? qb + 4096 : (const char*)(wg + (kt0 + 4) * 64 + 32 * he);
  const char* mptr = mb0 + (((vsrc & 1) && !tya) ? dgu : 0) + vom;
  auto wload = [&](int p, int rows, unsigned dst) {
    long off = (long)(rows * (p < 3 ? qstr : F_ * 2)) + (p == 1 ? dp1 : (p == 4 ? dgu : 0));
    if (p == 2) { asm volatile("" : "+s"(off)); GLDS_NC(mptr + off, dst); }
    else glds16_s((p < 2 ? qb : gb) + off, p < 2 ? voq : vog, dst);
  };
#pragma unroll
  for (int i = 0; i < 5; ++i) wload(i, 0, shm_base + wid * 1024 + 32768 + i * 8192);
  __builtin_amdgcn_sched_barrier(0);
  const int fr = lane & 15, fq = lane >> 4, q4 = fr >> 2, pp = lane & 3;
  const int ff = ((q4 >> 1) & 1) | ((fq & 1) << 1);
  int aoff[2], boff[2];
#pragma unroll
  for (int ks = 0; ks < 2; ++ks) aoff[ks] = (wm * 64 + fr) * 128 + (((ks * 4 + fq) ^ (fr & 7)) * 16);
#pragma unroll
  for (int j = 0; j < 2; ++j) boff[j] = 32768 + (8 * fq + q4) * 128 + (((2 * wc + j) ^ ff) * 32) + pp * 8;

  f32x4 acc[4][10];
#pragma unroll
  for (int a = 0; a < 4; ++a)
#pragma unroll
    for (int b = 0; b < 10; ++b) acc[a][b] = f32x4{0.f, 0.f, 0.f, 0.f};

  auto stage_part = [&](int buf, int kt, int i, int h) {
    if (i < 5) wload(i, kt * 64 + (h - 1) * 32, shm_base + buf * G1_STAGE + (wid - 4 + 4 * h) * 1024 + 32768 + i * 8192);
    else stage_a(buf, kt, i - 5, h);
  };
  constexpr int NT = D_ / 64;
  constexpr int DMP = 2 * G1_STAGE;
  constexpr int CF = AUX + 4096, QS = CF + 1280, CNT = CF + 3840;
  bf16x8 af0[4], af1[4];
  bf16x4 bl[10], bh[10];
#pragma unroll
  for (int i = 0; i < 4; ++i) { af1[i] = bf16x8{0, 0, 0, 0, 0, 0, 0, 0}; bl[i] = bf16x4{0, 0, 0, 0}; bh[i] = bf16x4{0, 0, 0, 0}; }
#define SB0() __builtin_amdgcn_sched_barrier(0)
  auto mm = [&](int n, int f, int ks, int mf) {
    const bf16x8 b = __builtin_shufflevector(bl[n % 10], bh[n % 10], 0, 1, 2, 3, 4, 5, 6, 7);
    if ((f >> 1) == 2) acc[mf][f] = __builtin_amdgcn_mfma_f32_16x16x32_bf16(ks ? af1[mf] : af0[mf], b, acc[mf][f], 0, 0, 0);
    else acc[mf][f] = __builtin_amdgcn_mfma_f32_16x16x32_bf16(b, ks ? af1[mf] : af0[mf], acc[mf][f], 0, 0, 0);
  };
  auto correct = [&]() {
    int l = threadIdx.x & 63;
    asm volatile("s_nop 7" : "+v"(l));
    const float* const tb = (const float*)(shm + DMP + wm * 2048);
    const float* const cf = (const float*)(shm + CF) + wc * 160;
    auto ld = [&](int f) { return f < 8 ? *(const f32x4*)(cf + (f < 4 ? f : f + 2) * 16 + (l >> 4) * 4) : *(const f32x4*)(tb + (f - 8) * 16 + (l >> 4) * 4); };
    float m[4];
    f32x4 cr[3];
#pragma unroll
    for (int mf = 0; mf < 4; ++mf) m[mf] = tb[mf * 16 + (l & 15)];
    cr[0] = ld(0);
    cr[1] = ld(1);
    const float c4 = cf[64 + (l & 15)], c5 = cf[80 + (l & 15)];
#pragma unroll
    for (int f = 0; f < 12; ++f) {
      if (f + 2 < 12) cr[(f + 2) % 3] = ld(f + 2);
      SB0();
      const f32x4 c = cr[f % 3];
#pragma unroll
      for (int i = 0; i < (f < 8 ? 4 : 2); ++i) {
        f32x4& a = f < 8 ? acc[i][f < 4 ? f : f + 2] : acc[f - 8][4 + i];
        float a0 = a[0], a1 = a[1], a2 = a[2], a3 = a[3];
        asm volatile("v_fmac_f32 %0, %4, %5\n\tv_fmac_f32 %1, %4, %6\n\tv_fmac_f32 %2, %4, %7\n\tv_fmac_f32 %3, %4, %8"
                     : "+v"(a0), "+v"(a1), "+v"(a2), "+v"(a3) : "v"(f < 8 ? m[i] : (i ? c5 : c4)), "v"(c[0]), "v"(c[1]), "v"(c[2]), "v"(c[3]));
        a = f32x4{a0, a1, a2, a3};
      }
      SB0();
    }
    asm volatile("s_nop 1");
  };
  auto kloop = [&](auto wmc) __attribute__((always_inline)) {
    constexpr bool LD = decltype(wmc)::value != 0;
    constexpr int grp = LD ? 1 : 0;
    const int WID = wid;
    const char* pa = nullptr;
    unsigned vob = 0;
    f32x4 ca = {0.f, 0.f, 0.f, 0.f}, cb = {0.f, 0.f, 0.f, 0.f};
    bf16x8 sel = {0, 0, 0, 0, 0, 0, 0, 0};
    int tro = 0;
    u64 tg = 0;
    const int psh = (wid == 2 && !tya) ? 1 : 0, pstr = wid < 3 ? qstr : F_ * 2;
    const char* const pbase = wid == 0 ? qb : (wid == 1 ? qb + dp1 : (wid == 2 ? mb0 : gb));
    if (!LD) {
      const int rs = mtile * 8 + (lane >> 3), vs = ((lane & 7) >> 1) ^ (((rs >> 1) & 1) | ((mtile & 1) << 1));
      pa = pbase + ((psh & vs) ? dgu : 0) + (rs * pstr + (vs >> psh) * 32 + (lane & 1) * 16);
      vob = rs * (F_ * 2) + vs * 32 + (lane & 1) * 16;
      const short one = fr == fq ? 0x3f80 : 0;
      sel = bf16x8{one, one, one, one, one, one, one, one};
      tro = q4 * 128 + ((fq ^ ((q4 >> 1) | ((mtile & 1) << 1))) * 32) + pp * 8;
      asm volatile("" : "+v"(sel), "+v"(tro), "+v"(ca), "+v"(cb));
    }
    auto share = [&](int t, unsigned dsta, unsigned dstb) {
      long off = (long)(t * 64 * pstr);
      asm volatile("" : "+s"(off));
      GLDS_NC(pa + off, shm_base + dsta);
      if (wid == 0) glds16_p(gb + dgu + (long)(t * 64 * F_ * 2), vob, shm_base + dstb);
    };
    auto accum = [&](int oa, int ob) {
      ca = __builtin_amdgcn_mfma_f32_16x16x32_bf16(tr_frag(shm3 + tro + oa), sel, ca, 0, 0, 0);
      if (wid == 0) cb = __builtin_amdgcn_mfma_f32_16x16x32_bf16(tr_frag(shm3 + tro + ob), sel, cb, 0, 0, 0);
    };
    auto cgather = [&]() {
      int l = threadIdx.x & 63;
      asm volatile("s_memrealtime %0\n\ts_waitcnt lgkmcnt(0)" : "=s"(tg), "+v"(l) :: "memory");
      const char* const p4 = l < 32 ? (const char*)(csum + (ntile * 5 + 4) * 512 + wid * 16) + (l >> 2) * 256
                                    : (const char*)(cstamp + (ntile * 4 + (l < 36 ? wid : 0)) * 8);
      if (l < 40) GLDS_SC1(p4 + (l & 3) * 16, shm_base + QS + wid * 640);
#pragma unroll
      for (int i = 0; i < 2; ++i) GLDS_SC1((const char*)(csum + (ntile * 5 + wid) * 512) + i * 1024 + l * 16, shm_base + DMP + wid * 2048 + i * 1024);
    };
    if (!LD) {
#pragma unroll
      for (int i = 0; i < ESH; ++i) share(2 + i, DMP + 7168, DMP + 7168);
      if (EROW) {
#pragma unroll
        for (int i = 0; i < 2; ++i) glds16_p(x + (size_t)(srow + i) * D_, lane * 16, shm_base + AUX + wid * 2048 + i * 1024);
      }
    }
    if (LD) {
#pragma unroll
      for (int i = 0; i < NP1; ++i) {
        stage_part(1, kz + 1, i >> 1, i & 1);
        asm volatile("" : "+v"(acc[0][i]), "+v"(acc[1][i]), "+v"(acc[2][i]), "+v"(acc[3][i]));
      }
    }
#pragma unroll
    for (int a = 0; a < 4; ++a)
      asm volatile("" : "+v"(acc[a][0]), "+v"(acc[a][1]), "+v"(acc[a][2]), "+v"(acc[a][3]), "+v"(acc[a][4]), "+v"(acc[a][5]), "+v"(acc[a][6]),
                   "+v"(acc[a][7]), "+v"(acc[a][8]), "+v"(acc[a][9]));
    asm volatile("" : "+v"(aoff[0]), "+v"(aoff[1]), "+v"(boff[0]), "+v"(boff[1]));
    TS1(3);
    if constexpr (LD) {
      TS1(4);
      WAIT_V(NP1);
      BAR();
    } else {
      TS1(4);
      if (wid) WAIT_V(ESH + 2 * EROW);
      WAIT_V(2 * ESH + 2 * EROW);
    }
    TS1(0);
#pragma clang loop unroll(disable)
    for (int kt = 0; kt < NT; ++kt) {
      int cur = kt & 1;
      if (!LD && kt == NT - 1) { correct(); __builtin_amdgcn_s_setprio(2); }
      if (!LD) {
        asm volatile("" : "+s"(cur));
        __builtin_amdgcn_s_waitcnt(0xC07F);
        asm volatile(".p2align 5\n\t.fill %0, 4, 0xBF800000" :: "n"(PADE));
        BAR();
      }
      lds_cptr sb = shm3;
      if (LD) {
        unsigned k2 = kt - 1;
        asm volatile("" : "+s"(k2));
        if (k2 < NT - 2) {
#pragma unroll
          for (int i = 0; i < NP1; ++i) stage_part(cur ^ 1, kt + 1, i >> 1, i & 1);
        }
      }
      if (LD && kt + 1 < NT) {
#pragma unroll
        for (int i = NP1; i < 18; ++i) stage_part(cur ^ 1, kt + 1, i >> 1, i & 1);
      } else if (LD) {
        correct();
      }
      auto fadr = [&](int n) {
        const int f = n < 2 ? 8 + n : (n < 4 ? 11 - n : (n - 4) >> 1), ks = n < 2 ? 1 : (n < 4 ? 0 : n & 1);
        return sb + boff[f & 1] + (f >> 1) * 8192 + ks * 4096;
      };
      auto rdl = [&](int n) { bl[n % 10] = tr_read(fadr(n % 20)); };
      auto rdh = [&](int n) { bh[n % 10] = tr_read(fadr(n % 20) + 512); };
      auto rda = [&](bf16x8* af, int ks, int mf) { af[mf] = *(const __attribute__((address_space(3))) bf16x8*)(sb + aoff[ks] + mf * 2048); };
      SB0();
      if (LD) __builtin_amdgcn_s_waitcnt(0xC07F);
      if (LD) asm volatile(".p2align 5\n\t.fill %0, 4, 0xBF800000" :: "n"(PADL));
      sfor<80>([&](auto uc) __attribute__((always_inline)) {
        constexpr int u = decltype(uc)::value;
        SB0();
        if constexpr (u < 16) {
          const int n = u >> 2;
          mm(n, n < 2 ? 8 + n : 11 - n, n < 2 ? 1 : 0, (n & 1) ? 3 - (u & 3) : u & 3);
        } else {
          const int f = (u - 16) >> 3, i = (u >> 1) & 3, ks = (u ^ i) & 1;
          if ((u & 7) == 0) { asm volatile("" :: "v"(bl[(5 + 2 * f) % 10]), "v"(bh[(5 + 2 * f) % 10])); SB0(); }
          mm(4 + 2 * f + ks, f, ks, (f & 1) ? 3 - i : i);
        }
        SB0();
        if (u == 0) { rdl(2); rdh(2); }
        if (u == 1) { rda(af0, 0, 0); rda(af0, 0, 1); }
        if (u == 2) { rda(af0, 0, 2); rda(af0, 0, 3); }
        if (u == 3) { rdl(3); rdh(3); }
        if constexpr (u >= 4 && u < 8) rda(af1, 1, 7 - u);
        if (u >= 8 && u < 16) { if (u & 1) rdh(u >> 1); else rdl(u >> 1); }
        if (u >= RD0 && u < RD0 + 55 && (u & 1)) {
          const int n = 8 + ((u - RD0) >> 2);
          if ((u - RD0) & 2) rdh(n); else rdl(n);
        }
      });
      SB0();
      if (LD)
        asm volatile("s_cmp_eq_u32 %0, %1\n\ts_waitcnt vmcnt(0) lgkmcnt(0)\n\ts_cbranch_scc1 1f\n\ts_barrier\n1:" :: "s"(kt), "n"(NT - 1) : "memory", "scc");
      SB0();
      {
        const int d = cur ? -G1_STAGE : G1_STAGE;
        aoff[0] += d; aoff[1] += d; boff[0] += d; boff[1] += d;
        asm volatile("" : "+v"(aoff[0]), "+v"(aoff[1]), "+v"(boff[0]), "+v"(boff[1]));
      }
      if (!LD && kt <= 19 - SD) {
        int lane = threadIdx.x & 63;
        asm volatile("" : "+v"(lane));
        auto clock = [&]() { u64 now; asm volatile("s_memrealtime %0\n\ts_waitcnt lgkmcnt(0)" : "=s"(now) :: "memory"); return now; };
        WAIT_V(0);
        if (kt <= 16 - SD) {
          if (kt >= 1 + KS0) { accum(DMP + wid * 1024, DMP + 4096); WAIT_L(0); }
          if (kt <= 15 - SD) {
            if (kt >= KS0) share(kt + SD, DMP + wid * 1024, DMP + 4096);
          } else {
            GLDS_SC1(rstat + m0 + wid * 64 + (lane & 32) + 2 * (lane & 15) + ((lane >> 4) & 1), shm_base + AUX + wid * 1024);
          }
          if (kt == 16 - SD && (lane & 15) < 4) {
            const __amdgpu_buffer_rsrc_t rc = __builtin_amdgcn_make_buffer_rsrc((void*)(csum + ntile * 5 * 512 + mtile * 64), 0, 5 * 2048, 0x00020000);
            const int co = (lane & 15) * 64 + (lane >> 4) * 16;
            __builtin_amdgcn_raw_buffer_store_b128(__builtin_bit_cast(u32x4, ca), rc, co + wid * 2048, 0, 16);
            if (wid == 0) __builtin_amdgcn_raw_buffer_store_b128(__builtin_bit_cast(u32x4, cb), rc, co + 4 * 2048, 0, 16);
          }
          if (kt < SD + KS0) {
            if (kt <= SD + KS0 - 3 - ESH) share(kt + 2 + ESH, DMP + 7168, DMP + 7168);
            const int sp = cur * G1_STAGE + 32768 + mtile * 1024;
            accum(sp + wid * 8192, sp + 4 * 8192);
            if (kt == 1 - EROW || kt == 2 - EROW) {
              const bf16x8 ones = {0x3f80, 0x3f80, 0x3f80, 0x3f80, 0x3f80, 0x3f80, 0x3f80, 0x3f80};
              const char* const xr = shm + AUX + wid * 2048 + (lane & 8) * 128 + (lane >> 4) * 128 + (lane & 7) * 16;
              const bf16x8 r0 = *(const bf16x8*)xr, r1 = *(const bf16x8*)(xr + 512);
              const f32x4 z = {0.f, 0.f, 0.f, 0.f};
              const f32x4 s1 = __builtin_amdgcn_mfma_f32_16x16x32_bf16(ones, r1, __builtin_amdgcn_mfma_f32_16x16x32_bf16(ones, r0, z, 0, 0, 0), 0, 0, 0);
              const f32x4 s2 = __builtin_amdgcn_mfma_f32_16x16x32_bf16(r1, r1, __builtin_amdgcn_mfma_f32_16x16x32_bf16(r0, r0, z, 0, 0, 0), 0, 0, 0);
              float t0 = __uint_as_float((unsigned)tg) + s1[0];
              asm volatile("" : "+v"(t0));
              const float t1 = __uint_as_float((unsigned)(tg >> 32)) + ((lane & 2) ? ((lane & 1) ? s2[3] : s2[2]) : ((lane & 1) ? s2[1] : s2[0]));
              tg = ((u64)__float_as_uint(t1) << 32) | __float_as_uint(t0);
            }
            if (kt < 2 - EROW) {
#pragma unroll
              for (int i = 0; i < 2; ++i) glds16_p(x + (size_t)(srow + i) * D_ + (kt + EROW) * 512, lane * 16, shm_base + AUX + wid * 2048 + i * 1024);
            }
            if (kt == PUB_K) {
              if (wid == 0 && lane < 8) *(unsigned*)(shm + CNT + lane * 4) = 0u;
              const bool dg = ((lane >> 2) & 3) == (lane >> 4);
              float sm = half_sum(dg ? __uint_as_float((unsigned)tg) : 0.f);
              asm volatile("" : "+v"(sm));
              const float sq = half_sum(dg ? __uint_as_float((unsigned)(tg >> 32)) : 0.f);
              const u64 now = clock();
              if ((lane & 31) == 0) {
                const __amdgpu_buffer_rsrc_t rs = __builtin_amdgcn_make_buffer_rsrc((void*)(rstat + srow), 0, 32, 0x00020000);
                __builtin_amdgcn_raw_buffer_store_b128(u32x4{__float_as_uint(sm), (unsigned)now, __float_as_uint(sq), (unsigned)now ^ (unsigned)(now >> 32)}, rs,
                                                       lane >> 1, 0, 16);
              }
            }
          }
          if (kt >= PF_K0 && kt < PF_K0 + 5) {
#pragma unroll
            for (int i = 0; i < 2; ++i) {
              const int c = (bid * 4 + wid) * 10 + (kt - PF_K0) * 2 + i;
              if (c < 9216) {
                const u16* src = (c < 7168 ? wd + (size_t)c * 512 : wo + (size_t)(c - 7168) * 512) + lane * 8;
                GLDS_NC(src, shm_base + DMP + 7168);
              }
            }
          }
          if (kt >= CP_K0 && kt <= CP_K0 + 4) {
            if (kt < CP_K0 + 3) {
              const int q1 = bid * 4 + WID + (kt - CP_K0) * 1024, q0 = q1 - 1024;
              if (q0 >= 0 && q0 < 1152) {
                const __amdgpu_buffer_rsrc_t rc = __builtin_amdgcn_make_buffer_rsrc((void*)(wcopy + (q0 & 1) * 1179648 + (q0 >> 1) * 2048), 0, 2048, 0x00020000);
                __builtin_amdgcn_raw_buffer_store_b128(*(const u32x4*)(shm + AUX + WID * 1024 + lane * 16), rc, (lane >> 3) * 256 + (lane & 7) * 16, 0, 16);
                WAIT_L(0);
              }
              if (q1 < 1152) {
                const char* src = (const char*)((q1 >> 1) < 448 ? wd + (size_t)(q1 >> 1) * 8 * D_ : wo + (size_t)((q1 >> 1) - 448) * 8 * D_);
                src += ((3 - ((unsigned)(uintptr_t)src >> 7)) & 7) * 128 + (q1 & 1) * 1024;
                GLDS(src + (lane >> 3) * 2048 + (lane & 7) * 16, shm_base + AUX + WID * 1024);
              }
            } else if (kt == CP_K0 + 4) {
              if (WID == 0) {
                const u64 now = clock();
                if (lane == 0) __hip_atomic_store(&donec[bid], now, RLX_AGENT);
              }
              if (WID == 2) GLDS_SC1((const char*)(done2 + bid * 128) + lane * 16, shm_base + DMP + 7168);
            }
          }
        } else if (kt == 17 - SD) {
          const u64 now = clock();
          if (lane == 0) __hip_atomic_store(&cstamp[(ntile * 4 + wid) * 8 + mtile], now, RLX_AGENT);
          if (WID >= 2) GLDS_SC1((const char*)donec + (WID - 2) * 1024 + lane * 16, shm_base + DMP + WID * 2048);
          {
            const char* const wp = (const char*)wd + (size_t)(xcd < 4 ? 28 + 5 * (xcd & 1) : 5 - 5 * (xcd & 1)) * 131072 + (idx * 4 + wid) * 1024;
#pragma unroll
            for (int i = 0; i < NWPF; ++i) glds16_p(wp + (long)((i & 1) + ((i >> 1) & 1) * 14 + (i >> 2) * 7) * 131072, lane * 16, shm_base + DMP + wid * 2048 + 1024);
          }
          u32x4 gr = *(const u32x4*)(shm + AUX + wid * 1024 + lane * 16);
          bool late = false;
          if (__builtin_expect(!__all(fresh(((u64)(gr[1] ^ gr[3]) << 32) | gr[1], t_own)), 0)) {
            const __amdgpu_buffer_rsrc_t rs = __builtin_amdgcn_make_buffer_rsrc((void*)(rstat + m0), 0, 4096, 0x00020000);
            const int prow = wid * 64 + (lane & 32) + 2 * (lane & 15) + ((lane >> 4) & 1);
            for (;;) {
              gr = __builtin_bit_cast(u32x4, __builtin_amdgcn_raw_buffer_load_b128(rs, prow * 16, 0, 16));
              if (__all(fresh(((u64)(gr[1] ^ gr[3]) << 32) | gr[1], t_own))) break;
              if (__builtin_amdgcn_s_memrealtime() - t_own > TIMEOUT_) { gr[1] = row_stats_serial(x + (size_t)(m0 + prow) * D_); late = true; break; }
              __builtin_amdgcn_s_sleep(2);
            }
          }
          float mean = __uint_as_float(gr[0]) * (1.0f / D_);
          asm volatile("" : "+v"(mean));
          const unsigned ps = pack_stats(mean, fmaxf(__builtin_fmaf(-mean, mean, __uint_as_float(gr[2]) * (1.0f / D_)), 0.f));
          const unsigned g0 = late ? gr[1] : ps;
          const float nm = -lo_f(g0), dn = hi_f(g0), r = __builtin_amdgcn_rcpf(dn);
          float k1 = -2.3022081986f * r, d2 = dn * dn;
          asm volatile("" : "+v"(k1));
          float k2 = -0.10294324f * r;
          asm volatile("" : "+v"(d2), "+v"(k2));
          const f32x4 fc = {nm, k1, k2 * r * r, d2};
          *(f32x4*)(shm + AUX + wid * 1024 + lane * 16) = fc;
        } else if (kt == 18 - SD) {
          const u32x4 c4 = *(const u32x4*)(shm + DMP + WID * 2048 + lane * 16);
          asm volatile("" :: "v"(c4) : "memory");
          cgather();
          const u64 win = tg - t_own;
          auto frs = [&](unsigned lo, unsigned hi) { return (((u64)hi << 32) | lo) - t_own - 100ull < win; };
          if (WID >= 2) *(unsigned*)(shm + CNT - 4 + WID * 4) = __all(frs(c4[0], c4[1]) & frs(c4[2], c4[3])) ? 1u : 0u;
        } else {
          const char* const sc = shm + (lane < 32 ? DMP + wid * 2048 + lane * 8 : QS + wid * 640 + (lane & 7) * 8);
          const int st = lane < 32 ? 256 : 64;
          const f32x4 fc = *(const f32x4*)(shm + AUX + wid * 1024 + lane * 16);
          f32x2 w[8];
          bool late = false;
          for (;;) {
            const u64 sv = *(const u64*)(shm + QS + wid * 640 + 512 + (lane & 15) * 8);
#pragma unroll
            for (int r = 0; r < 8; ++r) w[r] = *(const f32x2*)(sc + r * st);
            if (__builtin_expect(__all(sv - t_own - 100ull < tg - t_own - 200ull), 1)) break;
            late = __builtin_amdgcn_s_memrealtime() - t_own > TIMEOUT_;
            if (late) break;
            __builtin_amdgcn_s_sleep(2);
            WAIT_L(0);
            cgather();
            WAIT_V(0);
          }
          float t0 = w[0][0], t1 = w[0][1];
#pragma unroll
          for (int r = 1; r < 8; ++r)
            asm("v_add_f32 %0, %0, %2\n\tv_add_f32 %1, %1, %3" : "+v"(t0), "+v"(t1) : "v"(w[r][0]), "v"(w[r][1]));
          if (__builtin_expect(late, 0)) {
            const int gq = (lane >> 3) & 3;
            const char* pc = (lane < 32 ? pbase + ((psh & gq) ? dgu : 0) + (gq >> psh) * 32 : gb + dgu + wid * 32) + (lane & 7) * 4;
            t0 = 0.f; t1 = 0.f;
#pragma unroll 1
            for (int k = 0; k < D_; ++k) { const unsigned v = *(const unsigned*)(pc + (size_t)k * (lane < 32 ? pstr : F_ * 2)); t0 += lo_f(v); t1 += hi_f(v); }
          }
          if (lane < 40)
            *(f32x2*)((float*)(shm + CF) + (lane < 32 ? (lane >> 4) * 160 + wid * 32 + (lane & 15) * 2 : (wid >> 1) * 160 + 128 + (wid & 1) * 16 + (lane & 7) * 2)) = f32x2{t0, t1};
          float* const tw = (float*)(shm + DMP + WID * 2048) + lane;
#pragma unroll
          for (int i = 0; i < 4; ++i) tw[i * 64] = fc[i];
          ((float*)(shm + QS + WID * 640))[lane] = fc[1] * (-1.0f / 2.3022081986f);
        }
      }
      SB0();
    }
#pragma unroll
    for (int u = 0; u < 8; ++u) mm(u >> 2, 8 + (u >> 2), 1, u & 3);
  TS1(1);
  __builtin_amdgcn_sched_barrier(0);
  int tz = threadIdx.x;
  asm volatile("s_nop 7" : "+v"(tz));
  __builtin_amdgcn_sched_barrier(0);
  const int li = tz & 15, lq = (tz >> 4) & 3, ln = tz & 63;
  const __amdgpu_buffer_rsrc_t ra = __builtin_amdgcn_make_buffer_rsrc((void*)abuf, 0, 16 * 72 * 16384, 0x00020000);
  float* const svr = (float*)(shm + QS + wm * 640);
  float* const tb = (float*)(shm + DMP + wm * 2048);
  typedef __attribute__((address_space(3))) unsigned lds_u32;
  lds_u32* const cnt = (lds_u32*)(shm3 + CNT);
  unsigned vcnt = shm_base + CNT;
  asm volatile("" : "+v"(vcnt));
  auto signal = [&](unsigned v) {
    unsigned t;
    asm volatile("v_mov_b32 %0, %2\n\ts_mov_b64 exec, 1\n\tds_add_u32 %1, %0\n\ts_mov_b64 exec, -1" : "=&v"(t) : "v"(vcnt), "s"(v) : "memory");
  };
  auto await = [&](unsigned mask, unsigned val) {
    unsigned t, u;
    asm volatile("1:\n\tds_read_b32 %0, %2\n\ts_waitcnt lgkmcnt(0)\n\tv_readfirstlane_b32 %1, %0\n\ts_and_b32 %1, %1, %3\n\ts_cmp_eq_u32 %1, %4\n\t"
                 "s_cbranch_scc1 2f\n\ts_sleep 1\n\ts_branch 1b\n\t2:" : "=&v"(t), "=&s"(u) : "v"(vcnt), "s"(mask), "s"(val) : "memory", "scc");
  };
  if (grp) __builtin_amdgcn_s_setprio(1);
  signal(1u << (8 * grp));
  f32x4 rf[4];
#pragma unroll
  for (int mf = 0; mf < 4; ++mf) rf[mf] = *(const f32x4*)(shm + AUX + (wm * 64 + mf * 16 + li) * 16);
  const int sbase = ((mtile * 2 + (wm >> 1)) * 72 * 8 + (wm & 1) * 4) * 2048;
  auto putf = [&](int kg, int mf, u32x4 v) {
    __builtin_amdgcn_raw_buffer_store_b128(v, ra, ln * 16 + sbase + ((kg >> 1) * 8 + mf) * 2048 + (kg & 1) * 1024, 0, 16);
  };
  auto gelp = [&](f32x2 tg, f32x2 tu, f32x2 k12, f32x2 k22, f32x2 d2) {
    const f32x2 arg = tg * __builtin_elementwise_fma(tg * tg, k22, k12);
    const f32x2 dn = __builtin_elementwise_fma(f32x2{__builtin_amdgcn_exp2f(arg[0]), __builtin_amdgcn_exp2f(arg[1])}, d2, d2);
    const f32x2 r = (tg * tu) * f32x2{__builtin_amdgcn_rcpf(dn[0]), __builtin_amdgcn_rcpf(dn[1])};
    return pack2(r[0], r[1]);
  };
  auto gels = [&](const f32x4& g, const f32x4& u, const f32x4& k) {
    float t0, t1, t2, t3, n0, n1, n2, n3;
    asm("v_mul_f32 %0, %8, %8\n\tv_mul_f32 %1, %9, %9\n\tv_mul_f32 %2, %10, %10\n\tv_mul_f32 %3, %11, %11\n\t"
        "v_fma_f32 %0, %0, %17, %16\n\tv_fma_f32 %1, %1, %17, %16\n\tv_fma_f32 %2, %2, %17, %16\n\tv_fma_f32 %3, %3, %17, %16\n\t"
        "v_mul_f32 %0, %8, %0\n\tv_mul_f32 %1, %9, %1\n\tv_mul_f32 %2, %10, %2\n\tv_mul_f32 %3, %11, %3\n\t"
        "v_exp_f32 %0, %0\n\tv_mul_f32 %4, %8, %12\n\tv_exp_f32 %1, %1\n\tv_mul_f32 %5, %9, %13\n\t"
        "v_exp_f32 %2, %2\n\tv_mul_f32 %6, %10, %14\n\tv_exp_f32 %3, %3\n\tv_mul_f32 %7, %11, %15\n\t"
        "v_fma_f32 %0, %0, %18, %18\n\tv_fma_f32 %1, %1, %18, %18\n\tv_rcp_f32 %0, %0\n\tv_fma_f32 %2, %2, %18, %18\n\t"
        "v_rcp_f32 %1, %1\n\tv_fma_f32 %3, %3, %18, %18\n\tv_rcp_f32 %2, %2\n\tv_mul_f32 %0, %4, %0\n\t"
        "v_rcp_f32 %3, %3\n\tv_mul_f32 %1, %5, %1\n\tv_mul_f32 %2, %6, %2\n\tv_mul_f32 %3, %7, %3\n\t"
        "v_cvt_pk_bf16_f32 %0, %0, %1\n\tv_cvt_pk_bf16_f32 %1, %2, %3"
        : "=&v"(t0), "=&v"(t1), "=&v"(t2), "=&v"(t3), "=&v"(n0), "=&v"(n1), "=&v"(n2), "=&v"(n3)
        : "v"(g[0]), "v"(g[1]), "v"(g[2]), "v"(g[3]), "v"(u[0]), "v"(u[1]), "v"(u[2]), "v"(u[3]), "v"(k[1]), "v"(k[2]), "v"(k[3]));
    return u32x2{__float_as_uint(t0), __float_as_uint(t1)};
  };
  auto gel2 = [&](int p, int kg, int mf0 = 0, int mf1 = 4, int nsc = 0, bool opq = false) {
#pragma unroll
    for (int mf = mf0; mf < mf1; ++mf) {
      const f32x2 k12 = {rf[mf][1], rf[mf][1]}, k22 = {rf[mf][2], rf[mf][2]}, d2 = {rf[mf][3], rf[mf][3]};
      u32x4 o;
      if (opq) asm volatile("" : "+v"(acc[mf][p * 2]), "+v"(acc[mf][p * 2 + 1]));
      if (mf < nsc) {
        const u32x2 a = gels(acc[mf][p * 2], acc[mf][p * 2 + 2], rf[mf]), b = gels(acc[mf][p * 2 + 1], acc[mf][p * 2 + 3], rf[mf]);
        o = u32x4{a[0], a[1], b[0], b[1]};
      } else {
#pragma unroll
        for (int i = 0; i < 4; ++i) {
          const int j = i >> 1, e = (i & 1) * 2;
          const f32x4 g = acc[mf][p * 2 + j], u = acc[mf][p * 2 + 2 + j];
          o[i] = gelp(f32x2{g[e], g[e + 1]}, f32x2{u[e], u[e + 1]}, k12, k22, d2);
        }
      }
      putf(kg, mf, o);
      if (mf < nsc) SB0();
    }
  };
  auto client = [&](int l) { return (((l & 7) * 4 + (mtile & 3)) * 8) + (((mtile >> 2) * 2 + ((l >> 4) & 1)) ^ ((l & 8) ? 5 : 0)); };
  auto notify = [&](u64 lanes) {
    int l = threadIdx.x & 63;
    asm volatile("" : "+v"(l));
    const unsigned off = (client(l) * 128 + (l & 32) + ntile) * 8;
    u64 now, vn;
    asm volatile("s_memrealtime %0\n\ts_nop 0\n\ts_waitcnt lgkmcnt(0)\n\tv_mov_b64 %1, %0\n\ts_mov_b64 exec, %4\n\tglobal_store_dwordx2 %2, %1, %3 sc1\n\ts_mov_b64 exec, -1"
                 : "=&s"(now), "=&v"(vn) : "v"(off), "s"(done1), "s"(lanes) : "memory");
  };
  const u64 myh = 0xffffull << ((wm >> 1) * 16);
  const u64 mrole = (xcd & 4) ? 0x00ff00ffull : 0xff00ff00ull;
  const u64 mall = (tya || (xcd & 3) == 3) ? 0xffffffffull : mrole;
  auto arrive = [&](auto pc) {
    constexpr int part = decltype(pc)::value;
    unsigned t;
    asm volatile("v_mov_b32 %0, 1\n\ts_mov_b64 exec, 1\n\tds_add_rtn_u32 %0, %1, %0 offset:%2\n\ts_mov_b64 exec, -1\n\ts_waitcnt lgkmcnt(0)"
                 : "=&v"(t) : "v"(vcnt), "n"(12 + 4 * part) : "memory");
    if (__builtin_amdgcn_readfirstlane(t) == 3u) notify(part ? (myh & mall) << 32 : myh & mrole);
  };
  const int role = 1 - (xcd >> 2);
  const int xb = role ? xcd : xcd ^ 5, par = xb & 1;
  const int mt2 = ((xb >> 1) * 4 + (idx & 3)) * 2 + par, nt2 = idx >> 2;
  auto rsrc = [](u64 b, unsigned n) { return u32x4{(unsigned)b, (unsigned)(b >> 32) & 0xffffu, n, 0x00020000u}; };
  u32x4 rw[2];
  int wsh[2];
  unsigned vb[2][2], cokw = 0;
  auto wsetup = [&](const u16* w, int rows, int crow0, int l) {
    const unsigned vr = (wid >> 1) * 32 + (wid & 1) * 8 + ((l >> 5) & 1) * 16 + ((l >> 3) & 3), vc = (((l >> 1) & 3) ^ ((l >> 4) & 1)) * 32 + (l & 1) * 16;
    u64 bs = (u64)(uintptr_t)(wcopy + (nt2 >> 2) * 1179648 + crow0 * 256 - 1024), bf = (u64)(uintptr_t)(w + nt2 * 128) - 1024;
    asm volatile("" : "+s"(bs), "+s"(bf));
#pragma unroll
    for (int i = 0; i < 2; ++i) {
      const bool slow = ((((unsigned)bf >> 7) + i) & 7) == (cokw ? 3u : 8u);
      const int sh = slow ? 8 : 11;
      rw[i] = rsrc(slow ? bs : bf + i * 128, ((rows - 1) << sh) + 128 + 1024);
      wsh[i] = 6 + sh;
      vb[i][0] = (vr << sh) + vc + 1024;
      vb[i][1] = ((vr + 4) << sh) + (vc ^ 64);
    }
  };
#define G2_W "s_add_u32 m0, %[l], %[ow]\n\ts_lshl_b32 %[t], %[k], %[s0]\n\tbuffer_load_dwordx4 %[v0], %[w0], %[t] offen sc1 lds\n\t" \
             "buffer_load_dwordx4 %[v1], %[w0], %[t] offen offset:1024 sc1 lds\n\t" \
             "s_add_u32 m0, m0, 8192\n\ts_lshl_b32 %[t], %[k], %[s1]\n\tbuffer_load_dwordx4 %[v2], %[w1], %[t] offen sc1 lds\n\t" \
             "buffer_load_dwordx4 %[v3], %[w1], %[t] offen offset:1024 sc1 lds"
#define G2_WOP [ow] "s"(16384 + wid * 2048), [k] "s"(kt), [s0] "s"(wsh[0]), [s1] "s"(wsh[1]), [w0] "s"(rw[0]), [w1] "s"(rw[1]), \
               [v0] "v"(vb[0][0]), [v1] "v"(vb[0][1]), [v2] "v"(vb[1][0]), [v3] "v"(vb[1][1])
#define G2_A "s_add_u32 m0, %[l], %[oa]\n\ts_lshl_b32 %[t], %[k], 14\n\tbuffer_load_dwordx4 %[va], %[a], %[t] offen sc1 lds\n\t" \
             "buffer_load_dwordx4 %[va], %[a], %[t] offen offset:1024 sc1 lds\n\t"
#define G2_A2 "buffer_load_dwordx4 %[va], %[a], %[t] offen offset:2048 sc1 lds\n\tbuffer_load_dwordx4 %[va], %[a], %[t] offen offset:3072 sc1 lds\n\t"
  constexpr int PD = G2_NSTAGE - 1;
  constexpr int NY = 28;
  const int ny = (xcd & 1) ? NY - DLT : NY + DLT;
  const u32x4 ra2 = rsrc((u64)(uintptr_t)abuf + mt2 * (72 * 16384), 72 * 16384);
  unsigned va, tab, nlo, nhi, sl = shm_base + 4 * G2_STAGE;
  const int kfirst = 28 * role + 5 * par;
  int q = 3, kt = kfirst + 15;
  u64 okb = 0;
  auto adv = [&]() { ++q; kt = __builtin_amdgcn_readlane(tab, q); sl = sl == shm_base + (G2_NSTAGE - 1) * G2_STAGE ? shm_base : sl + G2_STAGE; };
  auto stage_y = [&]() {
    unsigned t;
    asm volatile(G2_A G2_A2 G2_W : [t] "=&s"(t) : [l] "s"(sl), [oa] "s"((wid ^ role) * 4096), [a] "s"(ra2), [va] "v"(va), G2_WOP : "memory", "scc", "m0");
    adv();
  };
  auto digest = [&](u64 v, u64 valid = ~0ull) {
    const u64 r = __ballot(fresh(v, t_own)) & valid;
    const unsigned hi = (unsigned)(r >> 32), lo = (unsigned)r | hi;
    okb |= __ballot(((nlo & ~lo) | (nhi & ~hi)) == 0u);
  };
  auto aload = [&](const u64* base, unsigned off) {
    asm volatile("s_nop 4\n\tglobal_load_dwordx2 v[254:255], %0, %1 sc1" :: "v"(off), "s"(base) : "memory", "v254", "v255");
    SB0();
  };
  auto aget = [&]() { u64 v; asm volatile("v_mov_b64 %0, v[254:255]" : "=v"(v) :: "memory"); return v; };
  auto snapshot = [&]() { aload(done1 + bid * 128, ln * 8); };
  auto peek = [&]() { digest(aget()); };
  auto snapshot1 = [&]() { aload(done1 + bid * 128 + role * 16, (ln & 15) * 8); };
  auto peek1 = [&]() { digest(aget(), 0xffffull << (16 * role)); };
  auto wpair = [&](int g, int i) {
    unsigned t;
    asm volatile("s_mov_b32 m0, %1\n\ts_lshl_b32 %0, %2, %3\n\tbuffer_load_dwordx4 %5, %4, %0 offen sc1 lds\n\tbuffer_load_dwordx4 %6, %4, %0 offen offset:1024 sc1 lds"
                 : "=&s"(t) : "s"(shm_base + (g + 1) * G2_STAGE + 16384 + i * 8192 + wid * 2048), "s"(kfirst + (g == 2 ? 14 : g)), "s"(wsh[i]), "s"(rw[i]),
                   "v"(vb[i][0]), "v"(vb[i][1]) : "memory", "scc", "m0");
    SB0();
  };
  auto aquad = [&](int g) {
    unsigned t;
    asm volatile(G2_A G2_A2 : [t] "=&s"(t) : [l] "s"(shm_base + (g + 1) * G2_STAGE), [oa] "s"((wid ^ role) * 4096), [a] "s"(ra2), [va] "v"(va),
                 [k] "s"(((okb >> g) & 1) ? kfirst + (g == 2 ? 14 : g) : 100) : "memory", "scc", "m0");
    SB0();
  };
  auto mid = [&]() {
    cokw = __builtin_amdgcn_readfirstlane(cnt[1] & cnt[2]);
    wsetup(wd, F_, 0, ln);
    va = ln * 16 + wid * 4096;
    {
      const int ext = ln >= NY ? 1 : 0, qq = ln - ext * DLT, so = ln - ny;
      int j0 = 5 * par, j1 = 5 - 5 * par;
      asm volatile("" : "+s"(j1));
      const int j = (qq < 8 ? j0 : (qq < 16 ? j1 : 2)) + ((unsigned)(0x92492444444444ull >> (2 * qq)) & 3);
      const int xs = ((role ^ ext) << 2) | ((unsigned)(0xfd5a80f5a0f5a0ull >> (2 * qq)) & 3);
      unsigned bits = ((0x41a8282u >> (4 * j)) & 15u) << (4 * xs), ob = (so < 16 ? 1u : 0u) << (2 * so), ty = 7 * xs + j;
      asm volatile("" : "+v"(bits), "+v"(ob), "+v"(ty));
      tab = so < 0 ? ty : so;
      nlo = ln < 8 ? bits : 0u;
      nhi = so < 0 ? bits - nlo : ob;
    }
    asm volatile("" :: "s"(rw[0]), "s"(rw[1]), "s"(wsh[0]), "s"(wsh[1]), "s"(ra2));
    asm volatile("" : "+v"(vb[0][0]), "+v"(vb[0][1]), "+v"(vb[1][0]), "+v"(vb[1][1]), "+v"(va), "+v"(tab), "+v"(nlo), "+v"(nhi));
    await(0xffffu, 0x404u);
    wpair(0, 0);
    wpair(0, 1);
    WAIT_V(4);
    arrive(std::integral_constant<int, 0>{});
    __builtin_amdgcn_s_setprio(PRB);
  };
  int tsel = ntile & 1;
  asm volatile("" : "+s"(tsel));
  if (tsel != 0) {
    if (grp == 0) gel2(0, 2 * (kt0 + he) + wc, 0, NSC, NSC);
    else gel2(0, 2 * (kt0 + he) + wc, 0, NSC);
    gel2(0, 2 * (kt0 + he) + wc, NSC, 4);
    if (grp == 0) mid();
#pragma unroll
    for (int mf = 0; mf < 4; ++mf) {
      f32x4 t[4];
#pragma unroll
      for (int i = 1; i < 4; ++i) t[i] = *(const f32x4*)(tb + i * 64 + mf * 16 + lq * 4);
      const f32x4 g = acc[mf][4], u = acc[mf][5];
      u32x2 o;
#pragma unroll
      for (int e = 0; e < 4; e += 2)
        o[e >> 1] = gelp(f32x2{g[e], g[e + 1]}, f32x2{u[e], u[e + 1]}, f32x2{t[1][e], t[1][e + 1]}, f32x2{t[2][e], t[2][e + 1]}, f32x2{t[3][e], t[3][e + 1]});
      *(u32x2*)(shm + wm * 4096 + mf * 1024 + wc * 512 + li * 32 + lq * 8) = o;
      if (mf == 1 || grp == 0) SB0();
      if (grp == 0) {
        wpair(1 + (mf >> 1), mf & 1);
        if (mf == SNP) snapshot1();
      }
    }
    signal(0x10000u << vid);
    TS1(5);
    if (grp) {
      gel2(3, 2 * (kt0 + 2 + he) + wc, 0, 4);
    } else {
#pragma unroll
      for (int mf = 0; mf < 4; ++mf) {
        gel2(3, 2 * (kt0 + 2 + he) + wc, mf, mf + 1);
        SB0();
        if (mf == 0) { WAIT_V(7 - 2 * SNP); peek1(); }
        if (mf < 3) aquad(mf);
        if (mf == 2) snapshot1();
      }
    }
    await(0x110000u << wm, 0x110000u << wm);
#pragma unroll
    for (int i = 0; i < 2; ++i) putf(2 * (kt0 + 4) + he, 2 * wc + i, __builtin_bit_cast(u32x4, tr_frag(shm3 + wm * 4096 + wc * 2048 + i * 1024 + ln * 8)));
    if (grp == 0) WAIT_V(9);
    TS1(6);
  } else {
    if (grp) __builtin_amdgcn_s_setprio(PRL0);
    float rd[4];
#pragma unroll
    for (int mf = 0; mf < 4; ++mf) rd[mf] = svr[mf * 16 + li];
    auto cvs = [&](const f32x4& a, const f32x4& b, const f32x4& r) {
      float t0, t1, t2, t3, t4, t5, t6, t7;
      asm("v_mul_f32 %0, %8, %16\n\tv_mul_f32 %4, %9, %17\n\tv_mul_f32 %1, %10, %18\n\tv_mul_f32 %5, %11, %19\n\t"
          "v_mul_f32 %2, %12, %16\n\tv_mul_f32 %6, %13, %17\n\tv_mul_f32 %3, %14, %18\n\tv_mul_f32 %7, %15, %19\n\t"
          "v_cvt_pk_bf16_f32 %0, %0, %4\n\tv_cvt_pk_bf16_f32 %1, %1, %5\n\tv_cvt_pk_bf16_f32 %2, %2, %6\n\tv_cvt_pk_bf16_f32 %3, %3, %7"
          : "=&v"(t0), "=&v"(t1), "=&v"(t2), "=&v"(t3), "=&v"(t4), "=&v"(t5), "=&v"(t6), "=&v"(t7)
          : "v"(a[0]), "v"(a[1]), "v"(a[2]), "v"(a[3]), "v"(b[0]), "v"(b[1]), "v"(b[2]), "v"(b[3]), "v"(r[0]), "v"(r[1]), "v"(r[2]), "v"(r[3]));
      return u32x4{__float_as_uint(t0), __float_as_uint(t1), __float_as_uint(t2), __float_as_uint(t3)};
    };
    auto cvp = [&](const f32x4& a, const f32x4& b) { return u32x4{pack2(a[0], a[1]), pack2(a[2], a[3]), pack2(b[0], b[1]), pack2(b[2], b[3])}; };
    auto cvk = [&](int mf, bool sc) {
      const f32x4 r4 = {rd[mf], rd[mf], rd[mf], rd[mf]};
      return sc ? cvs(acc[mf][2], acc[mf][3], r4) : cvp(acc[mf][2] * r4, acc[mf][3] * r4);
    };
    auto cvv = [&](int mf, bool sc) {
      const f32x4 r4 = *(const f32x4*)(svr + mf * 16 + lq * 4);
      return sc ? cvs(acc[mf][4], acc[mf][5], r4) : cvp(acc[mf][4] * r4, acc[mf][5] * r4);
    };
    u32x4 kx[4], vx[4], qx[4], hx[4];
    const bf16x8 ones = {0x3f80, 0x3f80, 0x3f80, 0x3f80, 0x3f80, 0x3f80, 0x3f80, 0x3f80};
    kx[2] = cvk(2, true); kx[3] = cvk(3, true);
    vx[2] = cvv(2, true); vx[3] = cvv(3, true);
    const bool giveh = !grp && wm == 3 && mtile < 7, needh = grp && wm == 0 && mtile > 0;
    char* const hptr = (char*)halo + (size_t)((needh ? mtile - 1 : mtile) * 32 + ntile + wc) * 16384;
    if (grp || wm < 3) {
      char* const ex = shm + (vid - wc) * 4096 + ln * 16;
      *(u32x4*)ex = kx[2]; *(u32x4*)(ex + 1024) = kx[3]; *(u32x4*)(ex + 2048) = vx[2]; *(u32x4*)(ex + 3072) = vx[3];
      signal(0x10000u << vid);
    } else if (giveh) {
      const __amdgpu_buffer_rsrc_t rh = __builtin_amdgcn_make_buffer_rsrc((void*)hptr, 0, 9216, 0x00020000);
      const u64 now = __builtin_amdgcn_s_memrealtime();
      const unsigned tag = (unsigned)now;
#pragma unroll
      for (int r = 0; r < 8; ++r) {
        const u32x4 w = r < 4 ? kx[2 + (r >> 1)] : vx[r >> 1];
        __builtin_amdgcn_raw_buffer_store_b128(u32x4{w[2 * (r & 1)], tag, w[2 * (r & 1) + 1], tag}, rh, ln * 16 + r * 1024, 0, 16);
      }
      __builtin_amdgcn_raw_buffer_store_b128(u32x4{tag, (unsigned)(now >> 32), tag, (unsigned)(now >> 32)}, rh, ln * 16 + 8192, 0, 16);
    }
    if (grp == 0) { gel2(3, 2 * (kt0 + 5 + he) + wc, 0, 4, NSC - 1, true); mid(); }
    kx[0] = cvk(0, false); kx[1] = cvk(1, false);
    vx[0] = cvv(0, false); vx[1] = cvv(1, false);
#pragma unroll
    for (int mf = 0; mf < 4; ++mf) qx[mf] = cvp(acc[mf][0], acc[mf][1]);
    f32x4 mk0, mk1;
#pragma unroll
    for (int j = 0; j < 4; ++j) {
      const int k0 = 4 * lq + j, k1 = 16 + 4 * lq + j;
      mk0[j] = (k0 >= li + 9 && k0 <= li + 16) ? 0.f : -1.0e30f;
      mk1[j] = (k1 >= li + 9 && k1 <= li + 16) ? 0.f : -1.0e30f;
    }
    auto max3 = [](float a, float b, float c) { float r; asm("v_max3_f32 %0, %1, %2, %3" : "=v"(r) : "v"(a), "v"(b), "v"(c)); return r; };
    auto max2 = [](float a, float b) { float r; asm("v_max_f32 %0, %1, %2" : "=v"(r) : "v"(a), "v"(b)); return r; };
    auto tiles = [&](int t0, const u32x4* kp, const u32x4* vp, bool first_rows) {
      const f32x4 z = {0.f, 0.f, 0.f, 0.f};
      const f32x4 mz = {-1.0e30f, -1.0e30f, -1.0e30f, -1.0e30f};
      f32x4 s0[2], s1[2];
#pragma unroll
      for (int n = 0; n < 2; ++n) {
        const bf16x8 qf = __builtin_bit_cast(bf16x8, qx[t0 + n]);
        s0[n] = __builtin_amdgcn_mfma_f32_16x16x32_bf16(__builtin_bit_cast(bf16x8, kp[n]), qf, first_rows ? mz : mk0, 0, 0, 0);
        s1[n] = __builtin_amdgcn_mfma_f32_16x16x32_bf16(__builtin_bit_cast(bf16x8, kx[t0 + n]), qf, mk1, 0, 0, 0);
      }
      SB0();
      if (grp == 0) { if (t0) wpair(1, 1); else aquad(1); }
      asm volatile("s_nop 7\n\ts_nop 3" : "+v"(s0[0]), "+v"(s1[0]), "+v"(s0[1]), "+v"(s1[1]));
      float mx[2];
#pragma unroll
      for (int n = 0; n < 2; ++n) mx[n] = max3(max3(s0[n][0], s0[n][1], s0[n][2]), max3(s0[n][3], s1[n][0], s1[n][1]), max2(s1[n][2], s1[n][3]));
#pragma unroll
      for (int n = 0; n < 2; ++n) { auto r = __builtin_amdgcn_permlane16_swap(__float_as_uint(mx[n]), __float_as_uint(mx[n]), false, false);
        mx[n] = max2(__uint_as_float(r[0]), __uint_as_float(r[1])); }
#pragma unroll
      for (int n = 0; n < 2; ++n) { auto r = __builtin_amdgcn_permlane32_swap(__float_as_uint(mx[n]), __float_as_uint(mx[n]), false, false);
        mx[n] = max2(__uint_as_float(r[0]), __uint_as_float(r[1])); }
      SB0();
      if (grp == 0) {
        if (t0) wpair(2, 0); else aquad(2);
        snapshot1();
      }
      constexpr float cs2 = 0.17677669529663687f * 1.4426950408889634f;
      f32x4 os[2], o0[2], o1[2];
#pragma unroll
      for (int n = 0; n < 2; ++n) {
        const float c = cs2 * rd[t0 + n], nm = -mx[n] * c;
        float e8[8];
#pragma unroll
        for (int j = 0; j < 4; ++j) {
          e8[j] = __builtin_amdgcn_exp2f(__builtin_fmaf(s0[n][j], c, nm));
          e8[4 + j] = __builtin_amdgcn_exp2f(__builtin_fmaf(s1[n][j], c, nm));
        }
        u32x4 pk;
#pragma unroll
        for (int j = 0; j < 4; ++j) pk[j] = pack2(e8[2 * j], e8[2 * j + 1]);
        const bf16x8 pf = __builtin_bit_cast(bf16x8, pk);
        const u32x4 va = {vp[n][0], vp[n][1], vx[t0 + n][0], vx[t0 + n][1]}, vb = {vp[n][2], vp[n][3], vx[t0 + n][2], vx[t0 + n][3]};
        os[n] = __builtin_amdgcn_mfma_f32_16x16x32_bf16(ones, pf, z, 0, 0, 0);
        o0[n] = __builtin_amdgcn_mfma_f32_16x16x32_bf16(__builtin_bit_cast(bf16x8, va), pf, z, 0, 0, 0);
        o1[n] = __builtin_amdgcn_mfma_f32_16x16x32_bf16(__builtin_bit_cast(bf16x8, vb), pf, z, 0, 0, 0);
      }
      if (grp == 0 && t0) { SB0(); wpair(2, 1); }
#pragma unroll
      for (int n = 0; n < 2; ++n) {
        const float inv = __builtin_amdgcn_rcpf(os[n][0]);
        const f32x4 n0 = o0[n] * inv, n1 = o1[n] * inv;
        putf(112 + ntile + wc, t0 + n, u32x4{pack2(n0[0], n0[1]), pack2(n0[2], n0[3]), pack2(n1[0], n1[1]), pack2(n1[2], n1[3])});
      }
    };
    TS1(5);
    if (grp == 0) wpair(1, 0);
    tiles(2, &kx[0], &vx[0], false);
    constexpr int HL = 131072, HS = DMP + 1024;
    auto hload = [&]() {
      asm volatile("s_mov_b32 m0, %2\n\ts_nop 0\n\tglobal_load_lds_dwordx4 %0, %1 offset:-4096 sc1\n\tglobal_load_lds_dwordx4 %0, %1 offset:-3072 sc1\n\t"
                   "global_load_lds_dwordx4 %0, %1 offset:-2048 sc1\n\tglobal_load_lds_dwordx4 %0, %1 offset:-1024 sc1\n\tglobal_load_lds_dwordx4 %0, %1 sc1\n\t"
                   "global_load_lds_dwordx4 %0, %1 offset:1024 sc1\n\tglobal_load_lds_dwordx4 %0, %1 offset:2048 sc1\n\tglobal_load_lds_dwordx4 %0, %1 offset:3072 sc1\n\t"
                   "s_mov_b32 m0, %4\n\ts_nop 0\n\tglobal_load_lds_dwordx4 %0, %3 sc1"
                   :: "v"(ln * 16), "s"(hptr + 4096), "s"(shm_base + HL + wc * 8192 + 4096), "s"(hptr + 8192), "s"(shm_base + HS + wc * 2048) : "memory", "m0");
    };
    if (needh) { await(0xffffu, 0x404u); hload(); }
    if (grp) { __builtin_amdgcn_s_setprio(PRL1); gel2(3, 2 * (kt0 + 5 + he) + wc, 0, 2, 0, true); __builtin_amdgcn_s_setprio(PRL0); }
    else {
      WAIT_V(4);
      peek1();
      aquad(0);
    }
    TS1(6);
    if (!grp || wm > 0) {
      await(0x8000u << vid, 0x8000u << vid);
#pragma unroll
      for (int i = 0; i < 4; ++i) hx[i] = *(const u32x4*)(shm + (vid - wc - 1) * 4096 + i * 1024 + ln * 16);
    } else if (!needh) {
#pragma unroll
      for (int i = 0; i < 4; ++i) hx[i] = u32x4{0u, 0u, 0u, 0u};
    } else {
      bool late = false;
      WAIT_V(2);
      for (;;) {
        const char* const hl = shm + HL + wc * 8192 + ln * 16;
        const u32x4 sp = *(const u32x4*)(shm + HS + wc * 2048 + ln * 16);
        unsigned df = 0;
#pragma unroll
        for (int i = 0; i < 4; ++i) {
          const u32x4 g0 = *(const u32x4*)(hl + i * 2048), g1 = *(const u32x4*)(hl + i * 2048 + 1024);
          df |= (g0[1] ^ sp[0]) | (g0[3] ^ sp[0]) | (g1[1] ^ sp[0]) | (g1[3] ^ sp[0]);
          hx[i] = u32x4{g0[0], g0[2], g1[0], g1[2]};
        }
        if (__builtin_expect(__all((df == 0) & fresh(((u64)sp[1] << 32) | sp[0], t_own)), 1)) break;
        late = __builtin_amdgcn_s_memrealtime() - t_own > TIMEOUT_;
        if (late) break;
        __builtin_amdgcn_s_sleep(1);
        WAIT_L(0);
        hload();
        WAIT_V(0);
      }
      if (__builtin_expect(late, 0)) {
        char* const fb = shm + HL + wc * 8192 + ln * 16;
#pragma unroll 1
        for (int i = 0; i < 32; ++i) {
          const int rec = i >> 3, e = i & 7;
          const int row = m0 - 32 + 2 * (rec < 2 ? li : lq * 4 + (e & 3)) + (rec & 1);
          const int col = (rec < 2 ? D_ + lq * 4 + (e & 3) : 2 * D_ + li) + (ntile + wc) * 32 + (e >> 2) * 16;
          const u16* xr = x + (size_t)row * D_;
          const unsigned stt = row_stats_serial(xr);
          const float mb = lo_f(stt);
          float a = 0.f;
#pragma unroll 1
          for (int k = 0; k < D_; ++k) a += (bf2f(xr[k]) - mb) * bf2f(wqkv[(size_t)k * 3072 + col]);
          *(u16*)(fb + rec * 1024 + e * 2) = f2bf(a / hi_f(stt));
        }
#pragma unroll
        for (int i = 0; i < 4; ++i) hx[i] = *(const u32x4*)(fb + i * 1024);
      }
    }
    tiles(0, &hx[0], &hx[2], grp && mtile == 0 && wm == 0);
    if (grp) { __builtin_amdgcn_s_setprio(PRL1); gel2(3, 2 * (kt0 + 5 + he) + wc, 2, 4, 0, true); }
    else WAIT_V(7);
  }
  TS1(2);
  const bool cok = cokw != 0u;
  {
  __builtin_amdgcn_sched_barrier(0);
  int tid = threadIdx.x & 255;
  asm volatile("" : "+v"(tid));
  __builtin_amdgcn_sched_barrier(0);
  const int lane = tid & 63;
  const int wm = (wid >> 1) & 1, wn = wid & 1;
  const int mtile = mt2, ntile = nt2;
  const int m0 = mtile * 128, n0 = ntile * 128;
  TS(0);
  constexpr int EP = 528;
  const int img0 = ((ny + 13) % G2_NSTAGE) * G2_STAGE, img1 = ((ny + 14) % G2_NSTAGE) * G2_STAGE;
  if constexpr (!LD) {
    const int p = wid;
    const u64* const mbox = &done1[bid * 128 + lane];
    auto stage_s = [&]() {
      unsigned t;
      asm volatile(G2_A G2_W : [t] "=&s"(t) : [l] "s"(sl), [oa] "s"((p >> 1) * 8192 + (p & 1) * 2048), [a] "s"(ra2), [va] "v"(va), G2_WOP : "memory", "scc", "m0");
      adv();
    };
    u64 ren = 0;
    auto need = [&]() {
      while (__builtin_expect(!((okb >> q) & 1), 0)) {
        digest(__hip_atomic_load(mbox, RLX_AGENT));
        if (!((okb >> q) & 1)) {
          __builtin_amdgcn_s_sleep(PSLEEP);
          if (__builtin_expect(!cok, 0)) notify(ren);
          if (GIVEUP()) break;
        }
      }
      asm volatile("" ::: "memory");
    };
    __builtin_amdgcn_s_setprio(0);
    if ((okb & 7) != 7) {
      const unsigned done = (unsigned)okb;
#pragma unroll 1
      for (q = 0; q < 3; ++q)
        if (!((done >> q) & 1)) {
          need();
          aquad(q);
        }
      if (done & 4) WAIT_V(0);
      WAIT_V(4);
    }
    TS(4);
    BARL();
    need();
    stage_y();
    WAIT_V(8);
    peek1();
    if (p == 0) snapshot();
    need();
    stage_y();
    if (p) okb = ~0ull;
    auto check = [&]() {
      if (__builtin_expect(okb != ~0ull && !(q & 1), 0)) {
        peek();
        if (okb != ~0ull) snapshot();
      }
    };
#define G2_RUN(N, A23) "s_branch 2f\n1:\n\tbuffer_load_dwordx4 %[v3], %[w1], %[t2] offen offset:1024 sc1 lds\n2:\n\t" \
      "s_add_u32 m0, %[l], %[oa]\n\ts_lshl_b32 %[t], %[k], 14\n\ts_waitcnt vmcnt(" N ")\n\ts_barrier\n\t" \
      "buffer_load_dwordx4 %[va], %[a], %[t] offen sc1 lds\n\ts_add_u32 %[q], %[q], 1\n\ts_lshl_b32 %[t2], %[k], %[s1]\n\t" \
      "buffer_load_dwordx4 %[va], %[a], %[t] offen offset:1024 sc1 lds\n\t" A23 \
      "s_add_u32 m0, %[l], %[ow]\n\ts_lshl_b32 %[t], %[k], %[s0]\n\t" \
      "buffer_load_dwordx4 %[v0], %[w0], %[t] offen sc1 lds\n\tv_readlane_b32 %[k], %[tab], %[q]\n\ts_add_u32 %[l], %[l], 0x8000\n\t" \
      "buffer_load_dwordx4 %[v1], %[w0], %[t] offen offset:1024 sc1 lds\n\ts_add_u32 m0, m0, 8192\n\ts_cmp_eq_u32 %[l], %[le]\n\ts_cselect_b32 %[l], %[lb], %[l]\n\t" \
      "buffer_load_dwordx4 %[v2], %[w1], %[t2] offen sc1 lds\n\ts_cmp_lt_u32 %[q], %[qe]\n\ts_cbranch_scc1 1b\n\t" \
      "buffer_load_dwordx4 %[v3], %[w1], %[t2] offen offset:1024 sc1 lds"
#define G2_RUNOP : [t] "=&s"(t), [t2] "=&s"(t2), [q] "+&s"(q), [k] "+&s"(kt), [l] "+&s"(sl) \
      : [oa] "s"(oa), [a] "s"(ra2), [va] "v"(va), [ow] "s"(16384 + wid * 2048), [s0] "s"(wsh[0]), [s1] "s"(wsh[1]), [w0] "s"(rw[0]), [w1] "s"(rw[1]), \
        [v0] "v"(vb[0][0]), [v1] "v"(vb[0][1]), [v2] "v"(vb[1][0]), [v3] "v"(vb[1][1]), [tab] "v"(tab), [qe] "s"(qe), \
        [lb] "s"(shm_base), [le] "s"(shm_base + G2_NSTAGE * G2_STAGE) : "memory", "scc", "m0"
    auto sync = [&](bool y) {
      if (y) WAIT_V(16); else WAIT_V(12);
      need();
      BAR();
      check();
    };
    auto upto = [&](bool y, int qe) {
#pragma unroll 1
      while (__builtin_expect(okb != ~0ull && q < qe, 0)) {
        sync(y);
        if (y) stage_y(); else stage_s();
      }
      if (q < qe) {
        unsigned t, t2;
        const int oa = y ? (p ^ role) * 4096 : (p >> 1) * 8192 + (p & 1) * 2048;
        if (y) asm volatile(G2_RUN("16", G2_A2) G2_RUNOP);
        else asm volatile(G2_RUN("12", "") G2_RUNOP);
      }
    };
    sync(true);
    ren = 0xffffffff00000000ull;
    if (p == 1) notify((myh & mall) << 32);
    stage_y();
    upto(true, 16 + 2 * p);
    sync(true);
    const u32x4 rwd[2] = {rw[0], rw[1]};
    const int wshd[2] = {wsh[0], wsh[1]};
    const unsigned vbd[4] = {vb[0][0], vb[0][1], vb[1][0], vb[1][1]};
    wsetup(wo, D_, F_, lane);
    const u32x4 rwo[2] = {rw[0], rw[1]};
    const int wsho[2] = {wsh[0], wsh[1]};
    unsigned vbo[5] = {vb[0][0], vb[0][1], vb[1][0], vb[1][1], (unsigned)(lane * 16 + (p >> 1) * 8192 + role * 4096 + (p & 1) * 2048 + (56 << 14))};
    asm volatile("" :: "s"(rwo[0]), "s"(rwo[1]), "s"(wsho[0]), "s"(wsho[1]));
    asm volatile("" : "+v"(vbo[0]), "+v"(vbo[1]), "+v"(vbo[2]), "+v"(vbo[3]), "+v"(vbo[4]));
    rw[0] = rwd[0]; rw[1] = rwd[1]; wsh[0] = wshd[0]; wsh[1] = wshd[1];
    vb[0][0] = vbd[0]; vb[0][1] = vbd[1]; vb[1][0] = vbd[2]; vb[1][1] = vbd[3];
    const char* ps = (const char*)slab + (size_t)(bid ^ 5) * 16384 + p * 4096;
    asm volatile("" : "+s"(ps));
    const u16* xp = x + (size_t)(m0 + role * 32) * D_ + n0;
    u16* op = out + (size_t)(m0 + role * 32) * D_ + n0;
    unsigned vo = ((tid >> 4) * 2 * D_ + (tid & 15) * 8) * 2, vi0 = img0 + (tid >> 4) * EP + (tid & 15) * 32, vi1 = vi0 + (img1 - img0);
    unsigned vo2 = vo + 64 * D_ * 2;
    asm volatile("" : "+s"(xp));
    asm volatile("" : "+s"(op));
    asm volatile("" : "+v"(vo), "+v"(vo2), "+v"(vi0), "+v"(vi1));
    stage_y();
    upto(true, ny);
    va = vbo[4];
    rw[0] = rwo[0]; rw[1] = rwo[1]; wsh[0] = wsho[0]; wsh[1] = wsho[1];
    vb[0][0] = vbo[0]; vb[0][1] = vbo[1]; vb[1][0] = vbo[2]; vb[1][1] = vbo[3];
    bool pok = true;
    upto(false, ny + (p == 0 ? 13 : (p == 1 ? 10 : 16)));
    if (p < 2) {
      sync(false);
      if (p == 1) {
        if (lane == 0) __hip_atomic_store(done2 + bid * 128, __builtin_amdgcn_s_memrealtime(), RLX_AGENT);
      } else {
        q += 2;
        need();
        --q;
        need();
        --q;
        aload(done2 + (bid ^ 5) * 128, 0);
      }
      stage_s();
      upto(false, ny + 15);
      WAIT_V(12);
      BAR();
      if (p == 0) pok = __all(fresh(aget(), t_own));
      stage_s();
    }
    WAIT_V(12);
    while (__builtin_expect(!pok, 0)) {
      aload(done2 + (bid ^ 5) * 128, 0);
      WAIT_V(0);
      pok = __all(fresh(aget(), t_own)) || GIVEUP();
      if (__builtin_expect(!cok, 0)) { notify(ren); if (lane == 0) __hip_atomic_store(done2 + bid * 128, __builtin_amdgcn_s_memrealtime(), RLX_AGENT); }
    }
    SB0();
    BAR();
    asm volatile("s_mov_b32 m0, %2\n\ts_nop 0\n\tglobal_load_lds_dwordx4 %0, %1 sc1\n\tglobal_load_lds_dwordx4 %0, %1 offset:1024 sc1\n\t"
                 "global_load_lds_dwordx4 %0, %1 offset:2048 sc1\n\tglobal_load_lds_dwordx4 %0, %1 offset:3072 sc1"
                 :: "v"(lane * 16), "s"(ps), "s"(sl + p * 4096) : "memory", "m0");
    SB0();
    WAIT_V(10);
    BAR();
    const __amdgpu_buffer_rsrc_t rx = __builtin_amdgcn_make_buffer_rsrc((void*)xp, 0, 96 * D_ * 2, 0x00020000);
    const __amdgpu_buffer_rsrc_t ro = __builtin_amdgcn_make_buffer_rsrc((void*)op, 0, 96 * D_ * 2, 0x00020000);
    u32x4 xv[4];
#pragma unroll
    for (int i = 0; i < 4; ++i) xv[i] = __builtin_bit_cast(u32x4, __builtin_amdgcn_raw_buffer_load_b128(rx, ((i & 1) ? vo2 : vo) + (i >> 1) * D_ * 2, 0, 0));
    WAIT_V(8);
    BAR();
    WAIT_V(4);
    BAR();
    f32x4 xf[8];
#pragma unroll
    for (int i = 0; i < 8; ++i) {
      const unsigned a = xv[i >> 1][(i & 1) * 2], b = xv[i >> 1][(i & 1) * 2 + 1];
      xf[i] = f32x4{lo_f(a), hi_f(a), lo_f(b), hi_f(b)};
      asm volatile("" : "+v"(xf[i]));
    }
#pragma unroll
    for (int h = 0; h < 2; ++h) {
      BAR();
      f32x4 t[4];
#pragma unroll
      for (int i = 0; i < 4; ++i) t[i] = *(const f32x4*)(shm + (h ? vi1 : vi0) + (i >> 1) * 16 * EP + (i & 1) * 16) + xf[h * 4 + i];
#pragma unroll
      for (int i = 0; i < 2; ++i)
        __builtin_amdgcn_raw_buffer_store_b128(u32x4{pack2(t[2 * i][0], t[2 * i][1]), pack2(t[2 * i][2], t[2 * i][3]), pack2(t[2 * i + 1][0], t[2 * i + 1][1]),
                                                     pack2(t[2 * i + 1][2], t[2 * i + 1][3])}, ro, (i ? vo2 : vo) + h * D_ * 2, 0, 2);
    }
    TS(5);
    if (tid == 0) __hip_atomic_store(&acks[bid], __builtin_amdgcn_s_memrealtime(), RLX_AGENT);
    if (__builtin_expect(!cok, 0) && p == 0) {
      const bool mine = lane <= 32;
      const u64* const pa = &acks[lane < 32 ? client(lane) : bid ^ 5];
      while (!__all(!mine || fresh(__hip_atomic_load(pa, RLX_AGENT), t_own))) {
        notify(ren);
        if (lane == 0) __hip_atomic_store(done2 + bid * 128, __builtin_amdgcn_s_memrealtime(), RLX_AGENT);
        __builtin_amdgcn_s_sleep(32);
        if (GIVEUP()) break;
      }
    }
  } else {
    TS(6);
    f32x4 acc[4][4], sacc[2][4];
    const int fr = lane & 15, fq = lane >> 4, q4 = fr >> 2, pp = lane & 3;
    const int ff = ((q4 >> 1) & 1) | ((fq & 1) << 1);
    int aof = G2_STAGE + wm * 8192 + lane * 16;
    int boff[4];
#pragma unroll
    for (int j = 0; j < 4; ++j) boff[j] = G2_STAGE + 16384 + wn * 8192 + (8 * fq + q4) * 128 + ((j ^ ff) * 32) + pp * 8;
#pragma unroll
    for (int b = 0; b < 4; ++b) {
#pragma unroll
      for (int a = 0; a < 4; ++a) acc[a][b] = f32x4{0.f, 0.f, 0.f, 0.f};
      sacc[0][b] = f32x4{0.f, 0.f, 0.f, 0.f}; sacc[1][b] = f32x4{0.f, 0.f, 0.f, 0.f};
    }
#define SB() __builtin_amdgcn_sched_barrier(0)
    struct Fr { bf16x8 a[4]; bf16x4 bl[4], bh[4]; };
    Fr f0[2], f1[2];
    auto rd = [&](Fr& f, lds_cptr sb, int ks, int t, bool sp) {
      constexpr int ord[12] = {0, 1, 8, 2, 3, 4, 5, 6, 7, 9, 10, 11};
      const int o = ord[t];
      if (o >= 8) f.a[o - 8] = *(const __attribute__((address_space(3))) bf16x8*)(sb + aof + (o - 8) * 2048 + ks * 1024);
      else if (o & 1) f.bh[o >> 1] = tr_read(sb + boff[o >> 1] + ks * 4096 + 512);
      else f.bl[o >> 1] = tr_read(sb + boff[o >> 1] + ks * 4096);
    };
    auto bfr = [&](const Fr& c, int j) { return __builtin_shufflevector(c.bl[j], c.bh[j], 0, 1, 2, 3, 4, 5, 6, 7); };
    int so = G2_STAGE;
    auto ystg = [&](const Fr* c, Fr* n, bool last) {
      int d = 0;
      sfor<32>([&](auto uc) __attribute__((always_inline)) {
        constexpr int u = decltype(uc)::value, t = u >> 1, mf = t >> 2, j = (t & 4) ? 3 - (t & 3) : t & 3, ks = (u ^ t) & 1;
        SB();
        asm volatile("v_mfma_f32_16x16x32_bf16 %0, %1, %2, %0" : "+v"(acc[mf][j]) : "v"(bfr(c[ks], j)), "v"(c[ks].a[mf]));
        SB();
        const int h = last ? 10 : 12;
        if (u < 2 * h) rd(n[u / h], shm3, u / h, u % h, true);
        if (u == 24) { d = so == (G2_NSTAGE - 1) * G2_STAGE ? -(G2_NSTAGE - 1) * G2_STAGE : G2_STAGE; asm volatile("" : "+s"(d)); }
        if (u == 25) { so += d; aof += d; asm volatile("" : "+v"(aof)); asm volatile("" : "+s"(so)); }
        if constexpr (u >= 26 && u < 30) { boff[u - 26] += d; asm volatile("" : "+v"(boff[u - 26])); }
      });
      SB();
      __builtin_amdgcn_s_waitcnt(0xC07F);
    };
    auto stg = [&](const Fr* c, Fr* n) {
      sfor<16>([&](auto uc) __attribute__((always_inline)) {
        constexpr int u = decltype(uc)::value, t = u >> 1, mf = t >> 2, j = (t & 4) ? 3 - (t & 3) : t & 3, ks = (u ^ t) & 1;
        SB();
        asm volatile("v_mfma_f32_16x16x32_bf16 %0, %1, %2, %0" : "+v"(sacc[mf][j]) : "v"(bfr(c[ks], j)), "v"(c[ks].a[mf]));
        SB();
        if (u < 10) { rd(n[0], shm3, 0, u, true); rd(n[1], shm3, 1, u, true); }
      });
      SB();
      __builtin_amdgcn_s_waitcnt(0xC07F);
    };
    asm volatile("" : "+v"(aof), "+v"(boff[0]), "+v"(boff[1]), "+v"(boff[2]), "+v"(boff[3]));
    asm volatile("" : "+v"(sacc[0][0]), "+v"(sacc[0][1]), "+v"(sacc[0][2]), "+v"(sacc[0][3]), "+v"(sacc[1][0]), "+v"(sacc[1][1]), "+v"(sacc[1][2]), "+v"(sacc[1][3]));
#pragma unroll
    for (int a = 0; a < 4; ++a) asm volatile("" : "+v"(acc[a][0]), "+v"(acc[a][1]), "+v"(acc[a][2]), "+v"(acc[a][3]));
    WAIT_V(0);
    arrive(std::integral_constant<int, 1>{});
    BARL();
    TS(7);
    __builtin_amdgcn_s_waitcnt(0xC07F);
    SB();
#pragma unroll
    for (int t = 0; t < 24; ++t) rd(f0[t & 1], shm3, t & 1, t >> 1, false);
    auto nxt = [&]() {
      const int d = so == (G2_NSTAGE - 1) * G2_STAGE ? -(G2_NSTAGE - 1) * G2_STAGE : G2_STAGE;
      so += d; aof += d; boff[0] += d; boff[1] += d; boff[2] += d; boff[3] += d;
      asm volatile("" : "+v"(aof), "+v"(boff[0]), "+v"(boff[1]), "+v"(boff[2]), "+v"(boff[3]));
    };
    nxt();
    __builtin_amdgcn_s_waitcnt(0xC07F);
#pragma unroll 1
    for (int g = 0; g < ny - 2; g += 2) {
      ystg(f0, f1, false);
      BAR();
      ystg(f1, f0, false);
      BAR();
    }
    if (!(DLT & 1)) { ystg(f0, f1, false); BAR(); }
    Fr sa[2], sb2[2];
    ystg((DLT & 1) ? f0 : f1, sa, true);
    asm volatile("" : "+v"(acc[0][0]), "+v"(acc[0][1]), "+v"(acc[0][2]), "+v"(acc[0][3]), "+v"(acc[1][0]), "+v"(acc[1][1]), "+v"(acc[1][2]), "+v"(acc[1][3]));
    TS(2);
    {
      const __amdgpu_buffer_rsrc_t rs = __builtin_amdgcn_make_buffer_rsrc((void*)((char*)slab + (size_t)bid * 16384), 0, 16384, 0x00020000);
#pragma unroll
      for (int f = 0; f < 4; ++f) {
        if (f) nxt();
        BAR();
        if (f & 1) stg(sb2, sa); else stg(sa, sb2);
        asm volatile("" : "+v"(acc[2 + (f >> 1)][(f & 1) * 2]), "+v"(acc[2 + (f >> 1)][(f & 1) * 2 + 1]));
        const f32x4 a = acc[2 + (f >> 1)][(f & 1) * 2], b = acc[2 + (f >> 1)][(f & 1) * 2 + 1];
        __builtin_amdgcn_raw_buffer_store_b128(u32x4{pack2(a[0], a[1]), pack2(a[2], a[3]), pack2(b[0], b[1]), pack2(b[2], b[3])}, rs,
                                               (f * 256 + tid) * 16, 0, 16);
        SB();
      }
    }
#pragma unroll 1
    for (int i = 2; i < 7; ++i) {
      nxt();
      BAR();
      stg(sa, sb2);
      nxt();
      BAR();
      stg(sb2, sa);
      WAIT_V(0);
    }
    nxt();
    BAR();
    stg(sa, sb2);
    int vi0 = img0 + (wm * 16 + fr) * EP + wn * 256 + fq * 16, vi1 = vi0 + (img1 - img0);
    asm volatile("" : "+v"(vi0), "+v"(vi1));
    BAR();
    u32x4 pv[4];
    {
      nxt();
      lds_cptr sb = shm3 + so + tid * 16;
#pragma unroll
      for (int f = 0; f < 4; ++f) pv[f] = *(const __attribute__((address_space(3))) u32x4*)(sb + f * 4096);
    }
    f32x4 pf[8];
    sfor<16>([&](auto uc) __attribute__((always_inline)) {
      constexpr int u = decltype(uc)::value, t = u >> 1, mf = t >> 2, j = (t & 4) ? 3 - (t & 3) : t & 3, ks = (u ^ t) & 1;
      SB();
      asm volatile("v_mfma_f32_16x16x32_bf16 %0, %1, %2, %0" : "+v"(sacc[mf][j]) : "v"(bfr(sb2[ks], j)), "v"(sb2[ks].a[mf]));
      SB();
      if constexpr (u >= 8) {
        const unsigned w0 = pv[(u - 8) >> 1][(u & 1) * 2], w1 = pv[(u - 8) >> 1][(u & 1) * 2 + 1];
        pf[u - 8] = f32x4{lo_f(w0), hi_f(w0), lo_f(w1), hi_f(w1)};
        asm volatile("" : "+v"(pf[u - 8]));
      }
    });
    SB();
    TS(1);
#pragma unroll
    for (int mf = 0; mf < 2; ++mf) {
      f32x4 ya[4], e[4];
#pragma unroll
      for (int j = 0; j < 4; ++j) { ya[j] = acc[mf][j] + pf[mf * 4 + j]; e[j] = ya[j] * -1.4426950408889634f; }
      SB();
#pragma unroll
      for (int j = 0; j < 4; ++j) {
        e[j] = f32x4{__builtin_amdgcn_exp2f(e[j][0]), __builtin_amdgcn_exp2f(e[j][1]), __builtin_amdgcn_exp2f(e[j][2]), __builtin_amdgcn_exp2f(e[j][3])};
        ya[j] *= sacc[mf][j];
      }
      SB();
#pragma unroll
      for (int j = 0; j < 4; ++j) e[j] += 1.0f;
      SB();
#pragma unroll
      for (int j = 0; j < 4; ++j)
        *(f32x4*)(shm + (mf ? vi1 : vi0) + j * 64) =
            ya[j] * f32x4{__builtin_amdgcn_rcpf(e[j][0]), __builtin_amdgcn_rcpf(e[j][1]), __builtin_amdgcn_rcpf(e[j][2]), __builtin_amdgcn_rcpf(e[j][3])};
      SB();
      BARL();
      SB();
    }
    TS(3);
  }
  TSFLUSH();
  }
  };
  if (wid < 4) kloop(std::integral_constant<int, 0>{});
  else kloop(std::integral_constant<int, 1>{});
  asm volatile(".Lcode_end:");
}

extern "C" void kernel_launch(void* const* d_in, const int* in_sizes, int n_in, void* d_out, int out_size, void* d_ws,
                              size_t ws_size, hipStream_t stream) {
  const u16* A_x = (const u16*)d_in[0];
  const u16* A_wqkv = (const u16*)d_in[1];
  const u16* A_wo = (const u16*)d_in[2];
  const u16* B_wg = (const u16*)d_in[4];
  const u16* B_wu = (const u16*)d_in[5];
  const u16* B_wd = (const u16*)d_in[6];
  u16* out = (u16*)d_out;
  fused_kernel<<<256, 512, 0, stream>>>(A_x, A_wqkv, B_wg, B_wu, B_wd, A_wo, out, (char*)d_ws);
}
```

```cpp
#include <hip/hip_runtime.h>
#include <hip/hip_bf16.h>
#include <stdint.h>
#include <type_traits>
#include <utility>

typedef unsigned short u16;
using bf16x8 = __attribute__((ext_vector_type(8))) short;
using bf16x4 = __attribute__((ext_vector_type(4))) short;
using f32x4 = __attribute__((ext_vector_type(4))) float;
using u32x2 = __attribute__((ext_vector_type(2))) unsigned;
using u32x4 = __attribute__((ext_vector_type(4))) unsigned;
typedef __attribute__((address_space(3))) const char* lds_cptr;

template <class F, int... I> __device__ __forceinline__ void sfor_(F&& f, std::integer_sequence<int, I...>) { (f(std::integral_constant<int, I>{}), ...); }
template <int N, class F> __device__ __forceinline__ void sfor(F&& f) { sfor_(f, std::make_integer_sequence<int, N>{}); }

constexpr int D_ = 1024, F_ = 3584;

__device__ __forceinline__ float bf2f(u16 h) { return __uint_as_float((unsigned)h << 16); }
__device__ __forceinline__ u16 f2bf(float f) {
  unsigned u = __float_as_uint(f);
  u += 0x7fffu + ((u >> 16) & 1u);
  return (u16)(u >> 16);
}
__device__ __forceinline__ float rbf(float f) { return bf2f(f2bf(f)); }
typedef __attribute__((ext_vector_type(2))) __bf16 bf16x2_t;
typedef __attribute__((ext_vector_type(2))) float f32x2;
__device__ __forceinline__ unsigned pack2(float a, float b) {
  bf16x2_t r = __builtin_convertvector((f32x2){a, b}, bf16x2_t);
  return __builtin_bit_cast(unsigned, r);
}
__device__ __forceinline__ float lo_f(unsigned p) { return __uint_as_float(p << 16); }
__device__ __forceinline__ float hi_f(unsigned p) { return __uint_as_float(p & 0xffff0000u); }

#define TSDECL
#define TSS(i) do {} while (0)
#define TSFLUSH() do {} while (0)
#define TS1(i) TSS(i)
#define TS(i) TSS(8 + (i))
#define WAIT_V(n) asm volatile("s_waitcnt vmcnt(%0)" ::"n"(n) : "memory")
#define WAIT_L(n) asm volatile("s_waitcnt lgkmcnt(%0)" ::"n"(n) : "memory")
#define BARL() do { WAIT_L(0); BAR(); } while (0)
#define BAR() do { asm volatile("" ::: "memory"); __builtin_amdgcn_s_barrier(); asm volatile("" ::: "memory"); } while (0)

__device__ __forceinline__ bf16x4 tr_read(lds_cptr p) {
  return __builtin_amdgcn_ds_read_tr16_b64_v4i16((__attribute__((address_space(3))) bf16x4*)p);
}
__device__ __forceinline__ bf16x8 tr_frag(lds_cptr p) {
  bf16x4 lo = tr_read(p), hi = tr_read(p + 512);
  bf16x8 r;
  r[0] = lo[0]; r[1] = lo[1]; r[2] = lo[2]; r[3] = lo[3];
  r[4] = hi[0]; r[5] = hi[1]; r[6] = hi[2]; r[7] = hi[3];
  return r;
}
__device__ __forceinline__ void glds16(const void* gsrc, unsigned lds_dst) {
  unsigned keep;
  asm volatile("s_mov_b32 %0, m0\n\ts_mov_b32 m0, %2\n\ts_nop 0\n\tglobal_load_lds_dwordx4 %1, off\n\ts_mov_b32 m0, %0"
               : "=&s"(keep) : "v"(gsrc), "s"(lds_dst) : "memory");
}
#define GLDS(gptr, laddr) glds16((const void*)(gptr), (unsigned)(laddr))
__device__ __forceinline__ void glds16_sc1(const void* gsrc, unsigned lds_dst) {
  unsigned keep;
  asm volatile("s_mov_b32 %0, m0\n\ts_mov_b32 m0, %2\n\ts_nop 0\n\tglobal_load_lds_dwordx4 %1, off sc1\n\ts_mov_b32 m0, %0"
               : "=&s"(keep) : "v"(gsrc), "s"(lds_dst) : "memory");
}
#define GLDS_SC1(gptr, laddr) glds16_sc1((const void*)(gptr), (unsigned)(laddr))
__device__ __forceinline__ void glds16_nc(const void* gsrc, unsigned lds_dst) {
  unsigned keep;
  asm volatile("s_mov_b32 %0, m0\n\ts_mov_b32 m0, %2\n\ts_nop 0\n\tglobal_load_lds_dwordx4 %1, off\n\ts_mov_b32 m0, %0"
               : "=&s"(keep) : "v"(gsrc), "s"(lds_dst));
}
#define GLDS_NC(gptr, laddr) glds16_nc((const void*)(gptr), (unsigned)(laddr))
__device__ __forceinline__ void glds16_s(const void* sbase, unsigned voff, unsigned lds_dst) {
  asm volatile("s_mov_b32 m0, %2\n\ts_nop 0\n\tglobal_load_lds_dwordx4 %0, %1" :: "v"(voff), "s"(sbase), "s"(lds_dst) : "m0");
}

__device__ __forceinline__ void glds16_p(const void* sbase, unsigned voff, unsigned lds_dst) {
  asm volatile("s_mov_b32 m0, %2\n\ts_nop 3\n\tglobal_load_lds_dwordx4 %0, %1" :: "v"(voff), "s"(sbase), "s"(lds_dst) : "m0");
}

typedef unsigned long long u64;
__device__ __forceinline__ bool fresh(u64 stamp, u64 t_own) {
  return stamp - t_own - 100ull < __builtin_amdgcn_s_memrealtime() - t_own;
}
constexpr u64 TIMEOUT_ = 200000;
#define GIVEUP() (__builtin_amdgcn_s_memrealtime() - t_own > 100000000ull)
__device__ __forceinline__ float half_sum(float v) {
  v += __builtin_bit_cast(float, __builtin_amdgcn_update_dpp(0, __builtin_bit_cast(int, v), 0xB1, 0xf, 0xf, false));
  v += __builtin_bit_cast(float, __builtin_amdgcn_update_dpp(0, __builtin_bit_cast(int, v), 0x4E, 0xf, 0xf, false));
  v += __builtin_bit_cast(float, __builtin_amdgcn_update_dpp(0, __builtin_bit_cast(int, v), 0x141, 0xf, 0xf, false));
  v += __builtin_bit_cast(float, __builtin_amdgcn_update_dpp(0, __builtin_bit_cast(int, v), 0x140, 0xf, 0xf, false));
  { auto r = __builtin_amdgcn_permlane16_swap(__float_as_uint(v), __float_as_uint(v), false, false); v = __uint_as_float(r[0]) + __uint_as_float(r[1]); }
  return v;
}
__device__ __forceinline__ unsigned pack_stats(float mean, float var) {
  return (unsigned)f2bf(mean) | ((unsigned)f2bf(sqrtf(rbf(rbf(var) + 1e-5f))) << 16);
}
__device__ __forceinline__ unsigned row_stats_serial(const u16* xr) {
  float s = 0.f, q = 0.f;
#pragma unroll 1
  for (int k = 0; k < D_; ++k) s += bf2f(xr[k]);
  const float mean = s * (1.0f / D_);
#pragma unroll 1
  for (int k = 0; k < D_; ++k) { const float d = bf2f(xr[k]) - mean; q += d * d; }
  return pack_stats(mean, q * (1.0f / D_));
}
constexpr int G1_STAGE = 32768 + 5 * 8192;
constexpr int CP_K0 = 3;
constexpr int PUB_K = 5;
constexpr int PF_K0 = 6;
constexpr int SD = 6, KS0 = 2;
constexpr int NP1 = 8;
constexpr int NSC = 2;
constexpr int DLT = 1;
static_assert(DLT <= 3, "");

#define RLX_AGENT __ATOMIC_RELAXED, __HIP_MEMORY_SCOPE_AGENT

constexpr int G2_STAGE = 16384 + 2 * 8192;
constexpr int G2_NSTAGE = 5;
constexpr size_t MB_ = 1024 * 1024;
__global__ __launch_bounds__(512, 2) __attribute__((amdgpu_num_vgpr(127))) void fused_kernel(const u16* __restrict__ x, const u16* __restrict__ wqkv,
                                                       const u16* __restrict__ wg, const u16* __restrict__ wu,
                                                       const u16* __restrict__ wd, const u16* __restrict__ wo,
                                                       u16* __restrict__ out, char* __restrict__ ws) {
  u16* const halo = (u16*)(ws + 4 * MB_);
  u16* const abuf = (u16*)(ws + 16 * MB_);
  char* const slab = ws + 64 * MB_;
  unsigned* const flags = (unsigned*)(ws + 96 * MB_);
  u32x4* const rstat = (u32x4*)(ws + 97 * MB_);
  char* const wcopy = ws + 104 * MB_;
  u64* const done1 = (u64*)(ws + 102 * MB_ + 512);
  u64* const done2 = (u64*)(ws + 102 * MB_);
  u64* const donec = (u64*)(ws + 100 * MB_ + 196608);
  u64* const acks = (u64*)(ws + 100 * MB_ + 262144);
  u64* const cstamp = (u64*)(ws + 100 * MB_ + 327680);
  float* const csum = (float*)(ws + 101 * MB_);
  __shared__ __attribute__((aligned(1024))) char shm[163840];
  constexpr int AUX = 2 * G1_STAGE + 8192;
  {
    u64 pc; int len;
    asm volatile("s_getpc_b64 %0\n.Lcode_beg:\n\ts_mov_b32 %1, .Lcode_end-.Lcode_beg" : "=s"(pc), "=s"(len));
    const int ol = ((blockIdx.x >> 3) + 32 * (threadIdx.x >> 6)) * 1024 + (threadIdx.x & 63) * 16;
    asm volatile("s_mov_b32 m0, %2\n\ts_nop 0\n\tglobal_load_lds_dwordx4 %0, %1"
                 :: "v"(ol < len - 16 ? ol : 0), "s"(pc & ~15ull), "s"((unsigned)(uintptr_t)(lds_cptr)shm + 2 * G1_STAGE + 7168) : "m0");
    __builtin_amdgcn_sched_barrier(0);
  }
  const u64 t_own = __builtin_amdgcn_s_memrealtime();
  __builtin_amdgcn_sched_barrier(0);
  TSDECL;
  const int tid = threadIdx.x, lane = tid & 63;
  const int wid = __builtin_amdgcn_readfirstlane(tid >> 6);
  const int bid = blockIdx.x, xcd = bid & 7, idx = bid >> 3;
  const int ntile = xcd * 4 + (idx & 3), mtile = idx >> 2;
  const int m0 = mtile * 256;
  const int hg = ntile >> 1;
  const bool tya = (ntile & 1) == 0;
  const int grp = wid >> 2, wc = (wid >> 1) & 1, wm = (((grp ^ ntile ^ 1) & 1) << 1) | (wid & 1), vid = wc * 4 + wm;
  const int srow = m0 + ntile * 8 + (wid & 3) * 2;
  lds_cptr shm3 = (lds_cptr)shm;
  const unsigned shm_base = (unsigned)(uintptr_t)shm3;
  const int r8 = tid >> 3, pos = tid & 7;
  const int phys = pos >> 1, half = pos & 1;
  const int fk = ((r8 >> 1) & 1) | (((r8 >> 3) & 1) << 1);
  const int vsrc = phys ^ fk;
  const int qstr = tya ? 3072 * 2 : F_ * 2;
  unsigned voa = (((r8 & 32) + 2 * (r8 & 15) + ((r8 >> 4) & 1)) * D_ + ((pos ^ (r8 & 7)) * 8)) * 2;
  unsigned vog = r8 * (F_ * 2) + vsrc * 32 + half * 16, voq = r8 * qstr + vsrc * 32 + half * 16;
  unsigned vom = r8 * qstr + (vsrc >> (tya ? 0 : 1)) * 32 + half * 16;
  asm volatile("" : "+v"(voa), "+v"(vog), "+v"(voq), "+v"(vom));
  asm volatile("" :: "s"(x), "s"(wqkv), "s"(wg), "s"(wu), "s"(wd), "s"(wo), "s"(out), "s"(ws));
  const char* const ab = (const char*)(x + (size_t)m0 * D_);
  int kz = 0;
  asm volatile("" : "+s"(kz));
  auto stage_a = [&](int buf, int kt, int i, int h) {
    glds16_s(ab + (long)(((i * 64 + (h - 1) * 32) * D_ + kt * 64) * 2), voa, shm_base + buf * G1_STAGE + (wid - 4 + 4 * h) * 1024 + i * 8192);
  };
#pragma unroll
  for (int i = 0; i < 4; ++i) stage_a(0, 0, i, 1);
  const int kt0 = (hg >> 1) * 7, he = hg & 1;
  const char* const gb = (const char*)(wg + (kt0 + (tya ? 5 + he : 2 + he)) * 64);
  const char* const qb = tya ? (const char*)(wqkv + 64 * hg) : gb - 256;
  const long dgu = (const char*)wu - (const char*)wg, dp1 = tya ? 2048 : dgu;
  const char* const mb0 = tya ? qb + 4096 : (const char*)(wg + (kt0 + 4) * 64 + 32 * he);
  const char* mptr = mb0 + (((vsrc & 1) && !tya) ? dgu : 0) + vom;
  auto wload = [&](int p, int rows, unsigned dst) {
    long off = (long)(rows * (p < 3 ? qstr : F_ * 2)) + (p == 1 ? dp1 : (p == 4 ? dgu : 0));
    if (p == 2) { asm volatile("" : "+s"(off)); GLDS_NC(mptr + off, dst); }
    else glds16_s((p < 2 ? qb : gb) + off, p < 2 ? voq : vog, dst);
  };
#pragma unroll
  for (int i = 0; i < 5; ++i) wload(i, 0, shm_base + wid * 1024 + 32768 + i * 8192);
  __builtin_amdgcn_sched_barrier(0);
  const int fr = lane & 15, fq = lane >> 4, q4 = fr >> 2, pp = lane & 3;
  const int ff = ((q4 >> 1) & 1) | ((fq & 1) << 1);
  int aoff[2], boff[2];
#pragma unroll
  for (int ks = 0; ks < 2; ++ks) aoff[ks] = (wm * 64 + fr) * 128 + (((ks * 4 + fq) ^ (fr & 7)) * 16);
#pragma unroll
  for (int j = 0; j < 2; ++j) boff[j] = 32768 + (8 * fq + q4) * 128 + (((2 * wc + j) ^ ff) * 32) + pp * 8;

  f32x4 acc[4][10];
#pragma unroll
  for (int a = 0; a < 4; ++a)
#pragma unroll
    for (int b = 0; b < 10; ++b) acc[a][b] = f32x4{0.f, 0.f, 0.f, 0.f};

  auto stage_part = [&](int buf, int kt, int i, int h) {
    if (i < 5) wload(i, kt * 64 + (h - 1) * 32, shm_base + buf * G1_STAGE + (wid - 4 + 4 * h) * 1024 + 32768 + i * 8192);
    else stage_a(buf, kt, i - 5, h);
  };
  constexpr int NT = D_ / 64;
  constexpr int DMP = 2 * G1_STAGE;
  constexpr int CF = AUX + 4096, QS = CF + 1280, CNT = CF + 3840;
  bf16x8 af0[4], af1[4];
  bf16x4 bl[10], bh[10];
#pragma unroll
  for (int i = 0; i < 4; ++i) { af1[i] = bf16x8{0, 0, 0, 0, 0, 0, 0, 0}; bl[i] = bf16x4{0, 0, 0, 0}; bh[i] = bf16x4{0, 0, 0, 0}; }
#define SB0() __builtin_amdgcn_sched_barrier(0)
  auto mm = [&](int n, int f, int ks, int mf) {
    const bf16x8 b = __builtin_shufflevector(bl[n % 10], bh[n % 10], 0, 1, 2, 3, 4, 5, 6, 7);
    if ((f >> 1) == 2) acc[mf][f] = __builtin_amdgcn_mfma_f32_16x16x32_bf16(ks ? af1[mf] : af0[mf], b, acc[mf][f], 0, 0, 0);
    else acc[mf][f] = __builtin_amdgcn_mfma_f32_16x16x32_bf16(b, ks ? af1[mf] : af0[mf], acc[mf][f], 0, 0, 0);
  };
  auto correct = [&]() {
    int l = threadIdx.x & 63;
    asm volatile("s_nop 7" : "+v"(l));
    const float* const tb = (const float*)(shm + DMP + wm * 2048);
    const float* const cf = (const float*)(shm + CF) + wc * 160;
    auto ld = [&](int f) { return f < 8 ? *(const f32x4*)(cf + (f < 4 ? f : f + 2) * 16 + (l >> 4) * 4) : *(const f32x4*)(tb + (f - 8) * 16 + (l >> 4) * 4); };
    float m[4];
    f32x4 cr[3];
#pragma unroll
    for (int mf = 0; mf < 4; ++mf) m[mf] = tb[mf * 16 + (l & 15)];
    cr[0] = ld(0);
    cr[1] = ld(1);
    const float c4 = cf[64 + (l & 15)], c5 = cf[80 + (l & 15)];
#pragma unroll
    for (int f = 0; f < 12; ++f) {
      if (f + 2 < 12) cr[(f + 2) % 3] = ld(f + 2);
      SB0();
      const f32x4 c = cr[f % 3];
#pragma unroll
      for (int i = 0; i < (f < 8 ? 4 : 2); ++i) {
        f32x4& a = f < 8 ? acc[i][f < 4 ? f : f + 2] : acc[f - 8][4 + i];
        float a0 = a[0], a1 = a[1], a2 = a[2], a3 = a[3];
        asm volatile("v_fmac_f32 %0, %4, %5\n\tv_fmac_f32 %1, %4, %6\n\tv_fmac_f32 %2, %4, %7\n\tv_fmac_f32 %3, %4, %8"
                     : "+v"(a0), "+v"(a1), "+v"(a2), "+v"(a3) : "v"(f < 8 ? m[i] : (i ? c5 : c4)), "v"(c[0]), "v"(c[1]), "v"(c[2]), "v"(c[3]));
        a = f32x4{a0, a1, a2, a3};
      }
      SB0();
    }
    asm volatile("s_nop 1");
  };
  auto kloop = [&](auto wmc) __attribute__((always_inline)) {
    constexpr bool LD = decltype(wmc)::value != 0;
    constexpr int grp = LD ? 1 : 0;
    const char* pa = nullptr;
    unsigned vob = 0;
    f32x4 ca = {0.f, 0.f, 0.f, 0.f}, cb = {0.f, 0.f, 0.f, 0.f};
    bf16x8 sel = {0, 0, 0, 0, 0, 0, 0, 0};
    int tro = 0;
    u64 tg = 0;
    const int psh = (wid == 2 && !tya) ? 1 : 0, pstr = wid < 3 ? qstr : F_ * 2;
    const char* const pbase = wid == 0 ? qb : (wid == 1 ? qb + dp1 : (wid == 2 ? mb0 : gb));
    if (!LD) {
      const int rs = mtile * 8 + (lane >> 3), vs = ((lane & 7) >> 1) ^ (((rs >> 1) & 1) | ((mtile & 1) << 1));
      pa = pbase + ((psh & vs) ? dgu : 0) + (rs * pstr + (vs >> psh) * 32 + (lane & 1) * 16);
      vob = rs * (F_ * 2) + vs * 32 + (lane & 1) * 16;
      const short one = fr == fq ? 0x3f80 : 0;
      sel = bf16x8{one, one, one, one, one, one, one, one};
      tro = q4 * 128 + ((fq ^ ((q4 >> 1) | ((mtile & 1) << 1))) * 32) + pp * 8;
      asm volatile("" : "+v"(sel), "+v"(tro), "+v"(ca), "+v"(cb));
    }
    auto share = [&](int t, unsigned dsta, unsigned dstb) {
      long off = (long)(t * 64 * pstr);
      asm volatile("" : "+s"(off));
      GLDS_NC(pa + off, shm_base + dsta);
      if (wid == 0) glds16_p(gb + dgu + (long)(t * 64 * F_ * 2), vob, shm_base + dstb);
    };
    auto accum = [&](int oa, int ob) {
      ca = __builtin_amdgcn_mfma_f32_16x16x32_bf16(tr_frag(shm3 + tro + oa), sel, ca, 0, 0, 0);
      if (wid == 0) cb = __builtin_amdgcn_mfma_f32_16x16x32_bf16(tr_frag(shm3 + tro + ob), sel, cb, 0, 0, 0);
    };
    auto cgather = [&]() {
      int l = threadIdx.x & 63;
      asm volatile("s_memrealtime %0\n\ts_waitcnt lgkmcnt(0)" : "=s"(tg), "+v"(l) :: "memory");
      const char* const p4 = l < 32 ? (const char*)(csum + (ntile * 5 + 4) * 512 + wid * 16) + (l >> 2) * 256
                                    : (const char*)(cstamp + (ntile * 4 + (l < 36 ? wid : 0)) * 8);
      if (l < 40) GLDS_SC1(p4 + (l & 3) * 16, shm_base + QS + wid * 640);
#pragma unroll
      for (int i = 0; i < 2; ++i) GLDS_SC1((const char*)(csum + (ntile * 5 + wid) * 512) + i * 1024 + l * 16, shm_base + DMP + wid * 2048 + i * 1024);
    };
    if (!LD) share(2, DMP + 7168, DMP + 7168);
    if (LD) {
#pragma unroll
      for (int i = 0; i < NP1; ++i) {
        stage_part(1, kz + 1, i >> 1, i & 1);
        asm volatile("" : "+v"(acc[0][i]), "+v"(acc[1][i]), "+v"(acc[2][i]), "+v"(acc[3][i]));
      }
    }
#pragma unroll
    for (int a = 0; a < 4; ++a)
      asm volatile("" : "+v"(acc[a][0]), "+v"(acc[a][1]), "+v"(acc[a][2]), "+v"(acc[a][3]), "+v"(acc[a][4]), "+v"(acc[a][5]), "+v"(acc[a][6]),
                   "+v"(acc[a][7]), "+v"(acc[a][8]), "+v"(acc[a][9]));
    asm volatile("" : "+v"(aoff[0]), "+v"(aoff[1]), "+v"(boff[0]), "+v"(boff[1]));
    if constexpr (LD) {
      WAIT_V(NP1);
      BAR();
    } else {
      if (wid) WAIT_V(1);
      WAIT_V(2);
    }
    TS1(0);
#pragma clang loop unroll(disable)
    for (int kt = 0; kt < NT; ++kt) {
      int cur = kt & 1;
      if (!LD && kt == NT - 1) { correct(); __builtin_amdgcn_s_setprio(2); }
      if (!LD) {
        asm volatile("" : "+s"(cur));
        __builtin_amdgcn_s_waitcnt(0xC07F);
        asm volatile(".p2align 5");
        BAR();
      }
      lds_cptr sb = shm3;
      if (LD) {
        unsigned k2 = kt - 1;
        asm volatile("" : "+s"(k2));
        if (k2 < NT - 2) {
#pragma unroll
          for (int i = 0; i < NP1; ++i) stage_part(cur ^ 1, kt + 1, i >> 1, i & 1);
        }
      }
      if (LD && kt + 1 < NT) {
#pragma unroll
        for (int i = NP1; i < 18; ++i) stage_part(cur ^ 1, kt + 1, i >> 1, i & 1);
      } else if (LD) {
        correct();
      }
      auto fadr = [&](int n) {
        const int f = n < 2 ? 8 + n : (n < 4 ? 11 - n : (n - 4) >> 1), ks = n < 2 ? 1 : (n < 4 ? 0 : n & 1);
        return sb + boff[f & 1] + (f >> 1) * 8192 + ks * 4096;
      };
      auto rdl = [&](int n) { bl[n % 10] = tr_read(fadr(n % 20)); };
      auto rdh = [&](int n) { bh[n % 10] = tr_read(fadr(n % 20) + 512); };
      auto rda = [&](bf16x8* af, int ks, int mf) { af[mf] = *(const __attribute__((address_space(3))) bf16x8*)(sb + aoff[ks] + mf * 2048); };
      SB0();
      if (LD) __builtin_amdgcn_s_waitcnt(0xC07F);
      if (LD) asm volatile(".p2align 5\n\ts_nop 0\n\ts_nop 0");
      sfor<80>([&](auto uc) __attribute__((always_inline)) {
        constexpr int u = decltype(uc)::value;
        SB0();
        if constexpr (u < 16) {
          const int n = u >> 2;
          mm(n, n < 2 ? 8 + n : 11 - n, n < 2 ? 1 : 0, (n & 1) ? 3 - (u & 3) : u & 3);
        } else {
          const int f = (u - 16) >> 3, i = (u >> 1) & 3, ks = (u ^ i) & 1;
          if ((u & 7) == 0) { asm volatile("" :: "v"(bl[(5 + 2 * f) % 10]), "v"(bh[(5 + 2 * f) % 10])); SB0(); }
          mm(4 + 2 * f + ks, f, ks, (f & 1) ? 3 - i : i);
        }
        SB0();
        if (u == 0) { rdl(2); rdh(2); }
        if (u == 1) { rda(af0, 0, 0); rda(af0, 0, 1); }
        if (u == 2) { rda(af0, 0, 2); rda(af0, 0, 3); }
        if (u == 3) { rdl(3); rdh(3); }
        if constexpr (u >= 4 && u < 8) rda(af1, 1, 7 - u);
        if (u >= 8 && u < 16) { if (u & 1) rdh(u >> 1); else rdl(u >> 1); }
        if (u >= 21 && u < 76 && (u & 1)) {
          const int n = 8 + ((u - 21) >> 2);
          if ((u - 21) & 2) rdh(n); else rdl(n);
        }
      });
      SB0();
      if (LD)
        asm volatile("s_cmp_eq_u32 %0, %1\n\ts_waitcnt vmcnt(0) lgkmcnt(0)\n\ts_cbranch_scc1 1f\n\ts_barrier\n1:" :: "s"(kt), "n"(NT - 1) : "memory", "scc");
      SB0();
      {
        const int d = cur ? -G1_STAGE : G1_STAGE;
        aoff[0] += d; aoff[1] += d; boff[0] += d; boff[1] += d;
        asm volatile("" : "+v"(aoff[0]), "+v"(aoff[1]), "+v"(boff[0]), "+v"(boff[1]));
      }
      if (!LD && kt <= 19 - SD) {
        int lane = threadIdx.x & 63;
        asm volatile("" : "+v"(lane));
        auto clock = [&]() { u64 now; asm volatile("s_memrealtime %0\n\ts_waitcnt lgkmcnt(0)" : "=s"(now) :: "memory"); return now; };
        WAIT_V(0);
        if (kt <= 16 - SD) {
          if (kt >= 1 + KS0) { accum(DMP + wid * 1024, DMP + 4096); WAIT_L(0); }
          if (kt <= 15 - SD) {
            if (kt >= KS0) share(kt + SD, DMP + wid * 1024, DMP + 4096);
          } else {
            GLDS_SC1(rstat + m0 + wid * 64 + (lane & 32) + 2 * (lane & 15) + ((lane >> 4) & 1), shm_base + AUX + wid * 1024);
          }
          if (kt == 16 - SD && (lane & 15) < 4) {
            const __amdgpu_buffer_rsrc_t rc = __builtin_amdgcn_make_buffer_rsrc((void*)(csum + ntile * 5 * 512 + mtile * 64), 0, 5 * 2048, 0x00020000);
            const int co = (lane & 15) * 64 + (lane >> 4) * 16;
            __builtin_amdgcn_raw_buffer_store_b128(__builtin_bit_cast(u32x4, ca), rc, co + wid * 2048, 0, 16);
            if (wid == 0) __builtin_amdgcn_raw_buffer_store_b128(__builtin_bit_cast(u32x4, cb), rc, co + 4 * 2048, 0, 16);
          }
          if (kt < SD + KS0) {
            if (kt <= SD + KS0 - 4) share(kt + 3, DMP + 7168, DMP + 7168);
            const int sp = cur * G1_STAGE + 32768 + mtile * 1024;
            accum(sp + wid * 8192, sp + 4 * 8192);
            if (kt == 1 || kt == 2) {
              const bf16x8 ones = {0x3f80, 0x3f80, 0x3f80, 0x3f80, 0x3f80, 0x3f80, 0x3f80, 0x3f80};
              const char* const xr = shm + AUX + wid * 2048 + (lane & 8) * 128 + (lane >> 4) * 128 + (lane & 7) * 16;
              const bf16x8 r0 = *(const bf16x8*)xr, r1 = *(const bf16x8*)(xr + 512);
              const f32x4 z = {0.f, 0.f, 0.f, 0.f};
              const f32x4 s1 = __builtin_amdgcn_mfma_f32_16x16x32_bf16(ones, r1, __builtin_amdgcn_mfma_f32_16x16x32_bf16(ones, r0, z, 0, 0, 0), 0, 0, 0);
              const f32x4 s2 = __builtin_amdgcn_mfma_f32_16x16x32_bf16(r1, r1, __builtin_amdgcn_mfma_f32_16x16x32_bf16(r0, r0, z, 0, 0, 0), 0, 0, 0);
              float t0 = __uint_as_float((unsigned)tg) + s1[0];
              asm volatile("" : "+v"(t0));
              const float t1 = __uint_as_float((unsigned)(tg >> 32)) + ((lane & 2) ? ((lane & 1) ? s2[3] : s2[2]) : ((lane & 1) ? s2[1] : s2[0]));
              tg = ((u64)__float_as_uint(t1) << 32) | __float_as_uint(t0);
            }
            if (kt < 2) {
#pragma unroll
              for (int i = 0; i < 2; ++i) glds16_p(x + (size_t)(srow + i) * D_ + kt * 512, lane * 16, shm_base + AUX + wid * 2048 + i * 1024);
            }
            if (kt == PUB_K) {
              if (wid == 0 && lane < 8) *(unsigned*)(shm + CNT + lane * 4) = 0u;
              const bool dg = ((lane >> 2) & 3) == (lane >> 4);
              float sm = half_sum(dg ? __uint_as_float((unsigned)tg) : 0.f);
              asm volatile("" : "+v"(sm));
              const float sq = half_sum(dg ? __uint_as_float((unsigned)(tg >> 32)) : 0.f);
              const u64 now = clock();
              if ((lane & 31) == 0) {
                const __amdgpu_buffer_rsrc_t rs = __builtin_amdgcn_make_buffer_rsrc((void*)(rstat + srow), 0, 32, 0x00020000);
                __builtin_amdgcn_raw_buffer_store_b128(u32x4{__float_as_uint(sm), (unsigned)now, __float_as_uint(sq), (unsigned)now ^ (unsigned)(now >> 32)}, rs,
                                                       lane >> 1, 0, 16);
              }
            }
          }
          if (kt >= PF_K0 && kt < PF_K0 + 5) {
#pragma unroll
            for (int i = 0; i < 2; ++i) {
              const int c = (bid * 4 + wid) * 10 + (kt - PF_K0) * 2 + i;
              if (c < 9216) {
                const u16* src = (c < 7168 ? wd + (size_t)c * 512 : wo + (size_t)(c - 7168) * 512) + lane * 8;
                GLDS_NC(src, shm_base + DMP + 7168);
              }
            }
          }
          if (kt >= CP_K0 && kt <= CP_K0 + 4) {
            if (kt < CP_K0 + 3) {
              const int q1 = bid * 4 + wid + (kt - CP_K0) * 1024, q0 = q1 - 1024;
              if (q0 >= 0 && q0 < 1152) {
                const __amdgpu_buffer_rsrc_t rc = __builtin_amdgcn_make_buffer_rsrc((void*)(wcopy + (q0 & 1) * 1179648 + (q0 >> 1) * 2048), 0, 2048, 0x00020000);
                __builtin_amdgcn_raw_buffer_store_b128(*(const u32x4*)(shm + AUX + wid * 1024 + lane * 16), rc, (lane >> 3) * 256 + (lane & 7) * 16, 0, 16);
                WAIT_L(0);
              }
              if (q1 < 1152) {
                const char* src = (const char*)((q1 >> 1) < 448 ? wd + (size_t)(q1 >> 1) * 8 * D_ : wo + (size_t)((q1 >> 1) - 448) * 8 * D_);
                src += ((3 - ((unsigned)(uintptr_t)src >> 7)) & 7) * 128 + (q1 & 1) * 1024;
                GLDS(src + (lane >> 3) * 2048 + (lane & 7) * 16, shm_base + AUX + wid * 1024);
              }
            } else if (kt == CP_K0 + 4) {
              if (wid == 0) {
                const u64 now = clock();
                if (lane == 0) __hip_atomic_store(&donec[bid], now, RLX_AGENT);
              }
              if (wid == 2) GLDS_SC1((const char*)(done2 + bid * 128) + lane * 16, shm_base + DMP + 7168);
            }
          }
        } else if (kt == 17 - SD) {
          const u64 now = clock();
          if (lane == 0) __hip_atomic_store(&cstamp[(ntile * 4 + wid) * 8 + mtile], now, RLX_AGENT);
          if (wid >= 2) GLDS_SC1((const char*)donec + (wid - 2) * 1024 + lane * 16, shm_base + DMP + wid * 2048);
          {
            const char* const wp = (const char*)wd + (size_t)(xcd < 4 ? 28 + 5 * (xcd & 1) : 5 - 5 * (xcd & 1)) * 131072 + (idx * 4 + wid) * 1024;
#pragma unroll
            for (int i = 0; i < 4; ++i) glds16_p(wp + (long)((i & 1) + (i >> 1) * 14) * 131072, lane * 16, shm_base + DMP + wid * 2048 + 1024);
          }
          u32x4 gr = *(const u32x4*)(shm + AUX + wid * 1024 + lane * 16);
          bool late = false;
          if (__builtin_expect(!__all(fresh(((u64)(gr[1] ^ gr[3]) << 32) | gr[1], t_own)), 0)) {
            const __amdgpu_buffer_rsrc_t rs = __builtin_amdgcn_make_buffer_rsrc((void*)(rstat + m0), 0, 4096, 0x00020000);
            const int prow = wid * 64 + (lane & 32) + 2 * (lane & 15) + ((lane >> 4) & 1);
            for (;;) {
              gr = __builtin_bit_cast(u32x4, __builtin_amdgcn_raw_buffer_load_b128(rs, prow * 16, 0, 16));
              if (__all(fresh(((u64)(gr[1] ^ gr[3]) << 32) | gr[1], t_own))) break;
              if (__builtin_amdgcn_s_memrealtime() - t_own > TIMEOUT_) { gr[1] = row_stats_serial(x + (size_t)(m0 + prow) * D_); late = true; break; }
              __builtin_amdgcn_s_sleep(2);
            }
          }
          float mean = __uint_as_float(gr[0]) * (1.0f / D_);
          asm volatile("" : "+v"(mean));
          const unsigned ps = pack_stats(mean, fmaxf(__builtin_fmaf(-mean, mean, __uint_as_float(gr[2]) * (1.0f / D_)), 0.f));
          const unsigned g0 = late ? gr[1] : ps;
          const float nm = -lo_f(g0), dn = hi_f(g0), r = __builtin_amdgcn_rcpf(dn);
          float k1 = -2.3022081986f * r, d2 = dn * dn;
          asm volatile("" : "+v"(k1));
          float k2 = -0.10294324f * r;
          asm volatile("" : "+v"(d2), "+v"(k2));
          const f32x4 fc = {nm, k1, k2 * r * r, d2};
          *(f32x4*)(shm + AUX + wid * 1024 + lane * 16) = fc;
        } else if (kt == 18 - SD) {
          const u32x4 c4 = *(const u32x4*)(shm + DMP + wid * 2048 + lane * 16);
          asm volatile("" :: "v"(c4) : "memory");
          cgather();
          const u64 win = tg - t_own;
          auto frs = [&](unsigned lo, unsigned hi) { return (((u64)hi << 32) | lo) - t_own - 100ull < win; };
          if (wid >= 2) *(unsigned*)(shm + CNT - 4 + wid * 4) = __all(frs(c4[0], c4[1]) & frs(c4[2], c4[3])) ? 1u : 0u;
        } else {
          const char* const sc = shm + (lane < 32 ? DMP + wid * 2048 + lane * 8 : QS + wid * 640 + (lane & 7) * 8);
          const int st = lane < 32 ? 256 : 64;
          const f32x4 fc = *(const f32x4*)(shm + AUX + wid * 1024 + lane * 16);
          f32x2 w[8];
          bool late = false;
          for (;;) {
            const u64 sv = *(const u64*)(shm + QS + wid * 640 + 512 + (lane & 15) * 8);
#pragma unroll
            for (int r = 0; r < 8; ++r) w[r] = *(const f32x2*)(sc + r * st);
            if (__builtin_expect(__all(sv - t_own - 100ull < tg - t_own - 200ull), 1)) break;
            late = __builtin_amdgcn_s_memrealtime() - t_own > TIMEOUT_;
            if (late) break;
            __builtin_amdgcn_s_sleep(2);
            WAIT_L(0);
            cgather();
            WAIT_V(0);
          }
          float t0 = w[0][0], t1 = w[0][1];
#pragma unroll
          for (int r = 1; r < 8; ++r)
            asm("v_add_f32 %0, %0, %2\n\tv_add_f32 %1, %1, %3" : "+v"(t0), "+v"(t1) : "v"(w[r][0]), "v"(w[r][1]));
          if (__builtin_expect(late, 0)) {
            const int gq = (lane >> 3) & 3;
            const char* pc = (lane < 32 ? pbase + ((psh & gq) ? dgu : 0) + (gq >> psh) * 32 : gb + dgu + wid * 32) + (lane & 7) * 4;
            t0 = 0.f; t1 = 0.f;
#pragma unroll 1
            for (int k = 0; k < D_; ++k) { const unsigned v = *(const unsigned*)(pc + (size_t)k * (lane < 32 ? pstr : F_ * 2)); t0 += lo_f(v); t1 += hi_f(v); }
          }
          if (lane < 40)
            *(f32x2*)((float*)(shm + CF) + (lane < 32 ? (lane >> 4) * 160 + wid * 32 + (lane & 15) * 2 : (wid >> 1) * 160 + 128 + (wid & 1) * 16 + (lane & 7) * 2)) = f32x2{t0, t1};
          float* const tw = (float*)(shm + DMP + wid * 2048) + lane;
#pragma unroll
          for (int i = 0; i < 4; ++i) tw[i * 64] = fc[i];
          ((float*)(shm + QS + wid * 640))[lane] = fc[1] * (-1.0f / 2.3022081986f);
        }
      }
      SB0();
    }
#pragma unroll
    for (int u = 0; u < 8; ++u) mm(u >> 2, 8 + (u >> 2), 1, u & 3);
  TS1(1);
  __builtin_amdgcn_sched_barrier(0);
  int tz = threadIdx.x;
  asm volatile("s_nop 7" : "+v"(tz));
  __builtin_amdgcn_sched_barrier(0);
  const int li = tz & 15, lq = (tz >> 4) & 3, ln = tz & 63;
  const __amdgpu_buffer_rsrc_t ra = __builtin_amdgcn_make_buffer_rsrc((void*)abuf, 0, 16 * 72 * 16384, 0x00020000);
  float* const svr = (float*)(shm + QS + wm * 640);
  float* const tb = (float*)(shm + DMP + wm * 2048);
  typedef __attribute__((address_space(3))) unsigned lds_u32;
  lds_u32* const cnt = (lds_u32*)(shm3 + CNT);
  unsigned vcnt = shm_base + CNT;
  asm volatile("" : "+v"(vcnt));
  auto signal = [&](unsigned v) {
    unsigned t;
    asm volatile("v_mov_b32 %0, %2\n\ts_mov_b64 exec, 1\n\tds_add_u32 %1, %0\n\ts_mov_b64 exec, -1" : "=&v"(t) : "v"(vcnt), "s"(v) : "memory");
  };
  auto await = [&](unsigned mask, unsigned val) {
    unsigned t, u;
    asm volatile("1:\n\tds_read_b32 %0, %2\n\ts_waitcnt lgkmcnt(0)\n\tv_readfirstlane_b32 %1, %0\n\ts_and_b32 %1, %1, %3\n\ts_cmp_eq_u32 %1, %4\n\t"
                 "s_cbranch_scc1 2f\n\ts_sleep 1\n\ts_branch 1b\n\t2:" : "=&v"(t), "=&s"(u) : "v"(vcnt), "s"(mask), "s"(val) : "memory", "scc");
  };
  if (grp) __builtin_amdgcn_s_setprio(1);
  signal(1u << (8 * grp));
  f32x4 rf[4];
#pragma unroll
  for (int mf = 0; mf < 4; ++mf) rf[mf] = *(const f32x4*)(shm + AUX + (wm * 64 + mf * 16 + li) * 16);
  const int sbase = ((mtile * 2 + (wm >> 1)) * 72 * 8 + (wm & 1) * 4) * 2048;
  auto putf = [&](int kg, int mf, u32x4 v) {
    __builtin_amdgcn_raw_buffer_store_b128(v, ra, ln * 16 + sbase + ((kg >> 1) * 8 + mf) * 2048 + (kg & 1) * 1024, 0, 16);
  };
  auto gelp = [&](f32x2 tg, f32x2 tu, f32x2 k12, f32x2 k22, f32x2 d2) {
    const f32x2 arg = tg * __builtin_elementwise_fma(tg * tg, k22, k12);
    const f32x2 dn = __builtin_elementwise_fma(f32x2{__builtin_amdgcn_exp2f(arg[0]), __builtin_amdgcn_exp2f(arg[1])}, d2, d2);
    const f32x2 r = (tg * tu) * f32x2{__builtin_amdgcn_rcpf(dn[0]), __builtin_amdgcn_rcpf(dn[1])};
    return pack2(r[0], r[1]);
  };
  auto gels = [&](const f32x4& g, const f32x4& u, const f32x4& k) {
    float t0, t1, t2, t3, n0, n1, n2, n3;
    asm("v_mul_f32 %0, %8, %8\n\tv_mul_f32 %1, %9, %9\n\tv_mul_f32 %2, %10, %10\n\tv_mul_f32 %3, %11, %11\n\t"
        "v_fma_f32 %0, %0, %17, %16\n\tv_fma_f32 %1, %1, %17, %16\n\tv_fma_f32 %2, %2, %17, %16\n\tv_fma_f32 %3, %3, %17, %16\n\t"
        "v_mul_f32 %0, %8, %0\n\tv_mul_f32 %1, %9, %1\n\tv_mul_f32 %2, %10, %2\n\tv_mul_f32 %3, %11, %3\n\t"
        "v_exp_f32 %0, %0\n\tv_mul_f32 %4, %8, %12\n\tv_exp_f32 %1, %1\n\tv_mul_f32 %5, %9, %13\n\t"
        "v_exp_f32 %2, %2\n\tv_mul_f32 %6, %10, %14\n\tv_exp_f32 %3, %3\n\tv_mul_f32 %7, %11, %15\n\t"
        "v_fma_f32 %0, %0, %18, %18\n\tv_fma_f32 %1, %1, %18, %18\n\tv_rcp_f32 %0, %0\n\tv_fma_f32 %2, %2, %18, %18\n\t"
        "v_rcp_f32 %1, %1\n\tv_fma_f32 %3, %3, %18, %18\n\tv_rcp_f32 %2, %2\n\tv_mul_f32 %0, %4, %0\n\t"
        "v_rcp_f32 %3, %3\n\tv_mul_f32 %1, %5, %1\n\tv_mul_f32 %2, %6, %2\n\tv_mul_f32 %3, %7, %3\n\t"
        "v_cvt_pk_bf16_f32 %0, %0, %1\n\tv_cvt_pk_bf16_f32 %1, %2, %3"
        : "=&v"(t0), "=&v"(t1), "=&v"(t2), "=&v"(t3), "=&v"(n0), "=&v"(n1), "=&v"(n2), "=&v"(n3)
        : "v"(g[0]), "v"(g[1]), "v"(g[2]), "v"(g[3]), "v"(u[0]), "v"(u[1]), "v"(u[2]), "v"(u[3]), "v"(k[1]), "v"(k[2]), "v"(k[3]));
    return u32x2{__float_as_uint(t0), __float_as_uint(t1)};
  };
  auto gel2 = [&](int p, int kg, int mf0 = 0, int mf1 = 4, int nsc = 0, bool opq = false) {
#pragma unroll
    for (int mf = mf0; mf < mf1; ++mf) {
      const f32x2 k12 = {rf[mf][1], rf[mf][1]}, k22 = {rf[mf][2], rf[mf][2]}, d2 = {rf[mf][3], rf[mf][3]};
      u32x4 o;
      if (opq) asm volatile("" : "+v"(acc[mf][p * 2]), "+v"(acc[mf][p * 2 + 1]));
      if (mf < nsc) {
        const u32x2 a = gels(acc[mf][p * 2], acc[mf][p * 2 + 2], rf[mf]), b = gels(acc[mf][p * 2 + 1], acc[mf][p * 2 + 3], rf[mf]);
        o = u32x4{a[0], a[1], b[0], b[1]};
      } else {
#pragma unroll
        for (int i = 0; i < 4; ++i) {
          const int j = i >> 1, e = (i & 1) * 2;
          const f32x4 g = acc[mf][p * 2 + j], u = acc[mf][p * 2 + 2 + j];
          o[i] = gelp(f32x2{g[e], g[e + 1]}, f32x2{u[e], u[e + 1]}, k12, k22, d2);
        }
      }
      putf(kg, mf, o);
      if (mf < nsc) SB0();
    }
  };
  auto client = [&](int l) { return (((l & 7) * 4 + (mtile & 3)) * 8) + (((mtile >> 2) * 2 + ((l >> 4) & 1)) ^ ((l & 8) ? 5 : 0)); };
  auto notify = [&](u64 lanes) {
    int l = threadIdx.x & 63;
    asm volatile("" : "+v"(l));
    const unsigned off = (client(l) * 128 + (l & 32) + ntile) * 8;
    u64 now, vn;
    asm volatile("s_memrealtime %0\n\ts_nop 0\n\ts_waitcnt lgkmcnt(0)\n\tv_mov_b64 %1, %0\n\ts_mov_b64 exec, %4\n\tglobal_store_dwordx2 %2, %1, %3 sc1\n\ts_mov_b64 exec, -1"
                 : "=&s"(now), "=&v"(vn) : "v"(off), "s"(done1), "s"(lanes) : "memory");
  };
  const u64 myh = 0xffffull << ((wm >> 1) * 16);
  const u64 mrole = (xcd & 4) ? 0x00ff00ffull : 0xff00ff00ull;
  const u64 mall = (tya || (xcd & 3) == 3) ? 0xffffffffull : mrole;
  auto arrive = [&](auto pc) {
    constexpr int part = decltype(pc)::value;
    unsigned t;
    asm volatile("v_mov_b32 %0, 1\n\ts_mov_b64 exec, 1\n\tds_add_rtn_u32 %0, %1, %0 offset:%2\n\ts_mov_b64 exec, -1\n\ts_waitcnt lgkmcnt(0)"
                 : "=&v"(t) : "v"(vcnt), "n"(12 + 4 * part) : "memory");
    if (__builtin_amdgcn_readfirstlane(t) == 3u) notify(part ? (myh & mall) << 32 : myh & mrole);
  };
  const int role = 1 - (xcd >> 2);
  const int xb = role ? xcd : xcd ^ 5, par = xb & 1;
  const int mt2 = ((xb >> 1) * 4 + (idx & 3)) * 2 + par, nt2 = idx >> 2;
  auto rsrc = [](u64 b, unsigned n) { return u32x4{(unsigned)b, (unsigned)(b >> 32) & 0xffffu, n, 0x00020000u}; };
  u32x4 rw[2];
  int wsh[2];
  unsigned vb[2][2], cokw = 0;
  auto wsetup = [&](const u16* w, int rows, int crow0, int l) {
    const unsigned vr = (wid >> 1) * 32 + (wid & 1) * 8 + ((l >> 5) & 1) * 16 + ((l >> 3) & 3), vc = (((l >> 1) & 3) ^ ((l >> 4) & 1)) * 32 + (l & 1) * 16;
    u64 bs = (u64)(uintptr_t)(wcopy + (nt2 >> 2) * 1179648 + crow0 * 256 - 1024), bf = (u64)(uintptr_t)(w + nt2 * 128) - 1024;
    asm volatile("" : "+s"(bs), "+s"(bf));
#pragma unroll
    for (int i = 0; i < 2; ++i) {
      const bool slow = ((((unsigned)bf >> 7) + i) & 7) == (cokw ? 3u : 8u);
      const int sh = slow ? 8 : 11;
      rw[i] = rsrc(slow ? bs : bf + i * 128, ((rows - 1) << sh) + 128 + 1024);
      wsh[i] = 6 + sh;
      vb[i][0] = (vr << sh) + vc + 1024;
      vb[i][1] = ((vr + 4) << sh) + (vc ^ 64);
    }
  };
#define G2_W "s_add_u32 m0, %[l], %[ow]\n\ts_lshl_b32 %[t], %[k], %[s0]\n\tbuffer_load_dwordx4 %[v0], %[w0], %[t] offen sc1 lds\n\t" \
             "buffer_load_dwordx4 %[v1], %[w0], %[t] offen offset:1024 sc1 lds\n\t" \
             "s_add_u32 m0, m0, 8192\n\ts_lshl_b32 %[t], %[k], %[s1]\n\tbuffer_load_dwordx4 %[v2], %[w1], %[t] offen sc1 lds\n\t" \
             "buffer_load_dwordx4 %[v3], %[w1], %[t] offen offset:1024 sc1 lds"
#define G2_WOP [ow] "s"(16384 + wid * 2048), [k] "s"(kt), [s0] "s"(wsh[0]), [s1] "s"(wsh[1]), [w0] "s"(rw[0]), [w1] "s"(rw[1]), \
               [v0] "v"(vb[0][0]), [v1] "v"(vb[0][1]), [v2] "v"(vb[1][0]), [v3] "v"(vb[1][1])
#define G2_A "s_add_u32 m0, %[l], %[oa]\n\ts_lshl_b32 %[t], %[k], 14\n\tbuffer_load_dwordx4 %[va], %[a], %[t] offen sc1 lds\n\t" \
             "buffer_load_dwordx4 %[va], %[a], %[t] offen offset:1024 sc1 lds\n\t"
#define G2_A2 "buffer_load_dwordx4 %[va], %[a], %[t] offen offset:2048 sc1 lds\n\tbuffer_load_dwordx4 %[va], %[a], %[t] offen offset:3072 sc1 lds\n\t"
  constexpr int NY = 28;
  const int ny = (xcd & 1) ? NY - DLT : NY + DLT;
  const u32x4 ra2 = rsrc((u64)(uintptr_t)abuf + mt2 * (72 * 16384), 72 * 16384);
  unsigned va, tab, nlo, nhi, sl = shm_base + 4 * G2_STAGE;
  const int kfirst = 28 * role + 5 * par;
  int q = 3, kt = kfirst + 15;
  u64 okb = 0;
  auto adv = [&]() { ++q; kt = __builtin_amdgcn_readlane(tab, q); sl = sl == shm_base + (G2_NSTAGE - 1) * G2_STAGE ? shm_base : sl + G2_STAGE; };
  auto stage_y = [&]() {
    unsigned t;
    asm volatile(G2_A G2_A2 G2_W : [t] "=&s"(t) : [l] "s"(sl), [oa] "s"((wid ^ role) * 4096), [a] "s"(ra2), [va] "v"(va), G2_WOP : "memory", "scc", "m0");
    adv();
  };
  auto digest = [&](u64 v, u64 valid = ~0ull) {
    const u64 r = __ballot(fresh(v, t_own)) & valid;
    const unsigned hi = (unsigned)(r >> 32), lo = (unsigned)r | hi;
    okb |= __ballot(((nlo & ~lo) | (nhi & ~hi)) == 0u);
  };
  auto aload = [&](const u64* base, unsigned off) {
    asm volatile("s_nop 4\n\tglobal_load_dwordx2 v[254:255], %0, %1 sc1" :: "v"(off), "s"(base) : "memory", "v254", "v255");
    SB0();
  };
  auto aget = [&]() { u64 v; asm volatile("v_mov_b64 %0, v[254:255]" : "=v"(v) :: "memory"); return v; };
  auto snapshot = [&]() { aload(done1 + bid * 128, ln * 8); };
  auto peek = [&]() { digest(aget()); };
  auto snapshot1 = [&]() { aload(done1 + bid * 128 + role * 16, (ln & 15) * 8); };
  auto peek1 = [&]() { digest(aget(), 0xffffull << (16 * role)); };
  auto wpair = [&](int g, int i) {
    unsigned t;
    asm volatile("s_mov_b32 m0, %1\n\ts_lshl_b32 %0, %2, %3\n\tbuffer_load_dwordx4 %5, %4, %0 offen sc1 lds\n\tbuffer_load_dwordx4 %6, %4, %0 offen offset:1024 sc1 lds"
                 : "=&s"(t) : "s"(shm_base + (g + 1) * G2_STAGE + 16384 + i * 8192 + wid * 2048), "s"(kfirst + (g == 2 ? 14 : g)), "s"(wsh[i]), "s"(rw[i]),
                   "v"(vb[i][0]), "v"(vb[i][1]) : "memory", "scc", "m0");
    SB0();
  };
  auto aquad = [&](int g) {
    unsigned t;
    asm volatile(G2_A G2_A2 : [t] "=&s"(t) : [l] "s"(shm_base + (g + 1) * G2_STAGE), [oa] "s"((wid ^ role) * 4096), [a] "s"(ra2), [va] "v"(va),
                 [k] "s"(((okb >> g) & 1) ? kfirst + (g == 2 ? 14 : g) : 100) : "memory", "scc", "m0");
    SB0();
  };
  auto mid = [&]() {
    cokw = __builtin_amdgcn_readfirstlane(cnt[1] & cnt[2]);
    wsetup(wd, F_, 0, ln);
    va = ln * 16 + wid * 4096;
    {
      const int ext = ln >= NY ? 1 : 0, qq = ln - ext * DLT, so = ln - ny;
      int j0 = 5 * par, j1 = 5 - 5 * par;
      asm volatile("" : "+s"(j1));
      const int j = (qq < 8 ? j0 : (qq < 16 ? j1 : 2)) + ((unsigned)(0x92492444444444ull >> (2 * qq)) & 3);
      const int xs = ((role ^ ext) << 2) | ((unsigned)(0xfd5a80f5a0f5a0ull >> (2 * qq)) & 3);
      unsigned bits = ((0x41a8282u >> (4 * j)) & 15u) << (4 * xs), ob = (so < 16 ? 1u : 0u) << (2 * so), ty = 7 * xs + j;
      asm volatile("" : "+v"(bits), "+v"(ob), "+v"(ty));
      tab = so < 0 ? ty : so;
      nlo = ln < 8 ? bits : 0u;
      nhi = so < 0 ? bits - nlo : ob;
    }
    asm volatile("" :: "s"(rw[0]), "s"(rw[1]), "s"(wsh[0]), "s"(wsh[1]), "s"(ra2));
    asm volatile("" : "+v"(vb[0][0]), "+v"(vb[0][1]), "+v"(vb[1][0]), "+v"(vb[1][1]), "+v"(va), "+v"(tab), "+v"(nlo), "+v"(nhi));
    await(0xffffu, 0x404u);
    wpair(0, 0);
    wpair(0, 1);
    WAIT_V(4);
    arrive(std::integral_constant<int, 0>{});
    __builtin_amdgcn_s_setprio(0);
  };
  int tsel = ntile & 1;
  asm volatile("" : "+s"(tsel));
  if (tsel != 0) {
    if (grp == 0) gel2(0, 2 * (kt0 + he) + wc, 0, NSC, NSC);
    else gel2(0, 2 * (kt0 + he) + wc, 0, NSC);
    gel2(0, 2 * (kt0 + he) + wc, NSC, 4);
    if (grp == 0) mid();
#pragma unroll
    for (int mf = 0; mf < 4; ++mf) {
      f32x4 t[4];
#pragma unroll
      for (int i = 1; i < 4; ++i) t[i] = *(const f32x4*)(tb + i * 64 + mf * 16 + lq * 4);
      const f32x4 g = acc[mf][4], u = acc[mf][5];
      u32x2 o;
#pragma unroll
      for (int e = 0; e < 4; e += 2)
        o[e >> 1] = gelp(f32x2{g[e], g[e + 1]}, f32x2{u[e], u[e + 1]}, f32x2{t[1][e], t[1][e + 1]}, f32x2{t[2][e], t[2][e + 1]}, f32x2{t[3][e], t[3][e + 1]});
      *(u32x2*)(shm + wm * 4096 + mf * 1024 + wc * 512 + li * 32 + lq * 8) = o;
      if (mf == 1 || grp == 0) SB0();
      if (grp == 0) {
        wpair(1 + (mf >> 1), mf & 1);
        if (mf == 3) snapshot1();
      }
    }
    signal(0x10000u << vid);
    if (grp) {
      gel2(3, 2 * (kt0 + 2 + he) + wc, 0, 4);
    } else {
#pragma unroll
      for (int mf = 0; mf < 4; ++mf) {
        gel2(3, 2 * (kt0 + 2 + he) + wc, mf, mf + 1);
        SB0();
        if (mf == 0) { WAIT_V(1); peek1(); }
        if (mf < 3) aquad(mf);
        if (mf == 2) snapshot1();
      }
    }
    await(0x110000u << wm, 0x110000u << wm);
#pragma unroll
    for (int i = 0; i < 2; ++i) putf(2 * (kt0 + 4) + he, 2 * wc + i, __builtin_bit_cast(u32x4, tr_frag(shm3 + wm * 4096 + wc * 2048 + i * 1024 + ln * 8)));
    if (grp == 0) WAIT_V(9);
  } else {
    if (grp) __builtin_amdgcn_s_setprio(3);
    float rd[4];
#pragma unroll
    for (int mf = 0; mf < 4; ++mf) rd[mf] = svr[mf * 16 + li];
    auto cvs = [&](const f32x4& a, const f32x4& b, const f32x4& r) {
      float t0, t1, t2, t3, t4, t5, t6, t7;
      asm("v_mul_f32 %0, %8, %16\n\tv_mul_f32 %4, %9, %17\n\tv_mul_f32 %1, %10, %18\n\tv_mul_f32 %5, %11, %19\n\t"
          "v_mul_f32 %2, %12, %16\n\tv_mul_f32 %6, %13, %17\n\tv_mul_f32 %3, %14, %18\n\tv_mul_f32 %7, %15, %19\n\t"
          "v_cvt_pk_bf16_f32 %0, %0, %4\n\tv_cvt_pk_bf16_f32 %1, %1, %5\n\tv_cvt_pk_bf16_f32 %2, %2, %6\n\tv_cvt_pk_bf16_f32 %3, %3, %7"
          : "=&v"(t0), "=&v"(t1), "=&v"(t2), "=&v"(t3), "=&v"(t4), "=&v"(t5), "=&v"(t6), "=&v"(t7)
          : "v"(a[0]), "v"(a[1]), "v"(a[2]), "v"(a[3]), "v"(b[0]), "v"(b[1]), "v"(b[2]), "v"(b[3]), "v"(r[0]), "v"(r[1]), "v"(r[2]), "v"(r[3]));
      return u32x4{__float_as_uint(t0), __float_as_uint(t1), __float_as_uint(t2), __float_as_uint(t3)};
    };
    auto cvp = [&](const f32x4& a, const f32x4& b) { return u32x4{pack2(a[0], a[1]), pack2(a[2], a[3]), pack2(b[0], b[1]), pack2(b[2], b[3])}; };
    auto cvk = [&](int mf, bool sc) {
      const f32x4 r4 = {rd[mf], rd[mf], rd[mf], rd[mf]};
      return sc ? cvs(acc[mf][2], acc[mf][3], r4) : cvp(acc[mf][2] * r4, acc[mf][3] * r4);
    };
    auto cvv = [&](int mf, bool sc) {
      const f32x4 r4 = *(const f32x4*)(svr + mf * 16 + lq * 4);
      return sc ? cvs(acc[mf][4], acc[mf][5], r4) : cvp(acc[mf][4] * r4, acc[mf][5] * r4);
    };
    u32x4 kx[4], vx[4], qx[4], hx[4];
    const bf16x8 ones = {0x3f80, 0x3f80, 0x3f80, 0x3f80, 0x3f80, 0x3f80, 0x3f80, 0x3f80};
    kx[2] = cvk(2, true); kx[3] = cvk(3, true);
    vx[2] = cvv(2, true); vx[3] = cvv(3, true);
    const bool giveh = !grp && wm == 3 && mtile < 7, needh = grp && wm == 0 && mtile > 0;
    char* const hptr = (char*)halo + (size_t)((needh ? mtile - 1 : mtile) * 32 + ntile + wc) * 16384;
    if (grp || wm < 3) {
      char* const ex = shm + (vid - wc) * 4096 + ln * 16;
      *(u32x4*)ex = kx[2]; *(u32x4*)(ex + 1024) = kx[3]; *(u32x4*)(ex + 2048) = vx[2]; *(u32x4*)(ex + 3072) = vx[3];
      signal(0x10000u << vid);
    } else if (giveh) {
      const __amdgpu_buffer_rsrc_t rh = __builtin_amdgcn_make_buffer_rsrc((void*)hptr, 0, 9216, 0x00020000);
      const u64 now = __builtin_amdgcn_s_memrealtime();
      const unsigned tag = (unsigned)now;
#pragma unroll
      for (int r = 0; r < 8; ++r) {
        const u32x4 w = r < 4 ? kx[2 + (r >> 1)] : vx[r >> 1];
        __builtin_amdgcn_raw_buffer_store_b128(u32x4{w[2 * (r & 1)], tag, w[2 * (r & 1) + 1], tag}, rh, ln * 16 + r * 1024, 0, 16);
      }
      __builtin_amdgcn_raw_buffer_store_b128(u32x4{tag, (unsigned)(now >> 32), tag, (unsigned)(now >> 32)}, rh, ln * 16 + 8192, 0, 16);
    }
    if (grp == 0) { gel2(3, 2 * (kt0 + 5 + he) + wc, 0, 4, NSC - 1, true); mid(); }
    kx[0] = cvk(0, false); kx[1] = cvk(1, false);
    vx[0] = cvv(0, false); vx[1] = cvv(1, false);
#pragma unroll
    for (int mf = 0; mf < 4; ++mf) qx[mf] = cvp(acc[mf][0], acc[mf][1]);
    f32x4 mk0, mk1;
#pragma unroll
    for (int j = 0; j < 4; ++j) {
      const int k0 = 4 * lq + j, k1 = 16 + 4 * lq + j;
      mk0[j] = (k0 >= li + 9 && k0 <= li + 16) ? 0.f : -1.0e30f;
      mk1[j] = (k1 >= li + 9 && k1 <= li + 16) ? 0.f : -1.0e30f;
    }
    auto max3 = [](float a, float b, float c) { float r; asm("v_max3_f32 %0, %1, %2, %3" : "=v"(r) : "v"(a), "v"(b), "v"(c)); return r; };
    auto max2 = [](float a, float b) { float r; asm("v_max_f32 %0, %1, %2" : "=v"(r) : "v"(a), "v"(b)); return r; };
    auto tiles = [&](int t0, const u32x4* kp, const u32x4* vp, bool first_rows) {
      const f32x4 z = {0.f, 0.f, 0.f, 0.f};
      const f32x4 mz = {-1.0e30f, -1.0e30f, -1.0e30f, -1.0e30f};
      f32x4 s0[2], s1[2];
#pragma unroll
      for (int n = 0; n < 2; ++n) {
        const bf16x8 qf = __builtin_bit_cast(bf16x8, qx[t0 + n]);
        s0[n] = __builtin_amdgcn_mfma_f32_16x16x32_bf16(__builtin_bit_cast(bf16x8, kp[n]), qf, first_rows ? mz : mk0, 0, 0, 0);
        s1[n] = __builtin_amdgcn_mfma_f32_16x16x32_bf16(__builtin_bit_cast(bf16x8, kx[t0 + n]), qf, mk1, 0, 0, 0);
      }
      SB0();
      if (grp == 0) { if (t0) wpair(1, 1); else aquad(1); }
      asm volatile("s_nop 7\n\ts_nop 3" : "+v"(s0[0]), "+v"(s1[0]), "+v"(s0[1]), "+v"(s1[1]));
      float mx[2];
#pragma unroll
      for (int n = 0; n < 2; ++n) mx[n] = max3(max3(s0[n][0], s0[n][1], s0[n][2]), max3(s0[n][3], s1[n][0], s1[n][1]), max2(s1[n][2], s1[n][3]));
#pragma unroll
      for (int n = 0; n < 2; ++n) { auto r = __builtin_amdgcn_permlane16_swap(__float_as_uint(mx[n]), __float_as_uint(mx[n]), false, false);
        mx[n] = max2(__uint_as_float(r[0]), __uint_as_float(r[1])); }
#pragma unroll
      for (int n = 0; n < 2; ++n) { auto r = __builtin_amdgcn_permlane32_swap(__float_as_uint(mx[n]), __float_as_uint(mx[n]), false, false);
        mx[n] = max2(__uint_as_float(r[0]), __uint_as_float(r[1])); }
      SB0();
      if (grp == 0) {
        if (t0) wpair(2, 0); else aquad(2);
        snapshot1();
      }
      constexpr float cs2 = 0.17677669529663687f * 1.4426950408889634f;
      f32x4 os[2], o0[2], o1[2];
#pragma unroll
      for (int n = 0; n < 2; ++n) {
        const float c = cs2 * rd[t0 + n], nm = -mx[n] * c;
        float e8[8];
#pragma unroll
        for (int j = 0; j < 4; ++j) {
          e8[j] = __builtin_amdgcn_exp2f(__builtin_fmaf(s0[n][j], c, nm));
          e8[4 + j] = __builtin_amdgcn_exp2f(__builtin_fmaf(s1[n][j], c, nm));
        }
        u32x4 pk;
#pragma unroll
        for (int j = 0; j < 4; ++j) pk[j] = pack2(e8[2 * j], e8[2 * j + 1]);
        const bf16x8 pf = __builtin_bit_cast(bf16x8, pk);
        const u32x4 va = {vp[n][0], vp[n][1], vx[t0 + n][0], vx[t0 + n][1]}, vb = {vp[n][2], vp[n][3], vx[t0 + n][2], vx[t0 + n][3]};
        os[n] = __builtin_amdgcn_mfma_f32_16x16x32_bf16(ones, pf, z, 0, 0, 0);
        o0[n] = __builtin_amdgcn_mfma_f32_16x16x32_bf16(__builtin_bit_cast(bf16x8, va), pf, z, 0, 0, 0);
        o1[n] = __builtin_amdgcn_mfma_f32_16x16x32_bf16(__builtin_bit_cast(bf16x8, vb), pf, z, 0, 0, 0);
      }
      if (grp == 0 && t0) { SB0(); wpair(2, 1); }
#pragma unroll
      for (int n = 0; n < 2; ++n) {
        const float inv = __builtin_amdgcn_rcpf(os[n][0]);
        const f32x4 n0 = o0[n] * inv, n1 = o1[n] * inv;
        putf(112 + ntile + wc, t0 + n, u32x4{pack2(n0[0], n0[1]), pack2(n0[2], n0[3]), pack2(n1[0], n1[1]), pack2(n1[2], n1[3])});
      }
    };
    if (grp == 0) wpair(1, 0);
    tiles(2, &kx[0], &vx[0], false);
    constexpr int HL = 131072, HS = DMP + 1024;
    auto hload = [&]() {
      asm volatile("s_mov_b32 m0, %2\n\ts_nop 0\n\tglobal_load_lds_dwordx4 %0, %1 offset:-4096 sc1\n\tglobal_load_lds_dwordx4 %0, %1 offset:-3072 sc1\n\t"
                   "global_load_lds_dwordx4 %0, %1 offset:-2048 sc1\n\tglobal_load_lds_dwordx4 %0, %1 offset:-1024 sc1\n\tglobal_load_lds_dwordx4 %0, %1 sc1\n\t"
                   "global_load_lds_dwordx4 %0, %1 offset:1024 sc1\n\tglobal_load_lds_dwordx4 %0, %1 offset:2048 sc1\n\tglobal_load_lds_dwordx4 %0, %1 offset:3072 sc1\n\t"
                   "s_mov_b32 m0, %4\n\ts_nop 0\n\tglobal_load_lds_dwordx4 %0, %3 sc1"
                   :: "v"(ln * 16), "s"(hptr + 4096), "s"(shm_base + HL + wc * 8192 + 4096), "s"(hptr + 8192), "s"(shm_base + HS + wc * 2048) : "memory", "m0");
    };
    if (needh) { await(0xffffu, 0x404u); hload(); }
    if (grp) { __builtin_amdgcn_s_setprio(1); gel2(3, 2 * (kt0 + 5 + he) + wc, 0, 2, 0, true); __builtin_amdgcn_s_setprio(3); }
    else {
      WAIT_V(4);
      peek1();
      aquad(0);
    }
    if (!grp || wm > 0) {
      await(0x8000u << vid, 0x8000u << vid);
#pragma unroll
      for (int i = 0; i < 4; ++i) hx[i] = *(const u32x4*)(shm + (vid - wc - 1) * 4096 + i * 1024 + ln * 16);
    } else if (!needh) {
#pragma unroll
      for (int i = 0; i < 4; ++i) hx[i] = u32x4{0u, 0u, 0u, 0u};
    } else {
      bool late = false;
      WAIT_V(2);
      for (;;) {
        const char* const hl = shm + HL + wc * 8192 + ln * 16;
        const u32x4 sp = *(const u32x4*)(shm + HS + wc * 2048 + ln * 16);
        unsigned df = 0;
#pragma unroll
        for (int i = 0; i < 4; ++i) {
          const u32x4 g0 = *(const u32x4*)(hl + i * 2048), g1 = *(const u32x4*)(hl + i * 2048 + 1024);
          df |= (g0[1] ^ sp[0]) | (g0[3] ^ sp[0]) | (g1[1] ^ sp[0]) | (g1[3] ^ sp[0]);
          hx[i] = u32x4{g0[0], g0[2], g1[0], g1[2]};
        }
        if (__builtin_expect(__all((df == 0) & fresh(((u64)sp[1] << 32) | sp[0], t_own)), 1)) break;
        late = __builtin_amdgcn_s_memrealtime() - t_own > TIMEOUT_;
        if (late) break;
        __builtin_amdgcn_s_sleep(1);
        WAIT_L(0);
        hload();
        WAIT_V(0);
      }
      if (__builtin_expect(late, 0)) {
        char* const fb = shm + HL + wc * 8192 + ln * 16;
#pragma unroll 1
        for (int i = 0; i < 32; ++i) {
          const int rec = i >> 3, e = i & 7;
          const int row = m0 - 32 + 2 * (rec < 2 ? li : lq * 4 + (e & 3)) + (rec & 1);
          const int col = (rec < 2 ? D_ + lq * 4 + (e & 3) : 2 * D_ + li) + (ntile + wc) * 32 + (e >> 2) * 16;
          const u16* xr = x + (size_t)row * D_;
          const unsigned stt = row_stats_serial(xr);
          const float mb = lo_f(stt);
          float a = 0.f;
#pragma unroll 1
          for (int k = 0; k < D_; ++k) a += (bf2f(xr[k]) - mb) * bf2f(wqkv[(size_t)k * 3072 + col]);
          *(u16*)(fb + rec * 1024 + e * 2) = f2bf(a / hi_f(stt));
        }
#pragma unroll
        for (int i = 0; i < 4; ++i) hx[i] = *(const u32x4*)(fb + i * 1024);
      }
    }
    tiles(0, &hx[0], &hx[2], grp && mtile == 0 && wm == 0);
    if (grp) { __builtin_amdgcn_s_setprio(1); gel2(3, 2 * (kt0 + 5 + he) + wc, 2, 4, 0, true); }
    else WAIT_V(7);
  }
  TS1(2);
  const bool cok = cokw != 0u;
  {
  __builtin_amdgcn_sched_barrier(0);
  int tid = threadIdx.x & 255;
  asm volatile("" : "+v"(tid));
  __builtin_amdgcn_sched_barrier(0);
  const int lane = tid & 63;
  const int wm = (wid >> 1) & 1, wn = wid & 1;
  const int mtile = mt2, ntile = nt2;
  const int m0 = mtile * 128, n0 = ntile * 128;
  TS(0);
  constexpr int EP = 528;
  const int img0 = ((ny + 13) % G2_NSTAGE) * G2_STAGE, img1 = ((ny + 14) % G2_NSTAGE) * G2_STAGE;
  if constexpr (!LD) {
    const int p = wid;
    const u64* const mbox = &done1[bid * 128 + lane];
    auto stage_s = [&]() {
      unsigned t;
      asm volatile(G2_A G2_W : [t] "=&s"(t) : [l] "s"(sl), [oa] "s"((p >> 1) * 8192 + (p & 1) * 2048), [a] "s"(ra2), [va] "v"(va), G2_WOP : "memory", "scc", "m0");
      adv();
    };
    u64 ren = 0;
    auto need = [&]() {
      while (__builtin_expect(!((okb >> q) & 1), 0)) {
        digest(__hip_atomic_load(mbox, RLX_AGENT));
        if (!((okb >> q) & 1)) {
          __builtin_amdgcn_s_sleep(2);
          if (__builtin_expect(!cok, 0)) notify(ren);
          if (GIVEUP()) break;
        }
      }
      asm volatile("" ::: "memory");
    };
    __builtin_amdgcn_s_setprio(0);
    if ((okb & 7) != 7) {
      const unsigned done = (unsigned)okb;
#pragma unroll 1
      for (q = 0; q < 3; ++q)
        if (!((done >> q) & 1)) {
          need();
          aquad(q);
        }
      if (done & 4) WAIT_V(0);
      WAIT_V(4);
    }
    TS(4);
    BARL();
    need();
    stage_y();
    WAIT_V(8);
    peek1();
    if (p == 0) snapshot();
    need();
    stage_y();
    if (p) okb = ~0ull;
    auto check = [&]() {
      if (__builtin_expect(okb != ~0ull && !(q & 1), 0)) {
        peek();
        if (okb != ~0ull) snapshot();
      }
    };
#define G2_RUN(N, A23) "s_branch 2f\n1:\n\tbuffer_load_dwordx4 %[v3], %[w1], %[t2] offen offset:1024 sc1 lds\n2:\n\t" \
      "s_add_u32 m0, %[l], %[oa]\n\ts_lshl_b32 %[t], %[k], 14\n\ts_waitcnt vmcnt(" N ")\n\ts_barrier\n\t" \
      "buffer_load_dwordx4 %[va], %[a], %[t] offen sc1 lds\n\ts_add_u32 %[q], %[q], 1\n\ts_lshl_b32 %[t2], %[k], %[s1]\n\t" \
      "buffer_load_dwordx4 %[va], %[a], %[t] offen offset:1024 sc1 lds\n\t" A23 \
      "s_add_u32 m0, %[l], %[ow]\n\ts_lshl_b32 %[t], %[k], %[s0]\n\t" \
      "buffer_load_dwordx4 %[v0], %[w0], %[t] offen sc1 lds\n\tv_readlane_b32 %[k], %[tab], %[q]\n\ts_add_u32 %[l], %[l], 0x8000\n\t" \
      "buffer_load_dwordx4 %[v1], %[w0], %[t] offen offset:1024 sc1 lds\n\ts_add_u32 m0, m0, 8192\n\ts_cmp_eq_u32 %[l], %[le]\n\ts_cselect_b32 %[l], %[lb], %[l]\n\t" \
      "buffer_load_dwordx4 %[v2], %[w1], %[t2] offen sc1 lds\n\ts_cmp_lt_u32 %[q], %[qe]\n\ts_cbranch_scc1 1b\n\t" \
      "buffer_load_dwordx4 %[v3], %[w1], %[t2] offen offset:1024 sc1 lds"
#define G2_RUNOP : [t] "=&s"(t), [t2] "=&s"(t2), [q] "+&s"(q), [k] "+&s"(kt), [l] "+&s"(sl) \
      : [oa] "s"(oa), [a] "s"(ra2), [va] "v"(va), [ow] "s"(16384 + wid * 2048), [s0] "s"(wsh[0]), [s1] "s"(wsh[1]), [w0] "s"(rw[0]), [w1] "s"(rw[1]), \
        [v0] "v"(vb[0][0]), [v1] "v"(vb[0][1]), [v2] "v"(vb[1][0]), [v3] "v"(vb[1][1]), [tab] "v"(tab), [qe] "s"(qe), \
        [lb] "s"(shm_base), [le] "s"(shm_base + G2_NSTAGE * G2_STAGE) : "memory", "scc", "m0"
    auto sync = [&](bool y) {
      if (y) WAIT_V(16); else WAIT_V(12);
      need();
      BAR();
      check();
    };
    auto upto = [&](bool y, int qe) {
#pragma unroll 1
      while (__builtin_expect(okb != ~0ull && q < qe, 0)) {
        sync(y);
        if (y) stage_y(); else stage_s();
      }
      if (q < qe) {
        unsigned t, t2;
        const int oa = y ? (p ^ role) * 4096 : (p >> 1) * 8192 + (p & 1) * 2048;
        if (y) asm volatile(G2_RUN("16", G2_A2) G2_RUNOP);
        else asm volatile(G2_RUN("12", "") G2_RUNOP);
      }
    };
    sync(true);
    ren = 0xffffffff00000000ull;
    if (p == 1) notify((myh & mall) << 32);
    stage_y();
    upto(true, 16 + 2 * p);
    sync(true);
    const u32x4 rwd[2] = {rw[0], rw[1]};
    const int wshd[2] = {wsh[0], wsh[1]};
    const unsigned vbd[4] = {vb[0][0], vb[0][1], vb[1][0], vb[1][1]};
    wsetup(wo, D_, F_, lane);
    const u32x4 rwo[2] = {rw[0], rw[1]};
    const int wsho[2] = {wsh[0], wsh[1]};
    unsigned vbo[5] = {vb[0][0], vb[0][1], vb[1][0], vb[1][1], (unsigned)(lane * 16 + (p >> 1) * 8192 + role * 4096 + (p & 1) * 2048 + (56 << 14))};
    asm volatile("" :: "s"(rwo[0]), "s"(rwo[1]), "s"(wsho[0]), "s"(wsho[1]));
    asm volatile("" : "+v"(vbo[0]), "+v"(vbo[1]), "+v"(vbo[2]), "+v"(vbo[3]), "+v"(vbo[4]));
    rw[0] = rwd[0]; rw[1] = rwd[1]; wsh[0] = wshd[0]; wsh[1] = wshd[1];
    vb[0][0] = vbd[0]; vb[0][1] = vbd[1]; vb[1][0] = vbd[2]; vb[1][1] = vbd[3];
    const char* ps = (const char*)slab + (size_t)(bid ^ 5) * 16384 + p * 4096;
    asm volatile("" : "+s"(ps));
    const u16* xp = x + (size_t)(m0 + role * 32) * D_ + n0;
    u16* op = out + (size_t)(m0 + role * 32) * D_ + n0;
    unsigned vo = ((tid >> 4) * 2 * D_ + (tid & 15) * 8) * 2, vi0 = img0 + (tid >> 4) * EP + (tid & 15) * 32, vi1 = vi0 + (img1 - img0);
    unsigned vo2 = vo + 64 * D_ * 2;
    asm volatile("" : "+s"(xp));
    asm volatile("" : "+s"(op));
    asm volatile("" : "+v"(vo), "+v"(vo2), "+v"(vi0), "+v"(vi1));
    stage_y();
    upto(true, ny);
    va = vbo[4];
    rw[0] = rwo[0]; rw[1] = rwo[1]; wsh[0] = wsho[0]; wsh[1] = wsho[1];
    vb[0][0] = vbo[0]; vb[0][1] = vbo[1]; vb[1][0] = vbo[2]; vb[1][1] = vbo[3];
    bool pok = true;
    upto(false, ny + (p == 0 ? 13 : (p == 1 ? 10 : 16)));
    if (p < 2) {
      sync(false);
      if (p == 1) {
        if (lane == 0) __hip_atomic_store(done2 + bid * 128, __builtin_amdgcn_s_memrealtime(), RLX_AGENT);
      } else {
        q += 2;
        need();
        --q;
        need();
        --q;
        aload(done2 + (bid ^ 5) * 128, 0);
      }
      stage_s();
      upto(false, ny + 15);
      WAIT_V(12);
      BAR();
      if (p == 0) pok = __all(fresh(aget(), t_own));
      stage_s();
    }
    WAIT_V(12);
    while (__builtin_expect(!pok, 0)) {
      aload(done2 + (bid ^ 5) * 128, 0);
      WAIT_V(0);
      pok = __all(fresh(aget(), t_own)) || GIVEUP();
      if (__builtin_expect(!cok, 0)) { notify(ren); if (lane == 0) __hip_atomic_store(done2 + bid * 128, __builtin_amdgcn_s_memrealtime(), RLX_AGENT); }
    }
    SB0();
    BAR();
    asm volatile("s_mov_b32 m0, %2\n\ts_nop 0\n\tglobal_load_lds_dwordx4 %0, %1 sc1\n\tglobal_load_lds_dwordx4 %0, %1 offset:1024 sc1\n\t"
                 "global_load_lds_dwordx4 %0, %1 offset:2048 sc1\n\tglobal_load_lds_dwordx4 %0, %1 offset:3072 sc1"
                 :: "v"(lane * 16), "s"(ps), "s"(sl + p * 4096) : "memory", "m0");
    SB0();
    WAIT_V(10);
    BAR();
    const __amdgpu_buffer_rsrc_t rx = __builtin_amdgcn_make_buffer_rsrc((void*)xp, 0, 96 * D_ * 2, 0x00020000);
    const __amdgpu_buffer_rsrc_t ro = __builtin_amdgcn_make_buffer_rsrc((void*)op, 0, 96 * D_ * 2, 0x00020000);
    u32x4 xv[4];
#pragma unroll
    for (int i = 0; i < 4; ++i) xv[i] = __builtin_bit_cast(u32x4, __builtin_amdgcn_raw_buffer_load_b128(rx, ((i & 1) ? vo2 : vo) + (i >> 1) * D_ * 2, 0, 0));
    WAIT_V(8);
    BAR();
    WAIT_V(4);
    BAR();
    f32x4 xf[8];
#pragma unroll
    for (int i = 0; i < 8; ++i) {
      const unsigned a = xv[i >> 1][(i & 1) * 2], b = xv[i >> 1][(i & 1) * 2 + 1];
      xf[i] = f32x4{lo_f(a), hi_f(a), lo_f(b), hi_f(b)};
      asm volatile("" : "+v"(xf[i]));
    }
#pragma unroll
    for (int h = 0; h < 2; ++h) {
      BAR();
      f32x4 t[4];
#pragma unroll
      for (int i = 0; i < 4; ++i) t[i] = *(const f32x4*)(shm + (h ? vi1 : vi0) + (i >> 1) * 16 * EP + (i & 1) * 16) + xf[h * 4 + i];
#pragma unroll
      for (int i = 0; i < 2; ++i)
        __builtin_amdgcn_raw_buffer_store_b128(u32x4{pack2(t[2 * i][0], t[2 * i][1]), pack2(t[2 * i][2], t[2 * i][3]), pack2(t[2 * i + 1][0], t[2 * i + 1][1]),
                                                     pack2(t[2 * i + 1][2], t[2 * i + 1][3])}, ro, (i ? vo2 : vo) + h * D_ * 2, 0, 2);
    }
    TS(5);
    if (tid == 0) __hip_atomic_store(&acks[bid], __builtin_amdgcn_s_memrealtime(), RLX_AGENT);
    if (__builtin_expect(!cok, 0) && p == 0) {
      const bool mine = lane <= 32;
      const u64* const pa = &acks[lane < 32 ? client(lane) : bid ^ 5];
      while (!__all(!mine || fresh(__hip_atomic_load(pa, RLX_AGENT), t_own))) {
        notify(ren);
        if (lane == 0) __hip_atomic_store(done2 + bid * 128, __builtin_amdgcn_s_memrealtime(), RLX_AGENT);
        __builtin_amdgcn_s_sleep(32);
        if (GIVEUP()) break;
      }
    }
  } else {
    TS(6);
    f32x4 acc[4][4], sacc[2][4];
    const int fr = lane & 15, fq = lane >> 4, q4 = fr >> 2, pp = lane & 3;
    const int ff = ((q4 >> 1) & 1) | ((fq & 1) << 1);
    int aof = G2_STAGE + wm * 8192 + lane * 16;
    int boff[4];
#pragma unroll
    for (int j = 0; j < 4; ++j) boff[j] = G2_STAGE + 16384 + wn * 8192 + (8 * fq + q4) * 128 + ((j ^ ff) * 32) + pp * 8;
#pragma unroll
    for (int b = 0; b < 4; ++b) {
#pragma unroll
      for (int a = 0; a < 4; ++a) acc[a][b] = f32x4{0.f, 0.f, 0.f, 0.f};
      sacc[0][b] = f32x4{0.f, 0.f, 0.f, 0.f}; sacc[1][b] = f32x4{0.f, 0.f, 0.f, 0.f};
    }
#define SB() __builtin_amdgcn_sched_barrier(0)
    struct Fr { bf16x8 a[4]; bf16x4 bl[4], bh[4]; };
    Fr f0[2], f1[2];
    auto rd = [&](Fr& f, lds_cptr sb, int ks, int t, bool sp) {
      constexpr int ord[12] = {0, 1, 8, 2, 3, 4, 5, 6, 7, 9, 10, 11};
      const int o = ord[t];
      if (o >= 8) f.a[o - 8] = *(const __attribute__((address_space(3))) bf16x8*)(sb + aof + (o - 8) * 2048 + ks * 1024);
      else if (o & 1) f.bh[o >> 1] = tr_read(sb + boff[o >> 1] + ks * 4096 + 512);
      else f.bl[o >> 1] = tr_read(sb + boff[o >> 1] + ks * 4096);
    };
    auto bfr = [&](const Fr& c, int j) { return __builtin_shufflevector(c.bl[j], c.bh[j], 0, 1, 2, 3, 4, 5, 6, 7); };
    int so = G2_STAGE;
    auto ystg = [&](const Fr* c, Fr* n, bool last) {
      int d = 0;
      sfor<32>([&](auto uc) __attribute__((always_inline)) {
        constexpr int u = decltype(uc)::value, t = u >> 1, mf = t >> 2, j = (t & 4) ? 3 - (t & 3) : t & 3, ks = (u ^ t) & 1;
        SB();
        asm volatile("v_mfma_f32_16x16x32_bf16 %0, %1, %2, %0" : "+v"(acc[mf][j]) : "v"(bfr(c[ks], j)), "v"(c[ks].a[mf]));
        SB();
        const int h = last ? 10 : 12;
        if (u < 2 * h) rd(n[u / h], shm3, u / h, u % h, true);
        if (u == 24) { d = so == (G2_NSTAGE - 1) * G2_STAGE ? -(G2_NSTAGE - 1) * G2_STAGE : G2_STAGE; asm volatile("" : "+s"(d)); }
        if (u == 25) { so += d; aof += d; asm volatile("" : "+v"(aof)); asm volatile("" : "+s"(so)); }
        if constexpr (u >= 26 && u < 30) { boff[u - 26] += d; asm volatile("" : "+v"(boff[u - 26])); }
      });
      SB();
      __builtin_amdgcn_s_waitcnt(0xC07F);
    };
    auto stg = [&](const Fr* c, Fr* n) {
      sfor<16>([&](auto uc) __attribute__((always_inline)) {
        constexpr int u = decltype(uc)::value, t = u >> 1, mf = t >> 2, j = (t & 4) ? 3 - (t & 3) : t & 3, ks = (u ^ t) & 1;
        SB();
        asm volatile("v_mfma_f32_16x16x32_bf16 %0, %1, %2, %0" : "+v"(sacc[mf][j]) : "v"(bfr(c[ks], j)), "v"(c[ks].a[mf]));
        SB();
        if (u < 10) { rd(n[0], shm3, 0, u, true); rd(n[1], shm3, 1, u, true); }
      });
      SB();
      __builtin_amdgcn_s_waitcnt(0xC07F);
    };
    asm volatile("" : "+v"(aof), "+v"(boff[0]), "+v"(boff[1]), "+v"(boff[2]), "+v"(boff[3]));
    asm volatile("" : "+v"(sacc[0][0]), "+v"(sacc[0][1]), "+v"(sacc[0][2]), "+v"(sacc[0][3]), "+v"(sacc[1][0]), "+v"(sacc[1][1]), "+v"(sacc[1][2]), "+v"(sacc[1][3]));
#pragma unroll
    for (int a = 0; a < 4; ++a) asm volatile("" : "+v"(acc[a][0]), "+v"(acc[a][1]), "+v"(acc[a][2]), "+v"(acc[a][3]));
    WAIT_V(0);
    arrive(std::integral_constant<int, 1>{});
    BARL();
    TS(7);
    __builtin_amdgcn_s_waitcnt(0xC07F);
    SB();
#pragma unroll
    for (int t = 0; t < 24; ++t) rd(f0[t & 1], shm3, t & 1, t >> 1, false);
    auto nxt = [&]() {
      const int d = so == (G2_NSTAGE - 1) * G2_STAGE ? -(G2_NSTAGE - 1) * G2_STAGE : G2_STAGE;
      so += d; aof += d; boff[0] += d; boff[1] += d; boff[2] += d; boff[3] += d;
      asm volatile("" : "+v"(aof), "+v"(boff[0]), "+v"(boff[1]), "+v"(boff[2]), "+v"(boff[3]));
    };
    nxt();
    __builtin_amdgcn_s_waitcnt(0xC07F);
#pragma unroll 1
    for (int g = 0; g < ny - 2; g += 2) {
      ystg(f0, f1, false);
      BAR();
      ystg(f1, f0, false);
      BAR();
    }
    if (!(DLT & 1)) { ystg(f0, f1, false); BAR(); }
    Fr sa[2], sb2[2];
    ystg((DLT & 1) ? f0 : f1, sa, true);
    asm volatile("" : "+v"(acc[0][0]), "+v"(acc[0][1]), "+v"(acc[0][2]), "+v"(acc[0][3]), "+v"(acc[1][0]), "+v"(acc[1][1]), "+v"(acc[1][2]), "+v"(acc[1][3]));
    TS(2);
    {
      const __amdgpu_buffer_rsrc_t rs = __builtin_amdgcn_make_buffer_rsrc((void*)((char*)slab + (size_t)bid * 16384), 0, 16384, 0x00020000);
#pragma unroll
      for (int f = 0; f < 4; ++f) {
        if (f) nxt();
        BAR();
        if (f & 1) stg(sb2, sa); else stg(sa, sb2);
        asm volatile("" : "+v"(acc[2 + (f >> 1)][(f & 1) * 2]), "+v"(acc[2 + (f >> 1)][(f & 1) * 2 + 1]));
        const f32x4 a = acc[2 + (f >> 1)][(f & 1) * 2], b = acc[2 + (f >> 1)][(f & 1) * 2 + 1];
        __builtin_amdgcn_raw_buffer_store_b128(u32x4{pack2(a[0], a[1]), pack2(a[2], a[3]), pack2(b[0], b[1]), pack2(b[2], b[3])}, rs,
                                               (f * 256 + tid) * 16, 0, 16);
        SB();
      }
    }
#pragma unroll 1
    for (int i = 2; i < 7; ++i) {
      nxt();
      BAR();
      stg(sa, sb2);
      nxt();
      BAR();
      stg(sb2, sa);
      WAIT_V(0);
    }
    nxt();
    BAR();
    stg(sa, sb2);
    int vi0 = img0 + (wm * 16 + fr) * EP + wn * 256 + fq * 16, vi1 = vi0 + (img1 - img0);
    asm volatile("" : "+v"(vi0), "+v"(vi1));
    BAR();
    u32x4 pv[4];
    {
      nxt();
      lds_cptr sb = shm3 + so + tid * 16;
#pragma unroll
      for (int f = 0; f < 4; ++f) pv[f] = *(const __attribute__((address_space(3))) u32x4*)(sb + f * 4096);
    }
    f32x4 pf[8];
    sfor<16>([&](auto uc) __attribute__((always_inline)) {
      constexpr int u = decltype(uc)::value, t = u >> 1, mf = t >> 2, j = (t & 4) ? 3 - (t & 3) : t & 3, ks = (u ^ t) & 1;
      SB();
      asm volatile("v_mfma_f32_16x16x32_bf16 %0, %1, %2, %0" : "+v"(sacc[mf][j]) : "v"(bfr(sb2[ks], j)), "v"(sb2[ks].a[mf]));
      SB();
      if constexpr (u >= 8) {
        const unsigned w0 = pv[(u - 8) >> 1][(u & 1) * 2], w1 = pv[(u - 8) >> 1][(u & 1) * 2 + 1];
        pf[u - 8] = f32x4{lo_f(w0), hi_f(w0), lo_f(w1), hi_f(w1)};
        asm volatile("" : "+v"(pf[u - 8]));
      }
    });
    SB();
    TS(1);
#pragma unroll
    for (int mf = 0; mf < 2; ++mf) {
      f32x4 ya[4], e[4];
#pragma unroll
      for (int j = 0; j < 4; ++j) { ya[j] = acc[mf][j] + pf[mf * 4 + j]; e[j] = ya[j] * -1.4426950408889634f; }
      SB();
#pragma unroll
      for (int j = 0; j < 4; ++j) {
        e[j] = f32x4{__builtin_amdgcn_exp2f(e[j][0]), __builtin_amdgcn_exp2f(e[j][1]), __builtin_amdgcn_exp2f(e[j][2]), __builtin_amdgcn_exp2f(e[j][3])};
        ya[j] *= sacc[mf][j];
      }
      SB();
#pragma unroll
      for (int j = 0; j < 4; ++j) e[j] += 1.0f;
      SB();
#pragma unroll
      for (int j = 0; j < 4; ++j)
        *(f32x4*)(shm + (mf ? vi1 : vi0) + j * 64) =
            ya[j] * f32x4{__builtin_amdgcn_rcpf(e[j][0]), __builtin_amdgcn_rcpf(e[j][1]), __builtin_amdgcn_rcpf(e[j][2]), __builtin_amdgcn_rcpf(e[j][3])};
      SB();
      BARL();
      SB();
    }
    TS(3);
  }
  TSFLUSH();
  }
  };
  if (wid < 4) kloop(std::integral_constant<int, 0>{});
  else kloop(std::integral_constant<int, 1>{});
  asm volatile(".Lcode_end:");
}

extern "C" void kernel_launch(void* const* d_in, const int* in_sizes, int n_in, void* d_out, int out_size, void* d_ws,
                              size_t ws_size, hipStream_t stream) {
  const u16* A_x = (const u16*)d_in[0];
  const u16* A_wqkv = (const u16*)d_in[1];
  const u16* A_wo = (const u16*)d_in[2];
  const u16* B_wg = (const u16*)d_in[4];
  const u16* B_wu = (const u16*)d_in[5];
  const u16* B_wd = (const u16*)d_in[6];
  u16* out = (u16*)d_out;
  fused_kernel<<<256, 512, 0, stream>>>(A_x, A_wqkv, B_wg, B_wu, B_wd, A_wo, out, (char*)d_ws);
}
```

```cpp
#include <hip/hip_runtime.h>
#include <hip/hip_bf16.h>
#include <stdint.h>
#include <type_traits>
#include <utility>

typedef unsigned short u16;
using bf16x8 = __attribute__((ext_vector_type(8))) short;
using bf16x4 = __attribute__((ext_vector_type(4))) short;
using f32x4 = __attribute__((ext_vector_type(4))) float;
using u32x2 = __attribute__((ext_vector_type(2))) unsigned;
using u32x4 = __attribute__((ext_vector_type(4))) unsigned;
typedef __attribute__((address_space(3))) const char* lds_cptr;

template <class F, int... I> __device__ __forceinline__ void sfor_(F&& f, std::integer_sequence<int, I...>) { (f(std::integral_constant<int, I>{}), ...); }
template <int N, class F> __device__ __forceinline__ void sfor(F&& f) { sfor_(f, std::make_integer_sequence<int, N>{}); }

constexpr int D_ = 1024, F_ = 3584;

__device__ __forceinline__ float bf2f(u16 h) { return __uint_as_float((unsigned)h << 16); }
__device__ __forceinline__ u16 f2bf(float f) {
  unsigned u = __float_as_uint(f);
  u += 0x7fffu + ((u >> 16) & 1u);
  return (u16)(u >> 16);
}
__device__ __forceinline__ float rbf(float f) { return bf2f(f2bf(f)); }
typedef __attribute__((ext_vector_type(2))) __bf16 bf16x2_t;
typedef __attribute__((ext_vector_type(2))) float f32x2;
__device__ __forceinline__ unsigned pack2(float a, float b) {
  bf16x2_t r = __builtin_convertvector((f32x2){a, b}, bf16x2_t);
  return __builtin_bit_cast(unsigned, r);
}
__device__ __forceinline__ float lo_f(unsigned p) { return __uint_as_float(p << 16); }
__device__ __forceinline__ float hi_f(unsigned p) { return __uint_as_float(p & 0xffff0000u); }

#define TSDECL
#define TSS(i) do {} while (0)
#define TSFLUSH() do {} while (0)
#define TS1(i) TSS(i)
#define TS(i) TSS(8 + (i))
#define WAIT_V(n) asm volatile("s_waitcnt vmcnt(%0)" ::"n"(n) : "memory")
#define WAIT_L(n) asm volatile("s_waitcnt lgkmcnt(%0)" ::"n"(n) : "memory")
#define BARL() do { WAIT_L(0); BAR(); } while (0)
#define BAR() do { asm volatile("" ::: "memory"); __builtin_amdgcn_s_barrier(); asm volatile("" ::: "memory"); } while (0)

__device__ __forceinline__ bf16x4 tr_read(lds_cptr p) {
  return __builtin_amdgcn_ds_read_tr16_b64_v4i16((__attribute__((address_space(3))) bf16x4*)p);
}
__device__ __forceinline__ bf16x8 tr_frag(lds_cptr p) {
  bf16x4 lo = tr_read(p), hi = tr_read(p + 512);
  bf16x8 r;
  r[0] = lo[0]; r[1] = lo[1]; r[2] = lo[2]; r[3] = lo[3];
  r[4] = hi[0]; r[5] = hi[1]; r[6] = hi[2]; r[7] = hi[3];
  return r;
}
__device__ __forceinline__ void glds16(const void* gsrc, unsigned lds_dst) {
  unsigned keep;
  asm volatile("s_mov_b32 %0, m0\n\ts_mov_b32 m0, %2\n\ts_nop 0\n\tglobal_load_lds_dwordx4 %1, off\n\ts_mov_b32 m0, %0"
               : "=&s"(keep) : "v"(gsrc), "s"(lds_dst) : "memory");
}
#define GLDS(gptr, laddr) glds16((const void*)(gptr), (unsigned)(laddr))
__device__ __forceinline__ void glds16_sc1(const void* gsrc, unsigned lds_dst) {
  unsigned keep;
  asm volatile("s_mov_b32 %0, m0\n\ts_mov_b32 m0, %2\n\ts_nop 0\n\tglobal_load_lds_dwordx4 %1, off sc1\n\ts_mov_b32 m0, %0"
               : "=&s"(keep) : "v"(gsrc), "s"(lds_dst) : "memory");
}
#define GLDS_SC1(gptr, laddr) glds16_sc1((const void*)(gptr), (unsigned)(laddr))
__device__ __forceinline__ void glds16_nc(const void* gsrc, unsigned lds_dst) {
  unsigned keep;
  asm volatile("s_mov_b32 %0, m0\n\ts_mov_b32 m0, %2\n\ts_nop 0\n\tglobal_load_lds_dwordx4 %1, off\n\ts_mov_b32 m0, %0"
               : "=&s"(keep) : "v"(gsrc), "s"(lds_dst));
}
#define GLDS_NC(gptr, laddr) glds16_nc((const void*)(gptr), (unsigned)(laddr))
__device__ __forceinline__ void glds16_s(const void* sbase, unsigned voff, unsigned lds_dst) {
  asm volatile("s_mov_b32 m0, %2\n\ts_nop 0\n\tglobal_load_lds_dwordx4 %0, %1" :: "v"(voff), "s"(sbase), "s"(lds_dst) : "m0");
}

__device__ __forceinline__ void glds16_p(const void* sbase, unsigned voff, unsigned lds_dst) {
  asm volatile("s_mov_b32 m0, %2\n\ts_nop 3\n\tglobal_load_lds_dwordx4 %0, %1" :: "v"(voff), "s"(sbase), "s"(lds_dst) : "m0");
}

typedef unsigned long long u64;
__device__ __forceinline__ bool fresh(u64 stamp, u64 t_own) {
  return stamp - t_own - 100ull < __builtin_amdgcn_s_memrealtime() - t_own;
}
constexpr u64 TIMEOUT_ = 200000;
#define GIVEUP() (__builtin_amdgcn_s_memrealtime() - t_own > 100000000ull)
__device__ __forceinline__ float half_sum(float v) {
  v += __builtin_bit_cast(float, __builtin_amdgcn_update_dpp(0, __builtin_bit_cast(int, v), 0xB1, 0xf, 0xf, false));
  v += __builtin_bit_cast(float, __builtin_amdgcn_update_dpp(0, __builtin_bit_cast(int, v), 0x4E, 0xf, 0xf, false));
  v += __builtin_bit_cast(float, __builtin_amdgcn_update_dpp(0, __builtin_bit_cast(int, v), 0x141, 0xf, 0xf, false));
  v += __builtin_bit_cast(float, __builtin_amdgcn_update_dpp(0, __builtin_bit_cast(int, v), 0x140, 0xf, 0xf, false));
  { auto r = __builtin_amdgcn_permlane16_swap(__float_as_uint(v), __float_as_uint(v), false, false); v = __uint_as_float(r[0]) + __uint_as_float(r[1]); }
  return v;
}
__device__ __forceinline__ unsigned pack_stats(float mean, float var) {
  return (unsigned)f2bf(mean) | ((unsigned)f2bf(sqrtf(rbf(rbf(var) + 1e-5f))) << 16);
}
__device__ __forceinline__ unsigned row_stats_serial(const u16* xr) {
  float s = 0.f, q = 0.f;
#pragma unroll 1
  for (int k = 0; k < D_; ++k) s += bf2f(xr[k]);
  const float mean = s * (1.0f / D_);
#pragma unroll 1
  for (int k = 0; k < D_; ++k) { const float d = bf2f(xr[k]) - mean; q += d * d; }
  return pack_stats(mean, q * (1.0f / D_));
}
constexpr int G1_STAGE = 32768 + 5 * 8192;
constexpr int CP_K0 = 2, CP_K1 = 8;
constexpr int PUB_K = 5;
constexpr int PF_K0 = 6;
constexpr int SD = 6, KS0 = 2;
constexpr int NP1 = 8;
constexpr int NSC = 2;
constexpr int DLT = 1;
static_assert(DLT <= 3, "");

#define RLX_AGENT __ATOMIC_RELAXED, __HIP_MEMORY_SCOPE_AGENT

constexpr int G2_STAGE = 16384 + 2 * 8192;
constexpr int G2_NSTAGE = 5;
constexpr size_t MB_ = 1024 * 1024;
__global__ __launch_bounds__(512, 2) __attribute__((amdgpu_num_vgpr(127))) void fused_kernel(const u16* __restrict__ x, const u16* __restrict__ wqkv,
                                                       const u16* __restrict__ wg, const u16* __restrict__ wu,
                                                       const u16* __restrict__ wd, const u16* __restrict__ wo,
                                                       u16* __restrict__ out, char* __restrict__ ws) {
  u16* const halo = (u16*)(ws + 4 * MB_);
  u16* const abuf = (u16*)(ws + 16 * MB_);
  char* const slab = ws + 64 * MB_;
  unsigned* const flags = (unsigned*)(ws + 96 * MB_);
  u32x4* const rstat = (u32x4*)(ws + 97 * MB_);
  char* const wcopy = ws + 104 * MB_;
  u64* const done1 = (u64*)(ws + 102 * MB_ + 512);
  u64* const done2 = (u64*)(ws + 102 * MB_);
  u64* const donec = (u64*)(ws + 100 * MB_ + 196608);
  u64* const acks = (u64*)(ws + 100 * MB_ + 262144);
  u64* const cstamp = (u64*)(ws + 100 * MB_ + 327680);
  float* const csum = (float*)(ws + 101 * MB_);
  __shared__ __attribute__((aligned(1024))) char shm[163840];
  constexpr int AUX = 2 * G1_STAGE + 8192;
  {
    u64 pc; int len;
    asm volatile("s_getpc_b64 %0\n.Lcode_beg:\n\ts_mov_b32 %1, .Lcode_end-.Lcode_beg" : "=s"(pc), "=s"(len));
    const int ol = ((blockIdx.x >> 3) + 32 * (threadIdx.x >> 6)) * 1024 + (threadIdx.x & 63) * 16;
    asm volatile("s_mov_b32 m0, %2\n\ts_nop 0\n\tglobal_load_lds_dwordx4 %0, %1"
                 :: "v"(ol < len - 16 ? ol : 0), "s"(pc & ~15ull), "s"((unsigned)(uintptr_t)(lds_cptr)shm + 2 * G1_STAGE + 7168) : "m0");
    __builtin_amdgcn_sched_barrier(0);
  }
  const u64 t_own = __builtin_amdgcn_s_memrealtime();
  __builtin_amdgcn_sched_barrier(0);
  TSDECL;
  const int tid = threadIdx.x, lane = tid & 63;
  const int wid = __builtin_amdgcn_readfirstlane(tid >> 6);
  const int bid = blockIdx.x, xcd = bid & 7, idx = bid >> 3;
  const int ntile = xcd * 4 + (idx & 3), mtile = idx >> 2;
  const int m0 = mtile * 256;
  const int hg = ntile >> 1;
  const bool tya = (ntile & 1) == 0;
  const int grp = wid >> 2, wc = (wid >> 1) & 1, wm = (((grp ^ ntile ^ 1) & 1) << 1) | (wid & 1), vid = wc * 4 + wm;
  const int srow = m0 + ntile * 8 + (wid & 3) * 2;
  lds_cptr shm3 = (lds_cptr)shm;
  const unsigned shm_base = (unsigned)(uintptr_t)shm3;
  const int r8 = tid >> 3, pos = tid & 7;
  const int phys = pos >> 1, half = pos & 1;
  const int fk = ((r8 >> 1) & 1) | (((r8 >> 3) & 1) << 1);
  const int vsrc = phys ^ fk;
  const int qstr = tya ? 3072 * 2 : F_ * 2;
  unsigned voa = (((r8 & 32) + 2 * (r8 & 15) + ((r8 >> 4) & 1)) * D_ + ((pos ^ (r8 & 7)) * 8)) * 2;
  unsigned vog = r8 * (F_ * 2) + vsrc * 32 + half * 16, voq = r8 * qstr + vsrc * 32 + half * 16;
  unsigned vom = r8 * qstr + (vsrc >> (tya ? 0 : 1)) * 32 + half * 16;
  asm volatile("" : "+v"(voa), "+v"(vog), "+v"(voq), "+v"(vom));
  asm volatile("" :: "s"(x), "s"(wqkv), "s"(wg), "s"(wu), "s"(wd), "s"(wo), "s"(out), "s"(ws));
  const char* const ab = (const char*)(x + (size_t)m0 * D_);
  int kz = 0;
  asm volatile("" : "+s"(kz));
  auto stage_a = [&](int buf, int kt, int i, int h) {
    glds16_s(ab + (long)(((i * 64 + (h - 1) * 32) * D_ + kt * 64) * 2), voa, shm_base + buf * G1_STAGE + (wid - 4 + 4 * h) * 1024 + i * 8192);
  };
#pragma unroll
  for (int i = 0; i < 4; ++i) stage_a(0, 0, i, 1);
  const int kt0 = (hg >> 1) * 7, he = hg & 1;
  const char* const gb = (const char*)(wg + (kt0 + (tya ? 5 + he : 2 + he)) * 64);
  const char* const qb = tya ? (const char*)(wqkv + 64 * hg) : gb - 256;
  const long dgu = (const char*)wu - (const char*)wg, dp1 = tya ? 2048 : dgu;
  const char* const mb0 = tya ? qb + 4096 : (const char*)(wg + (kt0 + 4) * 64 + 32 * he);
  const char* mptr = mb0 + (((vsrc & 1) && !tya) ? dgu : 0) + vom;
  auto wload = [&](int p, int rows, unsigned dst) {
    long off = (long)(rows * (p < 3 ? qstr : F_ * 2)) + (p == 1 ? dp1 : (p == 4 ? dgu : 0));
    if (p == 2) { asm volatile("" : "+s"(off)); GLDS_NC(mptr + off, dst); }
    else glds16_s((p < 2 ? qb : gb) + off, p < 2 ? voq : vog, dst);
  };
#pragma unroll
  for (int i = 0; i < 5; ++i) wload(i, 0, shm_base + wid * 1024 + 32768 + i * 8192);
  __builtin_amdgcn_sched_barrier(0);
  const int fr = lane & 15, fq = lane >> 4, q4 = fr >> 2, pp = lane & 3;
  const int ff = ((q4 >> 1) & 1) | ((fq & 1) << 1);
  int aoff[2], boff[2];
#pragma unroll
  for (int ks = 0; ks < 2; ++ks) aoff[ks] = (wm * 64 + fr) * 128 + (((ks * 4 + fq) ^ (fr & 7)) * 16);
#pragma unroll
  for (int j = 0; j < 2; ++j) boff[j] = 32768 + (8 * fq + q4) * 128 + (((2 * wc + j) ^ ff) * 32) + pp * 8;

  f32x4 acc[4][10];
#pragma unroll
  for (int a = 0; a < 4; ++a)
#pragma unroll
    for (int b = 0; b < 10; ++b) acc[a][b] = f32x4{0.f, 0.f, 0.f, 0.f};

  auto stage_part = [&](int buf, int kt, int i, int h) {
    if (i < 5) wload(i, kt * 64 + (h - 1) * 32, shm_base + buf * G1_STAGE + (wid - 4 + 4 * h) * 1024 + 32768 + i * 8192);
    else stage_a(buf, kt, i - 5, h);
  };
  constexpr int NT = D_ / 64;
  constexpr int DMP = 2 * G1_STAGE;
  constexpr int CF = AUX + 4096, QS = CF + 1280, CNT = CF + 3840;
  bf16x8 af0[4], af1[4];
  bf16x4 bl[10], bh[10];
#pragma unroll
  for (int i = 0; i < 4; ++i) { af1[i] = bf16x8{0, 0, 0, 0, 0, 0, 0, 0}; bl[i] = bf16x4{0, 0, 0, 0}; bh[i] = bf16x4{0, 0, 0, 0}; }
#define SB0() __builtin_amdgcn_sched_barrier(0)
  auto mm = [&](int n, int f, int ks, int mf) {
    const bf16x8 b = __builtin_shufflevector(bl[n % 10], bh[n % 10], 0, 1, 2, 3, 4, 5, 6, 7);
    if ((f >> 1) == 2) acc[mf][f] = __builtin_amdgcn_mfma_f32_16x16x32_bf16(ks ? af1[mf] : af0[mf], b, acc[mf][f], 0, 0, 0);
    else acc[mf][f] = __builtin_amdgcn_mfma_f32_16x16x32_bf16(b, ks ? af1[mf] : af0[mf], acc[mf][f], 0, 0, 0);
  };
  auto correct = [&]() {
    int l = threadIdx.x & 63;
    asm volatile("s_nop 7" : "+v"(l));
    const float* const tb = (const float*)(shm + DMP + wm * 2048);
    const float* const cf = (const float*)(shm + CF) + wc * 160;
    auto ld = [&](int f) { return f < 8 ? *(const f32x4*)(cf + (f < 4 ? f : f + 2) * 16 + (l >> 4) * 4) : *(const f32x4*)(tb + (f - 8) * 16 + (l >> 4) * 4); };
    float m[4];
    f32x4 cr[3];
#pragma unroll
    for (int mf = 0; mf < 4; ++mf) m[mf] = tb[mf * 16 + (l & 15)];
    cr[0] = ld(0);
    cr[1] = ld(1);
    const float c4 = cf[64 + (l & 15)], c5 = cf[80 + (l & 15)];
#pragma unroll
    for (int f = 0; f < 12; ++f) {
      if (f + 2 < 12) cr[(f + 2) % 3] = ld(f + 2);
      SB0();
      const f32x4 c = cr[f % 3];
#pragma unroll
      for (int i = 0; i < (f < 8 ? 4 : 2); ++i) {
        f32x4& a = f < 8 ? acc[i][f < 4 ? f : f + 2] : acc[f - 8][4 + i];
        float a0 = a[0], a1 = a[1], a2 = a[2], a3 = a[3];
        asm volatile("v_fmac_f32 %0, %4, %5\n\tv_fmac_f32 %1, %4, %6\n\tv_fmac_f32 %2, %4, %7\n\tv_fmac_f32 %3, %4, %8"
                     : "+v"(a0), "+v"(a1), "+v"(a2), "+v"(a3) : "v"(f < 8 ? m[i] : (i ? c5 : c4)), "v"(c[0]), "v"(c[1]), "v"(c[2]), "v"(c[3]));
        a = f32x4{a0, a1, a2, a3};
      }
      SB0();
    }
    asm volatile("s_nop 1");
  };
  auto kloop = [&](auto wmc) __attribute__((always_inline)) {
    constexpr bool LD = decltype(wmc)::value != 0;
    constexpr int grp = LD ? 1 : 0;
    const char* pa = nullptr;
    unsigned vob = 0;
    f32x4 ca = {0.f, 0.f, 0.f, 0.f}, cb = {0.f, 0.f, 0.f, 0.f};
    bf16x8 sel = {0, 0, 0, 0, 0, 0, 0, 0};
    int tro = 0;
    u64 tg = 0;
    const int psh = (wid == 2 && !tya) ? 1 : 0, pstr = wid < 3 ? qstr : F_ * 2;
    const char* const pbase = wid == 0 ? qb : (wid == 1 ? qb + dp1 : (wid == 2 ? mb0 : gb));
    if (!LD) {
      const int rs = mtile * 8 + (lane >> 3), vs = ((lane & 7) >> 1) ^ (((rs >> 1) & 1) | ((mtile & 1) << 1));
      pa = pbase + ((psh & vs) ? dgu : 0) + (rs * pstr + (vs >> psh) * 32 + (lane & 1) * 16);
      vob = rs * (F_ * 2) + vs * 32 + (lane & 1) * 16;
      const short one = fr == fq ? 0x3f80 : 0;
      sel = bf16x8{one, one, one, one, one, one, one, one};
      tro = q4 * 128 + ((fq ^ ((q4 >> 1) | ((mtile & 1) << 1))) * 32) + pp * 8;
      asm volatile("" : "+v"(sel), "+v"(tro), "+v"(ca), "+v"(cb));
    }
    auto share = [&](int t, unsigned dsta, unsigned dstb) {
      long off = (long)(t * 64 * pstr);
      asm volatile("" : "+s"(off));
      GLDS_NC(pa + off, shm_base + dsta);
      if (wid == 0) glds16_p(gb + dgu + (long)(t * 64 * F_ * 2), vob, shm_base + dstb);
    };
    auto accum = [&](int oa, int ob) {
      ca = __builtin_amdgcn_mfma_f32_16x16x32_bf16(tr_frag(shm3 + tro + oa), sel, ca, 0, 0, 0);
      if (wid == 0) cb = __builtin_amdgcn_mfma_f32_16x16x32_bf16(tr_frag(shm3 + tro + ob), sel, cb, 0, 0, 0);
    };
    auto cgather = [&]() {
      int l = threadIdx.x & 63;
      asm volatile("s_memrealtime %0\n\ts_waitcnt lgkmcnt(0)" : "=s"(tg), "+v"(l) :: "memory");
      const char* const p4 = l < 32 ? (const char*)(csum + (ntile * 5 + 4) * 512 + wid * 16) + (l >> 2) * 256
                                    : (const char*)(cstamp + (ntile * 4 + (l < 36 ? wid : 0)) * 8);
      if (l < 40) GLDS_SC1(p4 + (l & 3) * 16, shm_base + QS + wid * 640);
#pragma unroll
      for (int i = 0; i < 2; ++i) GLDS_SC1((const char*)(csum + (ntile * 5 + wid) * 512) + i * 1024 + l * 16, shm_base + DMP + wid * 2048 + i * 1024);
    };
    if (!LD) share(2, DMP + 7168, DMP + 7168);
    if (LD) {
#pragma unroll
      for (int i = 0; i < NP1; ++i) {
        stage_part(1, kz + 1, i >> 1, i & 1);
        asm volatile("" : "+v"(acc[0][i]), "+v"(acc[1][i]), "+v"(acc[2][i]), "+v"(acc[3][i]));
      }
    }
#pragma unroll
    for (int a = 0; a < 4; ++a)
      asm volatile("" : "+v"(acc[a][0]), "+v"(acc[a][1]), "+v"(acc[a][2]), "+v"(acc[a][3]), "+v"(acc[a][4]), "+v"(acc[a][5]), "+v"(acc[a][6]),
                   "+v"(acc[a][7]), "+v"(acc[a][8]), "+v"(acc[a][9]));
    asm volatile("" : "+v"(aoff[0]), "+v"(aoff[1]), "+v"(boff[0]), "+v"(boff[1]));
    if constexpr (LD) {
      WAIT_V(NP1);
      BAR();
    } else {
      if (wid) WAIT_V(1);
      WAIT_V(2);
    }
    TS1(0);
#pragma clang loop unroll(disable)
    for (int kt = 0; kt < NT; ++kt) {
      int cur = kt & 1;
      if (!LD && kt == NT - 1) { correct(); __builtin_amdgcn_s_setprio(2); }
      if (!LD) {
        asm volatile("" : "+s"(cur));
        __builtin_amdgcn_s_waitcnt(0xC07F);
        asm volatile(".p2align 5");
        BAR();
      }
      lds_cptr sb = shm3;
      if (LD) {
        unsigned k2 = kt - 1;
        asm volatile("" : "+s"(k2));
        if (k2 < NT - 2) {
#pragma unroll
          for (int i = 0; i < NP1; ++i) stage_part(cur ^ 1, kt + 1, i >> 1, i & 1);
        }
      }
      if (LD && kt + 1 < NT) {
#pragma unroll
        for (int i = NP1; i < 18; ++i) stage_part(cur ^ 1, kt + 1, i >> 1, i & 1);
      } else if (LD) {
        correct();
      }
      auto fadr = [&](int n) {
        const int f = n < 2 ? 8 + n : (n < 4 ? 11 - n : (n - 4) >> 1), ks = n < 2 ? 1 : (n < 4 ? 0 : n & 1);
        return sb + boff[f & 1] + (f >> 1) * 8192 + ks * 4096;
      };
      auto rdl = [&](int n) { bl[n % 10] = tr_read(fadr(n % 20)); };
      auto rdh = [&](int n) { bh[n % 10] = tr_read(fadr(n % 20) + 512); };
      auto rda = [&](bf16x8* af, int ks, int mf) { af[mf] = *(const __attribute__((address_space(3))) bf16x8*)(sb + aoff[ks] + mf * 2048); };
      SB0();
      if (LD) __builtin_amdgcn_s_waitcnt(0xC07F);
      if (LD) asm volatile(".p2align 5\n\ts_nop 0\n\ts_nop 0");
      sfor<80>([&](auto uc) __attribute__((always_inline)) {
        constexpr int u = decltype(uc)::value;
        SB0();
        if constexpr (u < 16) {
          const int n = u >> 2;
          mm(n, n < 2 ? 8 + n : 11 - n, n < 2 ? 1 : 0, (n & 1) ? 3 - (u & 3) : u & 3);
        } else {
          const int f = (u - 16) >> 3, i = (u >> 1) & 3, ks = (u ^ i) & 1;
          if ((u & 7) == 0) { asm volatile("" :: "v"(bl[(5 + 2 * f) % 10]), "v"(bh[(5 + 2 * f) % 10])); SB0(); }
          mm(4 + 2 * f + ks, f, ks, (f & 1) ? 3 - i : i);
        }
        SB0();
        if (u == 0) { rdl(2); rdh(2); }
        if (u == 1) { rda(af0, 0, 0); rda(af0, 0, 1); }
        if (u == 2) { rda(af0, 0, 2); rda(af0, 0, 3); }
        if (u == 3) { rdl(3); rdh(3); }
        if constexpr (u >= 4 && u < 8) rda(af1, 1, 7 - u);
        if (u >= 8 && u < 16) { if (u & 1) rdh(u >> 1); else rdl(u >> 1); }
        if (u >= 21 && u < 76 && (u & 1)) {
          const int n = 8 + ((u - 21) >> 2);
          if ((u - 21) & 2) rdh(n); else rdl(n);
        }
      });
      SB0();
      if (LD)
        asm volatile("s_cmp_eq_u32 %0, %1\n\ts_waitcnt vmcnt(0) lgkmcnt(0)\n\ts_cbranch_scc1 1f\n\ts_barrier\n1:" :: "s"(kt), "n"(NT - 1) : "memory", "scc");
      SB0();
      {
        const int d = cur ? -G1_STAGE : G1_STAGE;
        aoff[0] += d; aoff[1] += d; boff[0] += d; boff[1] += d;
        asm volatile("" : "+v"(aoff[0]), "+v"(aoff[1]), "+v"(boff[0]), "+v"(boff[1]));
      }
      if (!LD && kt <= 19 - SD) {
        int lane = threadIdx.x & 63;
        asm volatile("" : "+v"(lane));
        auto clock = [&]() { u64 now; asm volatile("s_memrealtime %0\n\ts_waitcnt lgkmcnt(0)" : "=s"(now) :: "memory"); return now; };
        WAIT_V(0);
        if (kt <= 16 - SD) {
          if (kt >= 1 + KS0) { accum(DMP + wid * 1024, DMP + 4096); WAIT_L(0); }
          if (kt <= 15 - SD) {
            if (kt >= KS0) share(kt + SD, DMP + wid * 1024, DMP + 4096);
          } else {
            GLDS_SC1(rstat + m0 + wid * 64 + (lane & 32) + 2 * (lane & 15) + ((lane >> 4) & 1), shm_base + AUX + wid * 1024);
          }
          if (kt == 16 - SD && (lane & 15) < 4) {
            const __amdgpu_buffer_rsrc_t rc = __builtin_amdgcn_make_buffer_rsrc((void*)(csum + ntile * 5 * 512 + mtile * 64), 0, 5 * 2048, 0x00020000);
            const int co = (lane & 15) * 64 + (lane >> 4) * 16;
            __builtin_amdgcn_raw_buffer_store_b128(__builtin_bit_cast(u32x4, ca), rc, co + wid * 2048, 0, 16);
            if (wid == 0) __builtin_amdgcn_raw_buffer_store_b128(__builtin_bit_cast(u32x4, cb), rc, co + 4 * 2048, 0, 16);
          }
          if (kt < SD + KS0) {
            if (kt <= SD + KS0 - 4) share(kt + 3, DMP + 7168, DMP + 7168);
            const int sp = cur * G1_STAGE + 32768 + mtile * 1024;
            accum(sp + wid * 8192, sp + 4 * 8192);
            if (kt == 1 || kt == 2) {
              const bf16x8 ones = {0x3f80, 0x3f80, 0x3f80, 0x3f80, 0x3f80, 0x3f80, 0x3f80, 0x3f80};
              const char* const xr = shm + AUX + wid * 2048 + (lane & 8) * 128 + (lane >> 4) * 128 + (lane & 7) * 16;
              const bf16x8 r0 = *(const bf16x8*)xr, r1 = *(const bf16x8*)(xr + 512);
              const f32x4 z = {0.f, 0.f, 0.f, 0.f};
              const f32x4 s1 = __builtin_amdgcn_mfma_f32_16x16x32_bf16(ones, r1, __builtin_amdgcn_mfma_f32_16x16x32_bf16(ones, r0, z, 0, 0, 0), 0, 0, 0);
              const f32x4 s2 = __builtin_amdgcn_mfma_f32_16x16x32_bf16(r1, r1, __builtin_amdgcn_mfma_f32_16x16x32_bf16(r0, r0, z, 0, 0, 0), 0, 0, 0);
              float t0 = __uint_as_float((unsigned)tg) + s1[0];
              asm volatile("" : "+v"(t0));
              const float t1 = __uint_as_float((unsigned)(tg >> 32)) + ((lane & 2) ? ((lane & 1) ? s2[3] : s2[2]) : ((lane & 1) ? s2[1] : s2[0]));
              tg = ((u64)__float_as_uint(t1) << 32) | __float_as_uint(t0);
            }
            if (kt < 2) {
#pragma unroll
              for (int i = 0; i < 2; ++i) glds16_p(x + (size_t)(srow + i) * D_ + kt * 512, lane * 16, shm_base + AUX + wid * 2048 + i * 1024);
            }
            if (kt == PUB_K) {
              if (wid == 0 && lane < 8) *(unsigned*)(shm + CNT + lane * 4) = 0u;
              const bool dg = ((lane >> 2) & 3) == (lane >> 4);
              float sm = half_sum(dg ? __uint_as_float((unsigned)tg) : 0.f);
              asm volatile("" : "+v"(sm));
              const float sq = half_sum(dg ? __uint_as_float((unsigned)(tg >> 32)) : 0.f);
              const u64 now = clock();
              if ((lane & 31) == 0) {
                const __amdgpu_buffer_rsrc_t rs = __builtin_amdgcn_make_buffer_rsrc((void*)(rstat + srow), 0, 32, 0x00020000);
                __builtin_amdgcn_raw_buffer_store_b128(u32x4{__float_as_uint(sm), (unsigned)now, __float_as_uint(sq), (unsigned)now ^ (unsigned)(now >> 32)}, rs,
                                                       lane >> 1, 0, 16);
              }
            }
          }
          if (kt >= PF_K0 && kt < PF_K0 + 5) {
#pragma unroll
            for (int i = 0; i < 2; ++i) {
              const int c = (bid * 4 + wid) * 10 + (kt - PF_K0) * 2 + i;
              if (c < 9216) {
                const u16* src = (c < 7168 ? wd + (size_t)c * 512 : wo + (size_t)(c - 7168) * 512) + lane * 8;
                GLDS_NC(src, shm_base + DMP + 7168);
              }
            }
          }
          if (kt >= CP_K0 && kt <= CP_K1) {
            if (kt < CP_K1 - 1) {
              const int r1 = kt - CP_K0 - wid, ja = bid * 4 + wid, jb = (wid == 0 && bid < 128) ? 1024 + bid : 2048;
              const int q1 = r1 == 0 ? ja : (r1 == 1 ? jb : 2048), q0 = r1 == 1 ? ja : (r1 == 2 ? jb : 2048);
              if (q0 < 1152) {
                const __amdgpu_buffer_rsrc_t rc = __builtin_amdgcn_make_buffer_rsrc((void*)(wcopy + (q0 & 1) * 1179648 + (q0 >> 1) * 2048), 0, 2048, 0x00020000);
                __builtin_amdgcn_raw_buffer_store_b128(*(const u32x4*)(shm + AUX + wid * 2048 + lane * 16), rc, (lane >> 3) * 256 + (lane & 7) * 16, 0, 16);
                WAIT_L(0);
              }
              if (q1 < 1152) {
                const char* src = (const char*)((q1 >> 1) < 448 ? wd + (size_t)(q1 >> 1) * 8 * D_ : wo + (size_t)((q1 >> 1) - 448) * 8 * D_);
                src += ((3 - ((unsigned)(uintptr_t)src >> 7)) & 7) * 128 + (q1 & 1) * 1024;
                GLDS(src + (lane >> 3) * 2048 + (lane & 7) * 16, shm_base + AUX + wid * 2048);
              }
            } else if (kt == CP_K1) {
              if (wid == 0) {
                const u64 now = clock();
                if (lane == 0) __hip_atomic_store(&donec[bid], now, RLX_AGENT);
              }
              if (wid == 2) GLDS_SC1((const char*)(done2 + bid * 128) + lane * 16, shm_base + DMP + 7168);
            }
          }
        } else if (kt == 17 - SD) {
          const u64 now = clock();
          if (lane == 0) __hip_atomic_store(&cstamp[(ntile * 4 + wid) * 8 + mtile], now, RLX_AGENT);
          if (wid >= 2) GLDS_SC1((const char*)donec + (wid - 2) * 1024 + lane * 16, shm_base + DMP + wid * 2048);
          {
            const char* const wp = (const char*)wd + (size_t)(xcd < 4 ? 28 + 5 * (xcd & 1) : 5 - 5 * (xcd & 1)) * 131072 + (idx * 4 + wid) * 1024;
#pragma unroll
            for (int i = 0; i < 4; ++i) glds16_p(wp + (long)((i & 1) + (i >> 1) * 14) * 131072, lane * 16, shm_base + DMP + wid * 2048 + 1024);
          }
          u32x4 gr = *(const u32x4*)(shm + AUX + wid * 1024 + lane * 16);
          bool late = false;
          if (__builtin_expect(!__all(fresh(((u64)(gr[1] ^ gr[3]) << 32) | gr[1], t_own)), 0)) {
            const __amdgpu_buffer_rsrc_t rs = __builtin_amdgcn_make_buffer_rsrc((void*)(rstat + m0), 0, 4096, 0x00020000);
            const int prow = wid * 64 + (lane & 32) + 2 * (lane & 15) + ((lane >> 4) & 1);
            for (;;) {
              gr = __builtin_bit_cast(u32x4, __builtin_amdgcn_raw_buffer_load_b128(rs, prow * 16, 0, 16));
              if (__all(fresh(((u64)(gr[1] ^ gr[3]) << 32) | gr[1], t_own))) break;
              if (__builtin_amdgcn_s_memrealtime() - t_own > TIMEOUT_) { gr[1] = row_stats_serial(x + (size_t)(m0 + prow) * D_); late = true; break; }
              __builtin_amdgcn_s_sleep(2);
            }
          }
          float mean = __uint_as_float(gr[0]) * (1.0f / D_);
          asm volatile("" : "+v"(mean));
          const unsigned ps = pack_stats(mean, fmaxf(__builtin_fmaf(-mean, mean, __uint_as_float(gr[2]) * (1.0f / D_)), 0.f));
          const unsigned g0 = late ? gr[1] : ps;
          const float nm = -lo_f(g0), dn = hi_f(g0), r = __builtin_amdgcn_rcpf(dn);
          float k1 = -2.3022081986f * r, d2 = dn * dn;
          asm volatile("" : "+v"(k1));
          float k2 = -0.10294324f * r;
          asm volatile("" : "+v"(d2), "+v"(k2));
          const f32x4 fc = {nm, k1, k2 * r * r, d2};
          *(f32x4*)(shm + AUX + wid * 1024 + lane * 16) = fc;
        } else if (kt == 18 - SD) {
          const u32x4 c4 = *(const u32x4*)(shm + DMP + wid * 2048 + lane * 16);
          asm volatile("" :: "v"(c4) : "memory");
          cgather();
          const u64 win = tg - t_own;
          auto frs = [&](unsigned lo, unsigned hi) { return (((u64)hi << 32) | lo) - t_own - 100ull < win; };
          if (wid >= 2) *(unsigned*)(shm + CNT - 4 + wid * 4) = __all(frs(c4[0], c4[1]) & frs(c4[2], c4[3])) ? 1u : 0u;
        } else {
          const char* const sc = shm + (lane < 32 ? DMP + wid * 2048 + lane * 8 : QS + wid * 640 + (lane & 7) * 8);
          const int st = lane < 32 ? 256 : 64;
          const f32x4 fc = *(const f32x4*)(shm + AUX + wid * 1024 + lane * 16);
          f32x2 w[8];
          bool late = false;
          for (;;) {
            const u64 sv = *(const u64*)(shm + QS + wid * 640 + 512 + (lane & 15) * 8);
#pragma unroll
            for (int r = 0; r < 8; ++r) w[r] = *(const f32x2*)(sc + r * st);
            if (__builtin_expect(__all(sv - t_own - 100ull < tg - t_own - 200ull), 1)) break;
            late = __builtin_amdgcn_s_memrealtime() - t_own > TIMEOUT_;
            if (late) break;
            __builtin_amdgcn_s_sleep(2);
            WAIT_L(0);
            cgather();
            WAIT_V(0);
          }
          float t0 = w[0][0], t1 = w[0][1];
#pragma unroll
          for (int r = 1; r < 8; ++r)
            asm("v_add_f32 %0, %0, %2\n\tv_add_f32 %1, %1, %3" : "+v"(t0), "+v"(t1) : "v"(w[r][0]), "v"(w[r][1]));
          if (__builtin_expect(late, 0)) {
            const int gq = (lane >> 3) & 3;
            const char* pc = (lane < 32 ? pbase + ((psh & gq) ? dgu : 0) + (gq >> psh) * 32 : gb + dgu + wid * 32) + (lane & 7) * 4;
            t0 = 0.f; t1 = 0.f;
#pragma unroll 1
            for (int k = 0; k < D_; ++k) { const unsigned v = *(const unsigned*)(pc + (size_t)k * (lane < 32 ? pstr : F_ * 2)); t0 += lo_f(v); t1 += hi_f(v); }
          }
          if (lane < 40)
            *(f32x2*)((float*)(shm + CF) + (lane < 32 ? (lane >> 4) * 160 + wid * 32 + (lane & 15) * 2 : (wid >> 1) * 160 + 128 + (wid & 1) * 16 + (lane & 7) * 2)) = f32x2{t0, t1};
          float* const tw = (float*)(shm + DMP + wid * 2048) + lane;
#pragma unroll
          for (int i = 0; i < 4; ++i) tw[i * 64] = fc[i];
          ((float*)(shm + QS + wid * 640))[lane] = fc[1] * (-1.0f / 2.3022081986f);
        }
      }
      SB0();
    }
#pragma unroll
    for (int u = 0; u < 8; ++u) mm(u >> 2, 8 + (u >> 2), 1, u & 3);
  TS1(1);
  __builtin_amdgcn_sched_barrier(0);
  int tz = threadIdx.x;
  asm volatile("s_nop 7" : "+v"(tz));
  __builtin_amdgcn_sched_barrier(0);
  const int li = tz & 15, lq = (tz >> 4) & 3, ln = tz & 63;
  const __amdgpu_buffer_rsrc_t ra = __builtin_amdgcn_make_buffer_rsrc((void*)abuf, 0, 16 * 72 * 16384, 0x00020000);
  float* const svr = (float*)(shm + QS + wm * 640);
  float* const tb = (float*)(shm + DMP + wm * 2048);
  typedef __attribute__((address_space(3))) unsigned lds_u32;
  lds_u32* const cnt = (lds_u32*)(shm3 + CNT);
  unsigned vcnt = shm_base + CNT;
  asm volatile("" : "+v"(vcnt));
  auto signal = [&](unsigned v) {
    unsigned t;
    asm volatile("v_mov_b32 %0, %2\n\ts_mov_b64 exec, 1\n\tds_add_u32 %1, %0\n\ts_mov_b64 exec, -1" : "=&v"(t) : "v"(vcnt), "s"(v) : "memory");
  };
  auto await = [&](unsigned mask, unsigned val) {
    unsigned t, u;
    asm volatile("1:\n\tds_read_b32 %0, %2\n\ts_waitcnt lgkmcnt(0)\n\tv_readfirstlane_b32 %1, %0\n\ts_and_b32 %1, %1, %3\n\ts_cmp_eq_u32 %1, %4\n\t"
                 "s_cbranch_scc1 2f\n\ts_sleep 1\n\ts_branch 1b\n\t2:" : "=&v"(t), "=&s"(u) : "v"(vcnt), "s"(mask), "s"(val) : "memory", "scc");
  };
  if (grp) __builtin_amdgcn_s_setprio(1);
  signal(1u << (8 * grp));
  f32x4 rf[4];
#pragma unroll
  for (int mf = 0; mf < 4; ++mf) rf[mf] = *(const f32x4*)(shm + AUX + (wm * 64 + mf * 16 + li) * 16);
  const int sbase = ((mtile * 2 + (wm >> 1)) * 72 * 8 + (wm & 1) * 4) * 2048;
  auto putf = [&](int kg, int mf, u32x4 v) {
    __builtin_amdgcn_raw_buffer_store_b128(v, ra, ln * 16 + sbase + ((kg >> 1) * 8 + mf) * 2048 + (kg & 1) * 1024, 0, 16);
  };
  auto gelp = [&](f32x2 tg, f32x2 tu, f32x2 k12, f32x2 k22, f32x2 d2) {
    const f32x2 arg = tg * __builtin_elementwise_fma(tg * tg, k22, k12);
    const f32x2 dn = __builtin_elementwise_fma(f32x2{__builtin_amdgcn_exp2f(arg[0]), __builtin_amdgcn_exp2f(arg[1])}, d2, d2);
    const f32x2 r = (tg * tu) * f32x2{__builtin_amdgcn_rcpf(dn[0]), __builtin_amdgcn_rcpf(dn[1])};
    return pack2(r[0], r[1]);
  };
  auto gels = [&](const f32x4& g, const f32x4& u, const f32x4& k) {
    float t0, t1, t2, t3, n0, n1, n2, n3;
    asm("v_mul_f32 %0, %8, %8\n\tv_mul_f32 %1, %9, %9\n\tv_mul_f32 %2, %10, %10\n\tv_mul_f32 %3, %11, %11\n\t"
        "v_fma_f32 %0, %0, %17, %16\n\tv_fma_f32 %1, %1, %17, %16\n\tv_fma_f32 %2, %2, %17, %16\n\tv_fma_f32 %3, %3, %17, %16\n\t"
        "v_mul_f32 %0, %8, %0\n\tv_mul_f32 %1, %9, %1\n\tv_mul_f32 %2, %10, %2\n\tv_mul_f32 %3, %11, %3\n\t"
        "v_exp_f32 %0, %0\n\tv_mul_f32 %4, %8, %12\n\tv_exp_f32 %1, %1\n\tv_mul_f32 %5, %9, %13\n\t"
        "v_exp_f32 %2, %2\n\tv_mul_f32 %6, %10, %14\n\tv_exp_f32 %3, %3\n\tv_mul_f32 %7, %11, %15\n\t"
        "v_fma_f32 %0, %0, %18, %18\n\tv_fma_f32 %1, %1, %18, %18\n\tv_rcp_f32 %0, %0\n\tv_fma_f32 %2, %2, %18, %18\n\t"
        "v_rcp_f32 %1, %1\n\tv_fma_f32 %3, %3, %18, %18\n\tv_rcp_f32 %2, %2\n\tv_mul_f32 %0, %4, %0\n\t"
        "v_rcp_f32 %3, %3\n\tv_mul_f32 %1, %5, %1\n\tv_mul_f32 %2, %6, %2\n\tv_mul_f32 %3, %7, %3\n\t"
        "v_cvt_pk_bf16_f32 %0, %0, %1\n\tv_cvt_pk_bf16_f32 %1, %2, %3"
        : "=&v"(t0), "=&v"(t1), "=&v"(t2), "=&v"(t3), "=&v"(n0), "=&v"(n1), "=&v"(n2), "=&v"(n3)
        : "v"(g[0]), "v"(g[1]), "v"(g[2]), "v"(g[3]), "v"(u[0]), "v"(u[1]), "v"(u[2]), "v"(u[3]), "v"(k[1]), "v"(k[2]), "v"(k[3]));
    return u32x2{__float_as_uint(t0), __float_as_uint(t1)};
  };
  auto gel2 = [&](int p, int kg, int mf0 = 0, int mf1 = 4, int nsc = 0, bool opq = false) {
#pragma unroll
    for (int mf = mf0; mf < mf1; ++mf) {
      const f32x2 k12 = {rf[mf][1], rf[mf][1]}, k22 = {rf[mf][2], rf[mf][2]}, d2 = {rf[mf][3], rf[mf][3]};
      u32x4 o;
      if (opq) asm volatile("" : "+v"(acc[mf][p * 2]), "+v"(acc[mf][p * 2 + 1]));
      if (mf < nsc) {
        const u32x2 a = gels(acc[mf][p * 2], acc[mf][p * 2 + 2], rf[mf]), b = gels(acc[mf][p * 2 + 1], acc[mf][p * 2 + 3], rf[mf]);
        o = u32x4{a[0], a[1], b[0], b[1]};
      } else {
#pragma unroll
        for (int i = 0; i < 4; ++i) {
          const int j = i >> 1, e = (i & 1) * 2;
          const f32x4 g = acc[mf][p * 2 + j], u = acc[mf][p * 2 + 2 + j];
          o[i] = gelp(f32x2{g[e], g[e + 1]}, f32x2{u[e], u[e + 1]}, k12, k22, d2);
        }
      }
      putf(kg, mf, o);
      if (mf < nsc) SB0();
    }
  };
  auto client = [&](int l) { return (((l & 7) * 4 + (mtile & 3)) * 8) + (((mtile >> 2) * 2 + ((l >> 4) & 1)) ^ ((l & 8) ? 5 : 0)); };
  auto notify = [&](u64 lanes) {
    int l = threadIdx.x & 63;
    asm volatile("" : "+v"(l));
    const unsigned off = (client(l) * 128 + (l & 32) + ntile) * 8;
    u64 now, vn;
    asm volatile("s_memrealtime %0\n\ts_nop 0\n\ts_waitcnt lgkmcnt(0)\n\tv_mov_b64 %1, %0\n\ts_mov_b64 exec, %4\n\tglobal_store_dwordx2 %2, %1, %3 sc1\n\ts_mov_b64 exec, -1"
                 : "=&s"(now), "=&v"(vn) : "v"(off), "s"(done1), "s"(lanes) : "memory");
  };
  const u64 myh = 0xffffull << ((wm >> 1) * 16);
  const u64 mrole = (xcd & 4) ? 0x00ff00ffull : 0xff00ff00ull;
  const u64 mall = (tya || (xcd & 3) == 3) ? 0xffffffffull : mrole;
  auto arrive = [&](auto pc) {
    constexpr int part = decltype(pc)::value;
    unsigned t;
    asm volatile("v_mov_b32 %0, 1\n\ts_mov_b64 exec, 1\n\tds_add_rtn_u32 %0, %1, %0 offset:%2\n\ts_mov_b64 exec, -1\n\ts_waitcnt lgkmcnt(0)"
                 : "=&v"(t) : "v"(vcnt), "n"(12 + 4 * part) : "memory");
    if (__builtin_amdgcn_readfirstlane(t) == 3u) notify(part ? (myh & mall) << 32 : myh & mrole);
  };
  const int role = 1 - (xcd >> 2);
  const int xb = role ? xcd : xcd ^ 5, par = xb & 1;
  const int mt2 = ((xb >> 1) * 4 + (idx & 3)) * 2 + par, nt2 = idx >> 2;
  auto rsrc = [](u64 b, unsigned n) { return u32x4{(unsigned)b, (unsigned)(b >> 32) & 0xffffu, n, 0x00020000u}; };
  u32x4 rw[2];
  int wsh[2];
  unsigned vb[2][2], cokw = 0;
  auto wsetup = [&](const u16* w, int rows, int crow0, int l) {
    const unsigned vr = (wid >> 1) * 32 + (wid & 1) * 8 + ((l >> 5) & 1) * 16 + ((l >> 3) & 3), vc = (((l >> 1) & 3) ^ ((l >> 4) & 1)) * 32 + (l & 1) * 16;
    u64 bs = (u64)(uintptr_t)(wcopy + (nt2 >> 2) * 1179648 + crow0 * 256 - 1024), bf = (u64)(uintptr_t)(w + nt2 * 128) - 1024;
    asm volatile("" : "+s"(bs), "+s"(bf));
#pragma unroll
    for (int i = 0; i < 2; ++i) {
      const bool slow = ((((unsigned)bf >> 7) + i) & 7) == (cokw ? 3u : 8u);
      const int sh = slow ? 8 : 11;
      rw[i] = rsrc(slow ? bs : bf + i * 128, ((rows - 1) << sh) + 128 + 1024);
      wsh[i] = 6 + sh;
      vb[i][0] = (vr << sh) + vc + 1024;
      vb[i][1] = ((vr + 4) << sh) + (vc ^ 64);
    }
  };
#define G2_W "s_add_u32 m0, %[l], %[ow]\n\ts_lshl_b32 %[t], %[k], %[s0]\n\tbuffer_load_dwordx4 %[v0], %[w0], %[t] offen sc1 lds\n\t" \
             "buffer_load_dwordx4 %[v1], %[w0], %[t] offen offset:1024 sc1 lds\n\t" \
             "s_add_u32 m0, m0, 8192\n\ts_lshl_b32 %[t], %[k], %[s1]\n\tbuffer_load_dwordx4 %[v2], %[w1], %[t] offen sc1 lds\n\t" \
             "buffer_load_dwordx4 %[v3], %[w1], %[t] offen offset:1024 sc1 lds"
#define G2_WOP [ow] "s"(16384 + wid * 2048), [k] "s"(kt), [s0] "s"(wsh[0]), [s1] "s"(wsh[1]), [w0] "s"(rw[0]), [w1] "s"(rw[1]), \
               [v0] "v"(vb[0][0]), [v1] "v"(vb[0][1]), [v2] "v"(vb[1][0]), [v3] "v"(vb[1][1])
#define G2_A "s_add_u32 m0, %[l], %[oa]\n\ts_lshl_b32 %[t], %[k], 14\n\tbuffer_load_dwordx4 %[va], %[a], %[t] offen sc1 lds\n\t" \
             "buffer_load_dwordx4 %[va], %[a], %[t] offen offset:1024 sc1 lds\n\t"
#define G2_A2 "buffer_load_dwordx4 %[va], %[a], %[t] offen offset:2048 sc1 lds\n\tbuffer_load_dwordx4 %[va], %[a], %[t] offen offset:3072 sc1 lds\n\t"
  constexpr int NY = 28;
  const int ny = (xcd & 1) ? NY - DLT : NY + DLT;
  const u32x4 ra2 = rsrc((u64)(uintptr_t)abuf + mt2 * (72 * 16384), 72 * 16384);
  unsigned va, tab, nlo, nhi, sl = shm_base + 4 * G2_STAGE;
  const int kfirst = 28 * role + 5 * par;
  int q = 3, kt = kfirst + 15;
  u64 okb = 0;
  auto adv = [&]() { ++q; kt = __builtin_amdgcn_readlane(tab, q); sl = sl == shm_base + (G2_NSTAGE - 1) * G2_STAGE ? shm_base : sl + G2_STAGE; };
  auto stage_y = [&]() {
    unsigned t;
    asm volatile(G2_A G2_A2 G2_W : [t] "=&s"(t) : [l] "s"(sl), [oa] "s"((wid ^ role) * 4096), [a] "s"(ra2), [va] "v"(va), G2_WOP : "memory", "scc", "m0");
    adv();
  };
  auto digest = [&](u64 v, u64 valid = ~0ull) {
    const u64 r = __ballot(fresh(v, t_own)) & valid;
    const unsigned hi = (unsigned)(r >> 32), lo = (unsigned)r | hi;
    okb |= __ballot(((nlo & ~lo) | (nhi & ~hi)) == 0u);
  };
  auto aload = [&](const u64* base, unsigned off) {
    asm volatile("s_nop 4\n\tglobal_load_dwordx2 v[254:255], %0, %1 sc1" :: "v"(off), "s"(base) : "memory", "v254", "v255");
    SB0();
  };
  auto aget = [&]() { u64 v; asm volatile("v_mov_b64 %0, v[254:255]" : "=v"(v) :: "memory"); return v; };
  auto snapshot = [&]() { aload(done1 + bid * 128, ln * 8); };
  auto peek = [&]() { digest(aget()); };
  auto snapshot1 = [&]() { aload(done1 + bid * 128 + role * 16, (ln & 15) * 8); };
  auto peek1 = [&]() { digest(aget(), 0xffffull << (16 * role)); };
  auto wpair = [&](int g, int i) {
    unsigned t;
    asm volatile("s_mov_b32 m0, %1\n\ts_lshl_b32 %0, %2, %3\n\tbuffer_load_dwordx4 %5, %4, %0 offen sc1 lds\n\tbuffer_load_dwordx4 %6, %4, %0 offen offset:1024 sc1 lds"
                 : "=&s"(t) : "s"(shm_base + (g + 1) * G2_STAGE + 16384 + i * 8192 + wid * 2048), "s"(kfirst + (g == 2 ? 14 : g)), "s"(wsh[i]), "s"(rw[i]),
                   "v"(vb[i][0]), "v"(vb[i][1]) : "memory", "scc", "m0");
    SB0();
  };
  auto aquad = [&](int g) {
    unsigned t;
    asm volatile(G2_A G2_A2 : [t] "=&s"(t) : [l] "s"(shm_base + (g + 1) * G2_STAGE), [oa] "s"((wid ^ role) * 4096), [a] "s"(ra2), [va] "v"(va),
                 [k] "s"(((okb >> g) & 1) ? kfirst + (g == 2 ? 14 : g) : 100) : "memory", "scc", "m0");
    SB0();
  };
  auto mid = [&]() {
    cokw = __builtin_amdgcn_readfirstlane(cnt[1] & cnt[2]);
    wsetup(wd, F_, 0, ln);
    va = ln * 16 + wid * 4096;
    {
      const int ext = ln >= NY ? 1 : 0, qq = ln - ext * DLT, so = ln - ny;
      int j0 = 5 * par, j1 = 5 - 5 * par;
      asm volatile("" : "+s"(j1));
      const int j = (qq < 8 ? j0 : (qq < 16 ? j1 : 2)) + ((unsigned)(0x92492444444444ull >> (2 * qq)) & 3);
      const int xs = ((role ^ ext) << 2) | ((unsigned)(0xfd5a80f5a0f5a0ull >> (2 * qq)) & 3);
      unsigned bits = ((0x41a8282u >> (4 * j)) & 15u) << (4 * xs), ob = (so < 16 ? 1u : 0u) << (2 * so), ty = 7 * xs + j;
      asm volatile("" : "+v"(bits), "+v"(ob), "+v"(ty));
      tab = so < 0 ? ty : so;
      nlo = ln < 8 ? bits : 0u;
      nhi = so < 0 ? bits - nlo : ob;
    }
    asm volatile("" :: "s"(rw[0]), "s"(rw[1]), "s"(wsh[0]), "s"(wsh[1]), "s"(ra2));
    asm volatile("" : "+v"(vb[0][0]), "+v"(vb[0][1]), "+v"(vb[1][0]), "+v"(vb[1][1]), "+v"(va), "+v"(tab), "+v"(nlo), "+v"(nhi));
    await(0xffffu, 0x404u);
    wpair(0, 0);
    wpair(0, 1);
    WAIT_V(4);
    arrive(std::integral_constant<int, 0>{});
    __builtin_amdgcn_s_setprio(0);
  };
  int tsel = ntile & 1;
  asm volatile("" : "+s"(tsel));
  if (tsel != 0) {
    if (grp == 0) gel2(0, 2 * (kt0 + he) + wc, 0, NSC, NSC);
    else gel2(0, 2 * (kt0 + he) + wc, 0, NSC);
    gel2(0, 2 * (kt0 + he) + wc, NSC, 4);
    if (grp == 0) mid();
#pragma unroll
    for (int mf = 0; mf < 4; ++mf) {
      f32x4 t[4];
#pragma unroll
      for (int i = 1; i < 4; ++i) t[i] = *(const f32x4*)(tb + i * 64 + mf * 16 + lq * 4);
      const f32x4 g = acc[mf][4], u = acc[mf][5];
      u32x2 o;
#pragma unroll
      for (int e = 0; e < 4; e += 2)
        o[e >> 1] = gelp(f32x2{g[e], g[e + 1]}, f32x2{u[e], u[e + 1]}, f32x2{t[1][e], t[1][e + 1]}, f32x2{t[2][e], t[2][e + 1]}, f32x2{t[3][e], t[3][e + 1]});
      *(u32x2*)(shm + wm * 4096 + mf * 1024 + wc * 512 + li * 32 + lq * 8) = o;
      if (mf == 1 || grp == 0) SB0();
      if (grp == 0) {
        wpair(1 + (mf >> 1), mf & 1);
        if (mf == 3) snapshot1();
      }
    }
    signal(0x10000u << vid);
    if (grp) {
      gel2(3, 2 * (kt0 + 2 + he) + wc, 0, 4);
    } else {
#pragma unroll
      for (int mf = 0; mf < 4; ++mf) {
        gel2(3, 2 * (kt0 + 2 + he) + wc, mf, mf + 1);
        SB0();
        if (mf == 0) { WAIT_V(1); peek1(); }
        if (mf < 3) aquad(mf);
        if (mf == 2) snapshot1();
      }
    }
    await(0x110000u << wm, 0x110000u << wm);
#pragma unroll
    for (int i = 0; i < 2; ++i) putf(2 * (kt0 + 4) + he, 2 * wc + i, __builtin_bit_cast(u32x4, tr_frag(shm3 + wm * 4096 + wc * 2048 + i * 1024 + ln * 8)));
    if (grp == 0) WAIT_V(9);
  } else {
    if (grp) __builtin_amdgcn_s_setprio(3);
    float rd[4];
#pragma unroll
    for (int mf = 0; mf < 4; ++mf) rd[mf] = svr[mf * 16 + li];
    auto cvs = [&](const f32x4& a, const f32x4& b, const f32x4& r) {
      float t0, t1, t2, t3, t4, t5, t6, t7;
      asm("v_mul_f32 %0, %8, %16\n\tv_mul_f32 %4, %9, %17\n\tv_mul_f32 %1, %10, %18\n\tv_mul_f32 %5, %11, %19\n\t"
          "v_mul_f32 %2, %12, %16\n\tv_mul_f32 %6, %13, %17\n\tv_mul_f32 %3, %14, %18\n\tv_mul_f32 %7, %15, %19\n\t"
          "v_cvt_pk_bf16_f32 %0, %0, %4\n\tv_cvt_pk_bf16_f32 %1, %1, %5\n\tv_cvt_pk_bf16_f32 %2, %2, %6\n\tv_cvt_pk_bf16_f32 %3, %3, %7"
          : "=&v"(t0), "=&v"(t1), "=&v"(t2), "=&v"(t3), "=&v"(t4), "=&v"(t5), "=&v"(t6), "=&v"(t7)
          : "v"(a[0]), "v"(a[1]), "v"(a[2]), "v"(a[3]), "v"(b[0]), "v"(b[1]), "v"(b[2]), "v"(b[3]), "v"(r[0]), "v"(r[1]), "v"(r[2]), "v"(r[3]));
      return u32x4{__float_as_uint(t0), __float_as_uint(t1), __float_as_uint(t2), __float_as_uint(t3)};
    };
    auto cvp = [&](const f32x4& a, const f32x4& b) { return u32x4{pack2(a[0], a[1]), pack2(a[2], a[3]), pack2(b[0], b[1]), pack2(b[2], b[3])}; };
    auto cvk = [&](int mf, bool sc) {
      const f32x4 r4 = {rd[mf], rd[mf], rd[mf], rd[mf]};
      return sc ? cvs(acc[mf][2], acc[mf][3], r4) : cvp(acc[mf][2] * r4, acc[mf][3] * r4);
    };
    auto cvv = [&](int mf, bool sc) {
      const f32x4 r4 = *(const f32x4*)(svr + mf * 16 + lq * 4);
      return sc ? cvs(acc[mf][4], acc[mf][5], r4) : cvp(acc[mf][4] * r4, acc[mf][5] * r4);
    };
    u32x4 kx[4], vx[4], qx[4], hx[4];
    const bf16x8 ones = {0x3f80, 0x3f80, 0x3f80, 0x3f80, 0x3f80, 0x3f80, 0x3f80, 0x3f80};
    kx[2] = cvk(2, true); kx[3] = cvk(3, true);
    vx[2] = cvv(2, true); vx[3] = cvv(3, true);
    const bool giveh = !grp && wm == 3 && mtile < 7, needh = grp && wm == 0 && mtile > 0;
    char* const hptr = (char*)halo + (size_t)((needh ? mtile - 1 : mtile) * 32 + ntile + wc) * 16384;
    if (grp || wm < 3) {
      char* const ex = shm + (vid - wc) * 4096 + ln * 16;
      *(u32x4*)ex = kx[2]; *(u32x4*)(ex + 1024) = kx[3]; *(u32x4*)(ex + 2048) = vx[2]; *(u32x4*)(ex + 3072) = vx[3];
      signal(0x10000u << vid);
    } else if (giveh) {
      const __amdgpu_buffer_rsrc_t rh = __builtin_amdgcn_make_buffer_rsrc((void*)hptr, 0, 9216, 0x00020000);
      const u64 now = __builtin_amdgcn_s_memrealtime();
      const unsigned tag = (unsigned)now;
#pragma unroll
      for (int r = 0; r < 8; ++r) {
        const u32x4 w = r < 4 ? kx[2 + (r >> 1)] : vx[r >> 1];
        __builtin_amdgcn_raw_buffer_store_b128(u32x4{w[2 * (r & 1)], tag, w[2 * (r & 1) + 1], tag}, rh, ln * 16 + r * 1024, 0, 16);
      }
      __builtin_amdgcn_raw_buffer_store_b128(u32x4{tag, (unsigned)(now >> 32), tag, (unsigned)(now >> 32)}, rh, ln * 16 + 8192, 0, 16);
    }
    if (grp == 0) { gel2(3, 2 * (kt0 + 5 + he) + wc, 0, 4, NSC - 1, true); mid(); }
    kx[0] = cvk(0, false); kx[1] = cvk(1, false);
    vx[0] = cvv(0, false); vx[1] = cvv(1, false);
#pragma unroll
    for (int mf = 0; mf < 4; ++mf) qx[mf] = cvp(acc[mf][0], acc[mf][1]);
    f32x4 mk0, mk1;
#pragma unroll
    for (int j = 0; j < 4; ++j) {
      const int k0 = 4 * lq + j, k1 = 16 + 4 * lq + j;
      mk0[j] = (k0 >= li + 9 && k0 <= li + 16) ? 0.f : -1.0e30f;
      mk1[j] = (k1 >= li + 9 && k1 <= li + 16) ? 0.f : -1.0e30f;
    }
    auto max3 = [](float a, float b, float c) { float r; asm("v_max3_f32 %0, %1, %2, %3" : "=v"(r) : "v"(a), "v"(b), "v"(c)); return r; };
    auto max2 = [](float a, float b) { float r; asm("v_max_f32 %0, %1, %2" : "=v"(r) : "v"(a), "v"(b)); return r; };
    auto tiles = [&](int t0, const u32x4* kp, const u32x4* vp, bool first_rows) {
      const f32x4 z = {0.f, 0.f, 0.f, 0.f};
      const f32x4 mz = {-1.0e30f, -1.0e30f, -1.0e30f, -1.0e30f};
      f32x4 s0[2], s1[2];
#pragma unroll
      for (int n = 0; n < 2; ++n) {
        const bf16x8 qf = __builtin_bit_cast(bf16x8, qx[t0 + n]);
        s0[n] = __builtin_amdgcn_mfma_f32_16x16x32_bf16(__builtin_bit_cast(bf16x8, kp[n]), qf, first_rows ? mz : mk0, 0, 0, 0);
        s1[n] = __builtin_amdgcn_mfma_f32_16x16x32_bf16(__builtin_bit_cast(bf16x8, kx[t0 + n]), qf, mk1, 0, 0, 0);
      }
      SB0();
      if (grp == 0) { if (t0) wpair(1, 1); else aquad(1); }
      asm volatile("s_nop 7\n\ts_nop 3" : "+v"(s0[0]), "+v"(s1[0]), "+v"(s0[1]), "+v"(s1[1]));
      float mx[2];
#pragma unroll
      for (int n = 0; n < 2; ++n) mx[n] = max3(max3(s0[n][0], s0[n][1], s0[n][2]), max3(s0[n][3], s1[n][0], s1[n][1]), max2(s1[n][2], s1[n][3]));
#pragma unroll
      for (int n = 0; n < 2; ++n) { auto r = __builtin_amdgcn_permlane16_swap(__float_as_uint(mx[n]), __float_as_uint(mx[n]), false, false);
        mx[n] = max2(__uint_as_float(r[0]), __uint_as_float(r[1])); }
#pragma unroll
      for (int n = 0; n < 2; ++n) { auto r = __builtin_amdgcn_permlane32_swap(__float_as_uint(mx[n]), __float_as_uint(mx[n]), false, false);
        mx[n] = max2(__uint_as_float(r[0]), __uint_as_float(r[1])); }
      SB0();
      if (grp == 0) {
        if (t0) wpair(2, 0); else aquad(2);
        snapshot1();
      }
      constexpr float cs2 = 0.17677669529663687f * 1.4426950408889634f;
      f32x4 os[2], o0[2], o1[2];
#pragma unroll
      for (int n = 0; n < 2; ++n) {
        const float c = cs2 * rd[t0 + n], nm = -mx[n] * c;
        float e8[8];
#pragma unroll
        for (int j = 0; j < 4; ++j) {
          e8[j] = __builtin_amdgcn_exp2f(__builtin_fmaf(s0[n][j], c, nm));
          e8[4 + j] = __builtin_amdgcn_exp2f(__builtin_fmaf(s1[n][j], c, nm));
        }
        u32x4 pk;
#pragma unroll
        for (int j = 0; j < 4; ++j) pk[j] = pack2(e8[2 * j], e8[2 * j + 1]);
        const bf16x8 pf = __builtin_bit_cast(bf16x8, pk);
        const u32x4 va = {vp[n][0], vp[n][1], vx[t0 + n][0], vx[t0 + n][1]}, vb = {vp[n][2], vp[n][3], vx[t0 + n][2], vx[t0 + n][3]};
        os[n] = __builtin_amdgcn_mfma_f32_16x16x32_bf16(ones, pf, z, 0, 0, 0);
        o0[n] = __builtin_amdgcn_mfma_f32_16x16x32_bf16(__builtin_bit_cast(bf16x8, va), pf, z, 0, 0, 0);
        o1[n] = __builtin_amdgcn_mfma_f32_16x16x32_bf16(__builtin_bit_cast(bf16x8, vb), pf, z, 0, 0, 0);
      }
      if (grp == 0 && t0) { SB0(); wpair(2, 1); }
#pragma unroll
      for (int n = 0; n < 2; ++n) {
        const float inv = __builtin_amdgcn_rcpf(os[n][0]);
        const f32x4 n0 = o0[n] * inv, n1 = o1[n] * inv;
        putf(112 + ntile + wc, t0 + n, u32x4{pack2(n0[0], n0[1]), pack2(n0[2], n0[3]), pack2(n1[0], n1[1]), pack2(n1[2], n1[3])});
      }
    };
    if (grp == 0) wpair(1, 0);
    tiles(2, &kx[0], &vx[0], false);
    constexpr int HL = 131072, HS = DMP + 1024;
    auto hload = [&]() {
      asm volatile("s_mov_b32 m0, %2\n\ts_nop 0\n\tglobal_load_lds_dwordx4 %0, %1 offset:-4096 sc1\n\tglobal_load_lds_dwordx4 %0, %1 offset:-3072 sc1\n\t"
                   "global_load_lds_dwordx4 %0, %1 offset:-2048 sc1\n\tglobal_load_lds_dwordx4 %0, %1 offset:-1024 sc1\n\tglobal_load_lds_dwordx4 %0, %1 sc1\n\t"
                   "global_load_lds_dwordx4 %0, %1 offset:1024 sc1\n\tglobal_load_lds_dwordx4 %0, %1 offset:2048 sc1\n\tglobal_load_lds_dwordx4 %0, %1 offset:3072 sc1\n\t"
                   "s_mov_b32 m0, %4\n\ts_nop 0\n\tglobal_load_lds_dwordx4 %0, %3 sc1"
                   :: "v"(ln * 16), "s"(hptr + 4096), "s"(shm_base + HL + wc * 8192 + 4096), "s"(hptr + 8192), "s"(shm_base + HS + wc * 2048) : "memory", "m0");
    };
    if (needh) { await(0xffffu, 0x404u); hload(); }
    if (grp) { __builtin_amdgcn_s_setprio(1); gel2(3, 2 * (kt0 + 5 + he) + wc, 0, 2, 0, true); __builtin_amdgcn_s_setprio(3); }
    else {
      WAIT_V(4);
      peek1();
      aquad(0);
    }
    if (!grp || wm > 0) {
      await(0x8000u << vid, 0x8000u << vid);
#pragma unroll
      for (int i = 0; i < 4; ++i) hx[i] = *(const u32x4*)(shm + (vid - wc - 1) * 4096 + i * 1024 + ln * 16);
    } else if (!needh) {
#pragma unroll
      for (int i = 0; i < 4; ++i) hx[i] = u32x4{0u, 0u, 0u, 0u};
    } else {
      bool late = false;
      WAIT_V(2);
      for (;;) {
        const char* const hl = shm + HL + wc * 8192 + ln * 16;
        const u32x4 sp = *(const u32x4*)(shm + HS + wc * 2048 + ln * 16);
        unsigned df = 0;
#pragma unroll
        for (int i = 0; i < 4; ++i) {
          const u32x4 g0 = *(const u32x4*)(hl + i * 2048), g1 = *(const u32x4*)(hl + i * 2048 + 1024);
          df |= (g0[1] ^ sp[0]) | (g0[3] ^ sp[0]) | (g1[1] ^ sp[0]) | (g1[3] ^ sp[0]);
          hx[i] = u32x4{g0[0], g0[2], g1[0], g1[2]};
        }
        if (__builtin_expect(__all((df == 0) & fresh(((u64)sp[1] << 32) | sp[0], t_own)), 1)) break;
        late = __builtin_amdgcn_s_memrealtime() - t_own > TIMEOUT_;
        if (late) break;
        __builtin_amdgcn_s_sleep(1);
        WAIT_L(0);
        hload();
        WAIT_V(0);
      }
      if (__builtin_expect(late, 0)) {
        char* const fb = shm + HL + wc * 8192 + ln * 16;
#pragma unroll 1
        for (int i = 0; i < 32; ++i) {
          const int rec = i >> 3, e = i & 7;
          const int row = m0 - 32 + 2 * (rec < 2 ? li : lq * 4 + (e & 3)) + (rec & 1);
          const int col = (rec < 2 ? D_ + lq * 4 + (e & 3) : 2 * D_ + li) + (ntile + wc) * 32 + (e >> 2) * 16;
          const u16* xr = x + (size_t)row * D_;
          const unsigned stt = row_stats_serial(xr);
          const float mb = lo_f(stt);
          float a = 0.f;
#pragma unroll 1
          for (int k = 0; k < D_; ++k) a += (bf2f(xr[k]) - mb) * bf2f(wqkv[(size_t)k * 3072 + col]);
          *(u16*)(fb + rec * 1024 + e * 2) = f2bf(a / hi_f(stt));
        }
#pragma unroll
        for (int i = 0; i < 4; ++i) hx[i] = *(const u32x4*)(fb + i * 1024);
      }
    }
    tiles(0, &hx[0], &hx[2], grp && mtile == 0 && wm == 0);
    if (grp) { __builtin_amdgcn_s_setprio(1); gel2(3, 2 * (kt0 + 5 + he) + wc, 2, 4, 0, true); }
    else WAIT_V(7);
  }
  TS1(2);
  const bool cok = cokw != 0u;
  {
  __builtin_amdgcn_sched_barrier(0);
  int tid = threadIdx.x & 255;
  asm volatile("" : "+v"(tid));
  __builtin_amdgcn_sched_barrier(0);
  const int lane = tid & 63;
  const int wm = (wid >> 1) & 1, wn = wid & 1;
  const int mtile = mt2, ntile = nt2;
  const int m0 = mtile * 128, n0 = ntile * 128;
  TS(0);
  constexpr int EP = 528;
  const int img0 = ((ny + 13) % G2_NSTAGE) * G2_STAGE, img1 = ((ny + 14) % G2_NSTAGE) * G2_STAGE;
  if constexpr (!LD) {
    const int p = wid;
    const u64* const mbox = &done1[bid * 128 + lane];
    auto stage_s = [&]() {
      unsigned t;
      asm volatile(G2_A G2_W : [t] "=&s"(t) : [l] "s"(sl), [oa] "s"((p >> 1) * 8192 + (p & 1) * 2048), [a] "s"(ra2), [va] "v"(va), G2_WOP : "memory", "scc", "m0");
      adv();
    };
    u64 ren = 0;
    auto need = [&]() {
      while (__builtin_expect(!((okb >> q) & 1), 0)) {
        digest(__hip_atomic_load(mbox, RLX_AGENT));
        if (!((okb >> q) & 1)) {
          __builtin_amdgcn_s_sleep(2);
          if (__builtin_expect(!cok, 0)) notify(ren);
          if (GIVEUP()) break;
        }
      }
      asm volatile("" ::: "memory");
    };
    __builtin_amdgcn_s_setprio(0);
    if ((okb & 7) != 7) {
      const unsigned done = (unsigned)okb;
#pragma unroll 1
      for (q = 0; q < 3; ++q)
        if (!((done >> q) & 1)) {
          need();
          aquad(q);
        }
      if (done & 4) WAIT_V(0);
      WAIT_V(4);
    }
    TS(4);
    BARL();
    need();
    stage_y();
    WAIT_V(8);
    peek1();
    if (p == 0) snapshot();
    need();
    stage_y();
    if (p) okb = ~0ull;
    auto check = [&]() {
      if (__builtin_expect(okb != ~0ull && !(q & 1), 0)) {
        peek();
        if (okb != ~0ull) snapshot();
      }
    };
#define G2_RUN(N, A23) "s_branch 2f\n1:\n\tbuffer_load_dwordx4 %[v3], %[w1], %[t2] offen offset:1024 sc1 lds\n2:\n\t" \
      "s_add_u32 m0, %[l], %[oa]\n\ts_lshl_b32 %[t], %[k], 14\n\ts_waitcnt vmcnt(" N ")\n\ts_barrier\n\t" \
      "buffer_load_dwordx4 %[va], %[a], %[t] offen sc1 lds\n\ts_add_u32 %[q], %[q], 1\n\ts_lshl_b32 %[t2], %[k], %[s1]\n\t" \
      "buffer_load_dwordx4 %[va], %[a], %[t] offen offset:1024 sc1 lds\n\t" A23 \
      "s_add_u32 m0, %[l], %[ow]\n\ts_lshl_b32 %[t], %[k], %[s0]\n\t" \
      "buffer_load_dwordx4 %[v0], %[w0], %[t] offen sc1 lds\n\tv_readlane_b32 %[k], %[tab], %[q]\n\ts_add_u32 %[l], %[l], 0x8000\n\t" \
      "buffer_load_dwordx4 %[v1], %[w0], %[t] offen offset:1024 sc1 lds\n\ts_add_u32 m0, m0, 8192\n\ts_cmp_eq_u32 %[l], %[le]\n\ts_cselect_b32 %[l], %[lb], %[l]\n\t" \
      "buffer_load_dwordx4 %[v2], %[w1], %[t2] offen sc1 lds\n\ts_cmp_lt_u32 %[q], %[qe]\n\ts_cbranch_scc1 1b\n\t" \
      "buffer_load_dwordx4 %[v3], %[w1], %[t2] offen offset:1024 sc1 lds"
#define G2_RUNOP : [t] "=&s"(t), [t2] "=&s"(t2), [q] "+&s"(q), [k] "+&s"(kt), [l] "+&s"(sl) \
      : [oa] "s"(oa), [a] "s"(ra2), [va] "v"(va), [ow] "s"(16384 + wid * 2048), [s0] "s"(wsh[0]), [s1] "s"(wsh[1]), [w0] "s"(rw[0]), [w1] "s"(rw[1]), \
        [v0] "v"(vb[0][0]), [v1] "v"(vb[0][1]), [v2] "v"(vb[1][0]), [v3] "v"(vb[1][1]), [tab] "v"(tab), [qe] "s"(qe), \
        [lb] "s"(shm_base), [le] "s"(shm_base + G2_NSTAGE * G2_STAGE) : "memory", "scc", "m0"
    auto sync = [&](bool y) {
      if (y) WAIT_V(16); else WAIT_V(12);
      need();
      BAR();
      check();
    };
    auto upto = [&](bool y, int qe) {
#pragma unroll 1
      while (__builtin_expect(okb != ~0ull && q < qe, 0)) {
        sync(y);
        if (y) stage_y(); else stage_s();
      }
      if (q < qe) {
        unsigned t, t2;
        const int oa = y ? (p ^ role) * 4096 : (p >> 1) * 8192 + (p & 1) * 2048;
        if (y) asm volatile(G2_RUN("16", G2_A2) G2_RUNOP);
        else asm volatile(G2_RUN("12", "") G2_RUNOP);
      }
    };
    sync(true);
    ren = 0xffffffff00000000ull;
    if (p == 1) notify((myh & mall) << 32);
    stage_y();
    upto(true, 16 + 2 * p);
    sync(true);
    const u32x4 rwd[2] = {rw[0], rw[1]};
    const int wshd[2] = {wsh[0], wsh[1]};
    const unsigned vbd[4] = {vb[0][0], vb[0][1], vb[1][0], vb[1][1]};
    wsetup(wo, D_, F_, lane);
    const u32x4 rwo[2] = {rw[0], rw[1]};
    const int wsho[2] = {wsh[0], wsh[1]};
    unsigned vbo[5] = {vb[0][0], vb[0][1], vb[1][0], vb[1][1], (unsigned)(lane * 16 + (p >> 1) * 8192 + role * 4096 + (p & 1) * 2048 + (56 << 14))};
    asm volatile("" :: "s"(rwo[0]), "s"(rwo[1]), "s"(wsho[0]), "s"(wsho[1]));
    asm volatile("" : "+v"(vbo[0]), "+v"(vbo[1]), "+v"(vbo[2]), "+v"(vbo[3]), "+v"(vbo[4]));
    rw[0] = rwd[0]; rw[1] = rwd[1]; wsh[0] = wshd[0]; wsh[1] = wshd[1];
    vb[0][0] = vbd[0]; vb[0][1] = vbd[1]; vb[1][0] = vbd[2]; vb[1][1] = vbd[3];
    const char* ps = (const char*)slab + (size_t)(bid ^ 5) * 16384 + p * 4096;
    asm volatile("" : "+s"(ps));
    const u16* xp = x + (size_t)(m0 + role * 32) * D_ + n0;
    u16* op = out + (size_t)(m0 + role * 32) * D_ + n0;
    unsigned vo = ((tid >> 4) * 2 * D_ + (tid & 15) * 8) * 2, vi0 = img0 + (tid >> 4) * EP + (tid & 15) * 32, vi1 = vi0 + (img1 - img0);
    unsigned vo2 = vo + 64 * D_ * 2;
    asm volatile("" : "+s"(xp));
    asm volatile("" : "+s"(op));
    asm volatile("" : "+v"(vo), "+v"(vo2), "+v"(vi0), "+v"(vi1));
    stage_y();
    upto(true, ny);
    va = vbo[4];
    rw[0] = rwo[0]; rw[1] = rwo[1]; wsh[0] = wsho[0]; wsh[1] = wsho[1];
    vb[0][0] = vbo[0]; vb[0][1] = vbo[1]; vb[1][0] = vbo[2]; vb[1][1] = vbo[3];
    bool pok = true;
    upto(false, ny + (p == 0 ? 13 : (p == 1 ? 10 : 16)));
    if (p < 2) {
      sync(false);
      if (p == 1) {
        if (lane == 0) __hip_atomic_store(done2 + bid * 128, __builtin_amdgcn_s_memrealtime(), RLX_AGENT);
      } else {
        q += 2;
        need();
        --q;
        need();
        --q;
        aload(done2 + (bid ^ 5) * 128, 0);
      }
      stage_s();
      upto(false, ny + 15);
      WAIT_V(12);
      BAR();
      if (p == 0) pok = __all(fresh(aget(), t_own));
      stage_s();
    }
    WAIT_V(12);
    while (__builtin_expect(!pok, 0)) {
      aload(done2 + (bid ^ 5) * 128, 0);
      WAIT_V(0);
      pok = __all(fresh(aget(), t_own)) || GIVEUP();
      if (__builtin_expect(!cok, 0)) { notify(ren); if (lane == 0) __hip_atomic_store(done2 + bid * 128, __builtin_amdgcn_s_memrealtime(), RLX_AGENT); }
    }
    SB0();
    BAR();
    asm volatile("s_mov_b32 m0, %2\n\ts_nop 0\n\tglobal_load_lds_dwordx4 %0, %1 sc1\n\tglobal_load_lds_dwordx4 %0, %1 offset:1024 sc1\n\t"
                 "global_load_lds_dwordx4 %0, %1 offset:2048 sc1\n\tglobal_load_lds_dwordx4 %0, %1 offset:3072 sc1"
                 :: "v"(lane * 16), "s"(ps), "s"(sl + p * 4096) : "memory", "m0");
    SB0();
    WAIT_V(10);
    BAR();
    const __amdgpu_buffer_rsrc_t rx = __builtin_amdgcn_make_buffer_rsrc((void*)xp, 0, 96 * D_ * 2, 0x00020000);
    const __amdgpu_buffer_rsrc_t ro = __builtin_amdgcn_make_buffer_rsrc((void*)op, 0, 96 * D_ * 2, 0x00020000);
    u32x4 xv[4];
#pragma unroll
    for (int i = 0; i < 4; ++i) xv[i] = __builtin_bit_cast(u32x4, __builtin_amdgcn_raw_buffer_load_b128(rx, ((i & 1) ? vo2 : vo) + (i >> 1) * D_ * 2, 0, 0));
    WAIT_V(8);
    BAR();
    WAIT_V(4);
    BAR();
    f32x4 xf[8];
#pragma unroll
    for (int i = 0; i < 8; ++i) {
      const unsigned a = xv[i >> 1][(i & 1) * 2], b = xv[i >> 1][(i & 1) * 2 + 1];
      xf[i] = f32x4{lo_f(a), hi_f(a), lo_f(b), hi_f(b)};
      asm volatile("" : "+v"(xf[i]));
    }
#pragma unroll
    for (int h = 0; h < 2; ++h) {
      BAR();
      f32x4 t[4];
#pragma unroll
      for (int i = 0; i < 4; ++i) t[i] = *(const f32x4*)(shm + (h ? vi1 : vi0) + (i >> 1) * 16 * EP + (i & 1) * 16) + xf[h * 4 + i];
#pragma unroll
      for (int i = 0; i < 2; ++i)
        __builtin_amdgcn_raw_buffer_store_b128(u32x4{pack2(t[2 * i][0], t[2 * i][1]), pack2(t[2 * i][2], t[2 * i][3]), pack2(t[2 * i + 1][0], t[2 * i + 1][1]),
                                                     pack2(t[2 * i + 1][2], t[2 * i + 1][3])}, ro, (i ? vo2 : vo) + h * D_ * 2, 0, 2);
    }
    TS(5);
    if (tid == 0) __hip_atomic_store(&acks[bid], __builtin_amdgcn_s_memrealtime(), RLX_AGENT);
    if (__builtin_expect(!cok, 0) && p == 0) {
      const bool mine = lane <= 32;
      const u64* const pa = &acks[lane < 32 ? client(lane) : bid ^ 5];
      while (!__all(!mine || fresh(__hip_atomic_load(pa, RLX_AGENT), t_own))) {
        notify(ren);
        if (lane == 0) __hip_atomic_store(done2 + bid * 128, __builtin_amdgcn_s_memrealtime(), RLX_AGENT);
        __builtin_amdgcn_s_sleep(32);
        if (GIVEUP()) break;
      }
    }
  } else {
    TS(6);
    f32x4 acc[4][4], sacc[2][4];
    const int fr = lane & 15, fq = lane >> 4, q4 = fr >> 2, pp = lane & 3;
    const int ff = ((q4 >> 1) & 1) | ((fq & 1) << 1);
    int aof = G2_STAGE + wm * 8192 + lane * 16;
    int boff[4];
#pragma unroll
    for (int j = 0; j < 4; ++j) boff[j] = G2_STAGE + 16384 + wn * 8192 + (8 * fq + q4) * 128 + ((j ^ ff) * 32) + pp * 8;
#pragma unroll
    for (int b = 0; b < 4; ++b) {
#pragma unroll
      for (int a = 0; a < 4; ++a) acc[a][b] = f32x4{0.f, 0.f, 0.f, 0.f};
      sacc[0][b] = f32x4{0.f, 0.f, 0.f, 0.f}; sacc[1][b] = f32x4{0.f, 0.f, 0.f, 0.f};
    }
#define SB() __builtin_amdgcn_sched_barrier(0)
    struct Fr { bf16x8 a[4]; bf16x4 bl[4], bh[4]; };
    Fr f0[2], f1[2];
    auto rd = [&](Fr& f, lds_cptr sb, int ks, int t, bool sp) {
      constexpr int ord[12] = {0, 1, 8, 2, 3, 4, 5, 6, 7, 9, 10, 11};
      const int o = ord[t];
      if (o >= 8) f.a[o - 8] = *(const __attribute__((address_space(3))) bf16x8*)(sb + aof + (o - 8) * 2048 + ks * 1024);
      else if (o & 1) f.bh[o >> 1] = tr_read(sb + boff[o >> 1] + ks * 4096 + 512);
      else f.bl[o >> 1] = tr_read(sb + boff[o >> 1] + ks * 4096);
    };
    auto bfr = [&](const Fr& c, int j) { return __builtin_shufflevector(c.bl[j], c.bh[j], 0, 1, 2, 3, 4, 5, 6, 7); };
    int so = G2_STAGE;
    auto ystg = [&](const Fr* c, Fr* n, bool last) {
      int d = 0;
      sfor<32>([&](auto uc) __attribute__((always_inline)) {
        constexpr int u = decltype(uc)::value, t = u >> 1, mf = t >> 2, j = (t & 4) ? 3 - (t & 3) : t & 3, ks = (u ^ t) & 1;
        SB();
        asm volatile("v_mfma_f32_16x16x32_bf16 %0, %1, %2, %0" : "+v"(acc[mf][j]) : "v"(bfr(c[ks], j)), "v"(c[ks].a[mf]));
        SB();
        const int h = last ? 10 : 12;
        if (u < 2 * h) rd(n[u / h], shm3, u / h, u % h, true);
        if (u == 24) { d = so == (G2_NSTAGE - 1) * G2_STAGE ? -(G2_NSTAGE - 1) * G2_STAGE : G2_STAGE; asm volatile("" : "+s"(d)); }
        if (u == 25) { so += d; aof += d; asm volatile("" : "+v"(aof)); asm volatile("" : "+s"(so)); }
        if constexpr (u >= 26 && u < 30) { boff[u - 26] += d; asm volatile("" : "+v"(boff[u - 26])); }
      });
      SB();
      __builtin_amdgcn_s_waitcnt(0xC07F);
    };
    auto stg = [&](const Fr* c, Fr* n) {
      sfor<16>([&](auto uc) __attribute__((always_inline)) {
        constexpr int u = decltype(uc)::value, t = u >> 1, mf = t >> 2, j = (t & 4) ? 3 - (t & 3) : t & 3, ks = (u ^ t) & 1;
        SB();
        asm volatile("v_mfma_f32_16x16x32_bf16 %0, %1, %2, %0" : "+v"(sacc[mf][j]) : "v"(bfr(c[ks], j)), "v"(c[ks].a[mf]));
        SB();
        if (u < 10) { rd(n[0], shm3, 0, u, true); rd(n[1], shm3, 1, u, true); }
      });
      SB();
      __builtin_amdgcn_s_waitcnt(0xC07F);
    };
    asm volatile("" : "+v"(aof), "+v"(boff[0]), "+v"(boff[1]), "+v"(boff[2]), "+v"(boff[3]));
    asm volatile("" : "+v"(sacc[0][0]), "+v"(sacc[0][1]), "+v"(sacc[0][2]), "+v"(sacc[0][3]), "+v"(sacc[1][0]), "+v"(sacc[1][1]), "+v"(sacc[1][2]), "+v"(sacc[1][3]));
#pragma unroll
    for (int a = 0; a < 4; ++a) asm volatile("" : "+v"(acc[a][0]), "+v"(acc[a][1]), "+v"(acc[a][2]), "+v"(acc[a][3]));
    WAIT_V(0);
    arrive(std::integral_constant<int, 1>{});
    BARL();
    TS(7);
    __builtin_amdgcn_s_waitcnt(0xC07F);
    SB();
#pragma unroll
    for (int t = 0; t < 24; ++t) rd(f0[t & 1], shm3, t & 1, t >> 1, false);
    auto nxt = [&]() {
      const int d = so == (G2_NSTAGE - 1) * G2_STAGE ? -(G2_NSTAGE - 1) * G2_STAGE : G2_STAGE;
      so += d; aof += d; boff[0] += d; boff[1] += d; boff[2] += d; boff[3] += d;
      asm volatile("" : "+v"(aof), "+v"(boff[0]), "+v"(boff[1]), "+v"(boff[2]), "+v"(boff[3]));
    };
    nxt();
    __builtin_amdgcn_s_waitcnt(0xC07F);
#pragma unroll 1
    for (int g = 0; g < ny - 2; g += 2) {
      ystg(f0, f1, false);
      BAR();
      ystg(f1, f0, false);
      BAR();
    }
    if (!(DLT & 1)) { ystg(f0, f1, false); BAR(); }
    Fr sa[2], sb2[2];
    ystg((DLT & 1) ? f0 : f1, sa, true);
    asm volatile("" : "+v"(acc[0][0]), "+v"(acc[0][1]), "+v"(acc[0][2]), "+v"(acc[0][3]), "+v"(acc[1][0]), "+v"(acc[1][1]), "+v"(acc[1][2]), "+v"(acc[1][3]));
    TS(2);
    {
      const __amdgpu_buffer_rsrc_t rs = __builtin_amdgcn_make_buffer_rsrc((void*)((char*)slab + (size_t)bid * 16384), 0, 16384, 0x00020000);
#pragma unroll
      for (int f = 0; f < 4; ++f) {
        if (f) nxt();
        BAR();
        if (f & 1) stg(sb2, sa); else stg(sa, sb2);
        asm volatile("" : "+v"(acc[2 + (f >> 1)][(f & 1) * 2]), "+v"(acc[2 + (f >> 1)][(f & 1) * 2 + 1]));
        const f32x4 a = acc[2 + (f >> 1)][(f & 1) * 2], b = acc[2 + (f >> 1)][(f & 1) * 2 + 1];
        __builtin_amdgcn_raw_buffer_store_b128(u32x4{pack2(a[0], a[1]), pack2(a[2], a[3]), pack2(b[0], b[1]), pack2(b[2], b[3])}, rs,
                                               (f * 256 + tid) * 16, 0, 16);
        SB();
      }
    }
#pragma unroll 1
    for (int i = 2; i < 7; ++i) {
      nxt();
      BAR();
      stg(sa, sb2);
      nxt();
      BAR();
      stg(sb2, sa);
      WAIT_V(0);
    }
    nxt();
    BAR();
    stg(sa, sb2);
    int vi0 = img0 + (wm * 16 + fr) * EP + wn * 256 + fq * 16, vi1 = vi0 + (img1 - img0);
    asm volatile("" : "+v"(vi0), "+v"(vi1));
    BAR();
    u32x4 pv[4];
    {
      nxt();
      lds_cptr sb = shm3 + so + tid * 16;
#pragma unroll
      for (int f = 0; f < 4; ++f) pv[f] = *(const __attribute__((address_space(3))) u32x4*)(sb + f * 4096);
    }
    f32x4 pf[8];
    sfor<16>([&](auto uc) __attribute__((always_inline)) {
      constexpr int u = decltype(uc)::value, t = u >> 1, mf = t >> 2, j = (t & 4) ? 3 - (t & 3) : t & 3, ks = (u ^ t) & 1;
      SB();
      asm volatile("v_mfma_f32_16x16x32_bf16 %0, %1, %2, %0" : "+v"(sacc[mf][j]) : "v"(bfr(sb2[ks], j)), "v"(sb2[ks].a[mf]));
      SB();
      if constexpr (u >= 8) {
        const unsigned w0 = pv[(u - 8) >> 1][(u & 1) * 2], w1 = pv[(u - 8) >> 1][(u & 1) * 2 + 1];
        pf[u - 8] = f32x4{lo_f(w0), hi_f(w0), lo_f(w1), hi_f(w1)};
        asm volatile("" : "+v"(pf[u - 8]));
      }
    });
    SB();
    TS(1);
#pragma unroll
    for (int mf = 0; mf < 2; ++mf) {
      f32x4 ya[4], e[4];
#pragma unroll
      for (int j = 0; j < 4; ++j) { ya[j] = acc[mf][j] + pf[mf * 4 + j]; e[j] = ya[j] * -1.4426950408889634f; }
      SB();
#pragma unroll
      for (int j = 0; j < 4; ++j) {
        e[j] = f32x4{__builtin_amdgcn_exp2f(e[j][0]), __builtin_amdgcn_exp2f(e[j][1]), __builtin_amdgcn_exp2f(e[j][2]), __builtin_amdgcn_exp2f(e[j][3])};
        ya[j] *= sacc[mf][j];
      }
      SB();
#pragma unroll
      for (int j = 0; j < 4; ++j) e[j] += 1.0f;
      SB();
#pragma unroll
      for (int j = 0; j < 4; ++j)
        *(f32x4*)(shm + (mf ? vi1 : vi0) + j * 64) =
            ya[j] * f32x4{__builtin_amdgcn_rcpf(e[j][0]), __builtin_amdgcn_rcpf(e[j][1]), __builtin_amdgcn_rcpf(e[j][2]), __builtin_amdgcn_rcpf(e[j][3])};
      SB();
      BARL();
      SB();
    }
    TS(3);
  }
  TSFLUSH();
  }
  };
  if (wid < 4) kloop(std::integral_constant<int, 0>{});
  else kloop(std::integral_constant<int, 1>{});
  asm volatile(".Lcode_end:");
}

extern "C" void kernel_launch(void* const* d_in, const int* in_sizes, int n_in, void* d_out, int out_size, void* d_ws,
                              size_t ws_size, hipStream_t stream) {
  const u16* A_x = (const u16*)d_in[0];
  const u16* A_wqkv = (const u16*)d_in[1];
  const u16* A_wo = (const u16*)d_in[2];
  const u16* B_wg = (const u16*)d_in[4];
  const u16* B_wu = (const u16*)d_in[5];
  const u16* B_wd = (const u16*)d_in[6];
  u16* out = (u16*)d_out;
  fused_kernel<<<256, 512, 0, stream>>>(A_x, A_wqkv, B_wg, B_wu, B_wd, A_wo, out, (char*)d_ws);
}
```

```cpp
#include <hip/hip_runtime.h>
#include <hip/hip_bf16.h>
#include <stdint.h>
#include <type_traits>
#include <utility>

typedef unsigned short u16;
using bf16x8 = __attribute__((ext_vector_type(8))) short;
using bf16x4 = __attribute__((ext_vector_type(4))) short;
using f32x4 = __attribute__((ext_vector_type(4))) float;
using u32x2 = __attribute__((ext_vector_type(2))) unsigned;
using u32x4 = __attribute__((ext_vector_type(4))) unsigned;
typedef __attribute__((address_space(3))) const char* lds_cptr;

template <class F, int... I> __device__ __forceinline__ void sfor_(F&& f, std::integer_sequence<int, I...>) { (f(std::integral_constant<int, I>{}), ...); }
template <int N, class F> __device__ __forceinline__ void sfor(F&& f) { sfor_(f, std::make_integer_sequence<int, N>{}); }

constexpr int D_ = 1024, F_ = 3584;

__device__ __forceinline__ float bf2f(u16 h) { return __uint_as_float((unsigned)h << 16); }
__device__ __forceinline__ u16 f2bf(float f) {
  unsigned u = __float_as_uint(f);
  u += 0x7fffu + ((u >> 16) & 1u);
  return (u16)(u >> 16);
}
__device__ __forceinline__ float rbf(float f) { return bf2f(f2bf(f)); }
typedef __attribute__((ext_vector_type(2))) __bf16 bf16x2_t;
typedef __attribute__((ext_vector_type(2))) float f32x2;
__device__ __forceinline__ unsigned pack2(float a, float b) {
  bf16x2_t r = __builtin_convertvector((f32x2){a, b}, bf16x2_t);
  return __builtin_bit_cast(unsigned, r);
}
__device__ __forceinline__ float lo_f(unsigned p) { return __uint_as_float(p << 16); }
__device__ __forceinline__ float hi_f(unsigned p) { return __uint_as_float(p & 0xffff0000u); }

#define TSDECL
#define TSS(i) do {} while (0)
#define TSFLUSH() do {} while (0)
#define TS1(i) TSS(i)
#define TS(i) TSS(8 + (i))
#define WAIT_V(n) asm volatile("s_waitcnt vmcnt(%0)" ::"n"(n) : "memory")
#define WAIT_L(n) asm volatile("s_waitcnt lgkmcnt(%0)" ::"n"(n) : "memory")
#define BARL() do { WAIT_L(0); BAR(); } while (0)
#define BAR() do { asm volatile("" ::: "memory"); __builtin_amdgcn_s_barrier(); asm volatile("" ::: "memory"); } while (0)

__device__ __forceinline__ bf16x4 tr_read(lds_cptr p) {
  return __builtin_amdgcn_ds_read_tr16_b64_v4i16((__attribute__((address_space(3))) bf16x4*)p);
}
__device__ __forceinline__ bf16x8 tr_frag(lds_cptr p) {
  bf16x4 lo = tr_read(p), hi = tr_read(p + 512);
  bf16x8 r;
  r[0] = lo[0]; r[1] = lo[1]; r[2] = lo[2]; r[3] = lo[3];
  r[4] = hi[0]; r[5] = hi[1]; r[6] = hi[2]; r[7] = hi[3];
  return r;
}
__device__ __forceinline__ void glds16(const void* gsrc, unsigned lds_dst) {
  unsigned keep;
  asm volatile("s_mov_b32 %0, m0\n\ts_mov_b32 m0, %2\n\ts_nop 0\n\tglobal_load_lds_dwordx4 %1, off\n\ts_mov_b32 m0, %0"
               : "=&s"(keep) : "v"(gsrc), "s"(lds_dst) : "memory");
}
#define GLDS(gptr, laddr) glds16((const void*)(gptr), (unsigned)(laddr))
__device__ __forceinline__ void glds16_sc1(const void* gsrc, unsigned lds_dst) {
  unsigned keep;
  asm volatile("s_mov_b32 %0, m0\n\ts_mov_b32 m0, %2\n\ts_nop 0\n\tglobal_load_lds_dwordx4 %1, off sc1\n\ts_mov_b32 m0, %0"
               : "=&s"(keep) : "v"(gsrc), "s"(lds_dst) : "memory");
}
#define GLDS_SC1(gptr, laddr) glds16_sc1((const void*)(gptr), (unsigned)(laddr))
__device__ __forceinline__ void glds16_nc(const void* gsrc, unsigned lds_dst) {
  unsigned keep;
  asm volatile("s_mov_b32 %0, m0\n\ts_mov_b32 m0, %2\n\ts_nop 0\n\tglobal_load_lds_dwordx4 %1, off\n\ts_mov_b32 m0, %0"
               : "=&s"(keep) : "v"(gsrc), "s"(lds_dst));
}
#define GLDS_NC(gptr, laddr) glds16_nc((const void*)(gptr), (unsigned)(laddr))
__device__ __forceinline__ void glds16_s(const void* sbase, unsigned voff, unsigned lds_dst) {
  asm volatile("s_mov_b32 m0, %2\n\ts_nop 0\n\tglobal_load_lds_dwordx4 %0, %1" :: "v"(voff), "s"(sbase), "s"(lds_dst) : "m0");
}

__device__ __forceinline__ void glds16_p(const void* sbase, unsigned voff, unsigned lds_dst) {
  asm volatile("s_mov_b32 m0, %2\n\ts_nop 3\n\tglobal_load_lds_dwordx4 %0, %1" :: "v"(voff), "s"(sbase), "s"(lds_dst) : "m0");
}

typedef unsigned long long u64;
__device__ __forceinline__ bool fresh(u64 stamp, u64 t_own) {
  return stamp - t_own - 100ull < __builtin_amdgcn_s_memrealtime() - t_own;
}
constexpr u64 TIMEOUT_ = 200000;
#define GIVEUP() (__builtin_amdgcn_s_memrealtime() - t_own > 100000000ull)
__device__ __forceinline__ float half_sum(float v) {
  v += __builtin_bit_cast(float, __builtin_amdgcn_update_dpp(0, __builtin_bit_cast(int, v), 0xB1, 0xf, 0xf, false));
  v += __builtin_bit_cast(float, __builtin_amdgcn_update_dpp(0, __builtin_bit_cast(int, v), 0x4E, 0xf, 0xf, false));
  v += __builtin_bit_cast(float, __builtin_amdgcn_update_dpp(0, __builtin_bit_cast(int, v), 0x141, 0xf, 0xf, false));
  v += __builtin_bit_cast(float, __builtin_amdgcn_update_dpp(0, __builtin_bit_cast(int, v), 0x140, 0xf, 0xf, false));
  { auto r = __builtin_amdgcn_permlane16_swap(__float_as_uint(v), __float_as_uint(v), false, false); v = __uint_as_float(r[0]) + __uint_as_float(r[1]); }
  return v;
}
__device__ __forceinline__ unsigned pack_stats(float mean, float var) {
  return (unsigned)f2bf(mean) | ((unsigned)f2bf(sqrtf(rbf(rbf(var) + 1e-5f))) << 16);
}
__device__ __forceinline__ unsigned row_stats_serial(const u16* xr) {
  float s = 0.f, q = 0.f;
#pragma unroll 1
  for (int k = 0; k < D_; ++k) s += bf2f(xr[k]);
  const float mean = s * (1.0f / D_);
#pragma unroll 1
  for (int k = 0; k < D_; ++k) { const float d = bf2f(xr[k]) - mean; q += d * d; }
  return pack_stats(mean, q * (1.0f / D_));
}
constexpr int G1_STAGE = 32768 + 5 * 8192;
constexpr int CP_K0 = 2, CP_K1 = 8;
constexpr int PUB_K = 5;
constexpr int PF_K0 = 6;
constexpr int SD = 6, KS0 = 2;
constexpr int NP1 = 8;
constexpr int NSC = 2;
constexpr int DLT = 1;
static_assert(DLT <= 3, "");

#define RLX_AGENT __ATOMIC_RELAXED, __HIP_MEMORY_SCOPE_AGENT

constexpr int G2_STAGE = 16384 + 2 * 8192;
constexpr int G2_NSTAGE = 5;
constexpr size_t MB_ = 1024 * 1024;
__global__ __launch_bounds__(512, 2) __attribute__((amdgpu_num_vgpr(127))) void fused_kernel(const u16* __restrict__ x, const u16* __restrict__ wqkv,
                                                       const u16* __restrict__ wg, const u16* __restrict__ wu,
                                                       const u16* __restrict__ wd, const u16* __restrict__ wo,
                                                       u16* __restrict__ out, char* __restrict__ ws) {
  u16* const halo = (u16*)(ws + 4 * MB_);
  u16* const abuf = (u16*)(ws + 16 * MB_);
  char* const slab = ws + 64 * MB_;
  unsigned* const flags = (unsigned*)(ws + 96 * MB_);
  u32x4* const rstat = (u32x4*)(ws + 97 * MB_);
  char* const wcopy = ws + 104 * MB_;
  u64* const done1 = (u64*)(ws + 102 * MB_ + 512);
  u64* const done2 = (u64*)(ws + 102 * MB_);
  u64* const donec = (u64*)(ws + 100 * MB_ + 196608);
  u64* const acks = (u64*)(ws + 100 * MB_ + 262144);
  u64* const cstamp = (u64*)(ws + 100 * MB_ + 327680);
  float* const csum = (float*)(ws + 101 * MB_);
  __shared__ __attribute__((aligned(1024))) char shm[163840];
  constexpr int AUX = 2 * G1_STAGE + 8192;
  {
    u64 pc; int len;
    asm volatile("s_getpc_b64 %0\n.Lcode_beg:\n\ts_mov_b32 %1, .Lcode_end-.Lcode_beg" : "=s"(pc), "=s"(len));
    const int ol = ((blockIdx.x >> 3) + 32 * (threadIdx.x >> 6)) * 1024 + (threadIdx.x & 63) * 16;
    asm volatile("s_mov_b32 m0, %2\n\ts_nop 0\n\tglobal_load_lds_dwordx4 %0, %1"
                 :: "v"(ol < len - 16 ? ol : 0), "s"(pc & ~15ull), "s"((unsigned)(uintptr_t)(lds_cptr)shm + 2 * G1_STAGE + 7168) : "m0");
    __builtin_amdgcn_sched_barrier(0);
  }
  const u64 t_own = __builtin_amdgcn_s_memrealtime();
  __builtin_amdgcn_sched_barrier(0);
  TSDECL;
  const int tid = threadIdx.x, lane = tid & 63;
  const int wid = __builtin_amdgcn_readfirstlane(tid >> 6);
  const int bid = blockIdx.x, xcd = bid & 7, idx = bid >> 3;
  const int ntile = xcd * 4 + (idx & 3), mtile = idx >> 2;
  const int m0 = mtile * 256;
  const int hg = ntile >> 1;
  const bool tya = (ntile & 1) == 0;
  const int grp = wid >> 2, wc = (wid >> 1) & 1, wm = (((grp ^ ntile ^ 1) & 1) << 1) | (wid & 1), vid = wc * 4 + wm;
  const int srow = m0 + ntile * 8 + (wid & 3) * 2;
  lds_cptr shm3 = (lds_cptr)shm;
  const unsigned shm_base = (unsigned)(uintptr_t)shm3;
  const int r8 = tid >> 3, pos = tid & 7;
  const int phys = pos >> 1, half = pos & 1;
  const int fk = ((r8 >> 1) & 1) | (((r8 >> 3) & 1) << 1);
  const int vsrc = phys ^ fk;
  const int qstr = tya ? 3072 * 2 : F_ * 2;
  unsigned voa = (((r8 & 32) + 2 * (r8 & 15) + ((r8 >> 4) & 1)) * D_ + ((pos ^ (r8 & 7)) * 8)) * 2;
  unsigned vog = r8 * (F_ * 2) + vsrc * 32 + half * 16, voq = r8 * qstr + vsrc * 32 + half * 16;
  unsigned vom = r8 * qstr + (vsrc >> (tya ? 0 : 1)) * 32 + half * 16;
  asm volatile("" : "+v"(voa), "+v"(vog), "+v"(voq), "+v"(vom));
  asm volatile("" :: "s"(x), "s"(wqkv), "s"(wg), "s"(wu), "s"(wd), "s"(wo), "s"(out), "s"(ws));
  const char* const ab = (const char*)(x + (size_t)m0 * D_);
  int kz = 0;
  asm volatile("" : "+s"(kz));
  auto stage_a = [&](int buf, int kt, int i, int h) {
    glds16_s(ab + (long)(((i * 64 + (h - 1) * 32) * D_ + kt * 64) * 2), voa, shm_base + buf * G1_STAGE + (wid - 4 + 4 * h) * 1024 + i * 8192);
  };
#pragma unroll
  for (int i = 0; i < 4; ++i) stage_a(0, 0, i, 1);
  const int kt0 = (hg >> 1) * 7, he = hg & 1;
  const char* const gb = (const char*)(wg + (kt0 + (tya ? 5 + he : 2 + he)) * 64);
  const char* const qb = tya ? (const char*)(wqkv + 64 * hg) : gb - 256;
  const long dgu = (const char*)wu - (const char*)wg, dp1 = tya ? 2048 : dgu;
  const char* const mb0 = tya ? qb + 4096 : (const char*)(wg + (kt0 + 4) * 64 + 32 * he);
  const char* mptr = mb0 + (((vsrc & 1) && !tya) ? dgu : 0) + vom;
  auto wload = [&](int p, int rows, unsigned dst) {
    long off = (long)(rows * (p < 3 ? qstr : F_ * 2)) + (p == 1 ? dp1 : (p == 4 ? dgu : 0));
    if (p == 2) { asm volatile("" : "+s"(off)); GLDS_NC(mptr + off, dst); }
    else glds16_s((p < 2 ? qb : gb) + off, p < 2 ? voq : vog, dst);
  };
#pragma unroll
  for (int i = 0; i < 5; ++i) wload(i, 0, shm_base + wid * 1024 + 32768 + i * 8192);
  __builtin_amdgcn_sched_barrier(0);
  const int fr = lane & 15, fq = lane >> 4, q4 = fr >> 2, pp = lane & 3;
  const int ff = ((q4 >> 1) & 1) | ((fq & 1) << 1);
  int aoff[2], boff[2];
#pragma unroll
  for (int ks = 0; ks < 2; ++ks) aoff[ks] = (wm * 64 + fr) * 128 + (((ks * 4 + fq) ^ (fr & 7)) * 16);
#pragma unroll
  for (int j = 0; j < 2; ++j) boff[j] = 32768 + (8 * fq + q4) * 128 + (((2 * wc + j) ^ ff) * 32) + pp * 8;

  f32x4 acc[4][10];
#pragma unroll
  for (int a = 0; a < 4; ++a)
#pragma unroll
    for (int b = 0; b < 10; ++b) acc[a][b] = f32x4{0.f, 0.f, 0.f, 0.f};

  auto stage_part = [&](int buf, int kt, int i, int h) {
    if (i < 5) wload(i, kt * 64 + (h - 1) * 32, shm_base + buf * G1_STAGE + (wid - 4 + 4 * h) * 1024 + 32768 + i * 8192);
    else stage_a(buf, kt, i - 5, h);
  };
  constexpr int NT = D_ / 64;
  constexpr int DMP = 2 * G1_STAGE;
  constexpr int CF = AUX + 4096, QS = CF + 1280, CNT = CF + 3840;
  bf16x8 af0[4], af1[4];
  bf16x4 bl[10], bh[10];
#pragma unroll
  for (int i = 0; i < 4; ++i) { af1[i] = bf16x8{0, 0, 0, 0, 0, 0, 0, 0}; bl[i] = bf16x4{0, 0, 0, 0}; bh[i] = bf16x4{0, 0, 0, 0}; }
#define SB0() __builtin_amdgcn_sched_barrier(0)
  auto mm = [&](int n, int f, int ks, int mf) {
    const bf16x8 b = __builtin_shufflevector(bl[n % 10], bh[n % 10], 0, 1, 2, 3, 4, 5, 6, 7);
    if ((f >> 1) == 2) acc[mf][f] = __builtin_amdgcn_mfma_f32_16x16x32_bf16(ks ? af1[mf] : af0[mf], b, acc[mf][f], 0, 0, 0);
    else acc[mf][f] = __builtin_amdgcn_mfma_f32_16x16x32_bf16(b, ks ? af1[mf] : af0[mf], acc[mf][f], 0, 0, 0);
  };
  auto correct = [&]() {
    int l = threadIdx.x & 63;
    asm volatile("s_nop 7" : "+v"(l));
    const float* const tb = (const float*)(shm + DMP + wm * 2048);
    const float* const cf = (const float*)(shm + CF) + wc * 160;
    auto ld = [&](int f) { return f < 8 ? *(const f32x4*)(cf + (f < 4 ? f : f + 2) * 16 + (l >> 4) * 4) : *(const f32x4*)(tb + (f - 8) * 16 + (l >> 4) * 4); };
    float m[4];
    f32x4 cr[3];
#pragma unroll
    for (int mf = 0; mf < 4; ++mf) m[mf] = tb[mf * 16 + (l & 15)];
    cr[0] = ld(0);
    cr[1] = ld(1);
    const float c4 = cf[64 + (l & 15)], c5 = cf[80 + (l & 15)];
#pragma unroll
    for (int f = 0; f < 12; ++f) {
      if (f + 2 < 12) cr[(f + 2) % 3] = ld(f + 2);
      SB0();
      const f32x4 c = cr[f % 3];
#pragma unroll
      for (int i = 0; i < (f < 8 ? 4 : 2); ++i) {
        f32x4& a = f < 8 ? acc[i][f < 4 ? f : f + 2] : acc[f - 8][4 + i];
        float a0 = a[0], a1 = a[1], a2 = a[2], a3 = a[3];
        asm volatile("v_fmac_f32 %0, %4, %5\n\tv_fmac_f32 %1, %4, %6\n\tv_fmac_f32 %2, %4, %7\n\tv_fmac_f32 %3, %4, %8"
                     : "+v"(a0), "+v"(a1), "+v"(a2), "+v"(a3) : "v"(f < 8 ? m[i] : (i ? c5 : c4)), "v"(c[0]), "v"(c[1]), "v"(c[2]), "v"(c[3]));
        a = f32x4{a0, a1, a2, a3};
      }
      SB0();
    }
    asm volatile("s_nop 1");
  };
  auto kloop = [&](auto wmc) __attribute__((always_inline)) {
    constexpr bool LD = decltype(wmc)::value != 0;
    constexpr int grp = LD ? 1 : 0;
    const char* pa = nullptr;
    unsigned vob = 0;
    f32x4 ca = {0.f, 0.f, 0.f, 0.f}, cb = {0.f, 0.f, 0.f, 0.f};
    bf16x8 sel = {0, 0, 0, 0, 0, 0, 0, 0};
    int tro = 0;
    u64 tg = 0;
    const int psh = (wid == 2 && !tya) ? 1 : 0, pstr = wid < 3 ? qstr : F_ * 2;
    const char* const pbase = wid == 0 ? qb : (wid == 1 ? qb + dp1 : (wid == 2 ? mb0 : gb));
    if (!LD) {
      const int rs = mtile * 8 + (lane >> 3), vs = ((lane & 7) >> 1) ^ (((rs >> 1) & 1) | ((mtile & 1) << 1));
      pa = pbase + ((psh & vs) ? dgu : 0) + (rs * pstr + (vs >> psh) * 32 + (lane & 1) * 16);
      vob = rs * (F_ * 2) + vs * 32 + (lane & 1) * 16;
      const short one = fr == fq ? 0x3f80 : 0;
      sel = bf16x8{one, one, one, one, one, one, one, one};
      tro = q4 * 128 + ((fq ^ ((q4 >> 1) | ((mtile & 1) << 1))) * 32) + pp * 8;
      asm volatile("" : "+v"(sel), "+v"(tro), "+v"(ca), "+v"(cb));
    }
    auto share = [&](int t, unsigned dsta, unsigned dstb) {
      long off = (long)(t * 64 * pstr);
      asm volatile("" : "+s"(off));
      GLDS_NC(pa + off, shm_base + dsta);
      if (wid == 0) glds16_p(gb + dgu + (long)(t * 64 * F_ * 2), vob, shm_base + dstb);
    };
    auto accum = [&](int oa, int ob) {
      ca = __builtin_amdgcn_mfma_f32_16x16x32_bf16(tr_frag(shm3 + tro + oa), sel, ca, 0, 0, 0);
      if (wid == 0) cb = __builtin_amdgcn_mfma_f32_16x16x32_bf16(tr_frag(shm3 + tro + ob), sel, cb, 0, 0, 0);
    };
    auto cgather = [&]() {
      int l = threadIdx.x & 63;
      asm volatile("s_memrealtime %0\n\ts_waitcnt lgkmcnt(0)" : "=s"(tg), "+v"(l) :: "memory");
      const char* const p4 = l < 32 ? (const char*)(csum + (ntile * 5 + 4) * 512 + wid * 16) + (l >> 2) * 256
                                    : (const char*)(cstamp + (ntile * 4 + (l < 36 ? wid : 0)) * 8);
      if (l < 40) GLDS_SC1(p4 + (l & 3) * 16, shm_base + QS + wid * 640);
#pragma unroll
      for (int i = 0; i < 2; ++i) GLDS_SC1((const char*)(csum + (ntile * 5 + wid) * 512) + i * 1024 + l * 16, shm_base + DMP + wid * 2048 + i * 1024);
    };
    if (!LD) share(2, DMP + 7168, DMP + 7168);
    if (LD) {
#pragma unroll
      for (int i = 0; i < NP1; ++i) {
        stage_part(1, kz + 1, i >> 1, i & 1);
        asm volatile("" : "+v"(acc[0][i]), "+v"(acc[1][i]), "+v"(acc[2][i]), "+v"(acc[3][i]));
      }
    }
#pragma unroll
    for (int a = 0; a < 4; ++a)
      asm volatile("" : "+v"(acc[a][0]), "+v"(acc[a][1]), "+v"(acc[a][2]), "+v"(acc[a][3]), "+v"(acc[a][4]), "+v"(acc[a][5]), "+v"(acc[a][6]),
                   "+v"(acc[a][7]), "+v"(acc[a][8]), "+v"(acc[a][9]));
    asm volatile("" : "+v"(aoff[0]), "+v"(aoff[1]), "+v"(boff[0]), "+v"(boff[1]));
    if constexpr (LD) {
      WAIT_V(NP1);
      BAR();
    } else {
      if (wid) WAIT_V(1);
      WAIT_V(2);
    }
    TS1(0);
#pragma clang loop unroll(disable)
    for (int kt = 0; kt < NT; ++kt) {
      int cur = kt & 1;
      if (!LD && kt == NT - 1) { correct(); __builtin_amdgcn_s_setprio(2); }
      if (!LD) {
        asm volatile("" : "+s"(cur));
        __builtin_amdgcn_s_waitcnt(0xC07F);
        asm volatile(".p2align 5");
        BAR();
      }
      lds_cptr sb = shm3;
      if (LD) {
        unsigned k2 = kt - 1;
        asm volatile("" : "+s"(k2));
        if (k2 < NT - 2) {
#pragma unroll
          for (int i = 0; i < NP1; ++i) stage_part(cur ^ 1, kt + 1, i >> 1, i & 1);
        }
      }
      if (LD && kt + 1 < NT) {
#pragma unroll
        for (int i = NP1; i < 18; ++i) stage_part(cur ^ 1, kt + 1, i >> 1, i & 1);
      } else if (LD) {
        correct();
      }
      auto fadr = [&](int n) {
        const int f = n < 2 ? 8 + n : (n < 4 ? 11 - n : (n - 4) >> 1), ks = n < 2 ? 1 : (n < 4 ? 0 : n & 1);
        return sb + boff[f & 1] + (f >> 1) * 8192 + ks * 4096;
      };
      auto rdl = [&](int n) { bl[n % 10] = tr_read(fadr(n % 20)); };
      auto rdh = [&](int n) { bh[n % 10] = tr_read(fadr(n % 20) + 512); };
      auto rda = [&](bf16x8* af, int ks, int mf) { af[mf] = *(const __attribute__((address_space(3))) bf16x8*)(sb + aoff[ks] + mf * 2048); };
      SB0();
      if (LD) __builtin_amdgcn_s_waitcnt(0xC07F);
      if (LD) asm volatile(".p2align 5\n\ts_nop 0\n\ts_nop 0");
      sfor<80>([&](auto uc) __attribute__((always_inline)) {
        constexpr int u = decltype(uc)::value;
        SB0();
        if constexpr (u < 16) {
          const int n = u >> 2;
          mm(n, n < 2 ? 8 + n : 11 - n, n < 2 ? 1 : 0, (n & 1) ? 3 - (u & 3) : u & 3);
        } else {
          const int f = (u - 16) >> 3, i = (u >> 1) & 3, ks = (u ^ i) & 1;
          if ((u & 7) == 0) { asm volatile("" :: "v"(bl[(5 + 2 * f) % 10]), "v"(bh[(5 + 2 * f) % 10])); SB0(); }
          mm(4 + 2 * f + ks, f, ks, (f & 1) ? 3 - i : i);
        }
        SB0();
        if (u == 0) { rdl(2); rdh(2); }
        if (u == 1) { rda(af0, 0, 0); rda(af0, 0, 1); }
        if (u == 2) { rda(af0, 0, 2); rda(af0, 0, 3); }
        if (u == 3) { rdl(3); rdh(3); }
        if constexpr (u >= 4 && u < 8) rda(af1, 1, 7 - u);
        if (u >= 8 && u < 16) { if (u & 1) rdh(u >> 1); else rdl(u >> 1); }
        if (u >= 21 && u < 76 && (u & 1)) {
          const int n = 8 + ((u - 21) >> 2);
          if ((u - 21) & 2) rdh(n); else rdl(n);
        }
      });
      SB0();
      if (LD)
        asm volatile("s_cmp_eq_u32 %0, %1\n\ts_waitcnt vmcnt(0) lgkmcnt(0)\n\ts_cbranch_scc1 1f\n\ts_barrier\n1:" :: "s"(kt), "n"(NT - 1) : "memory", "scc");
      SB0();
      {
        const int d = cur ? -G1_STAGE : G1_STAGE;
        aoff[0] += d; aoff[1] += d; boff[0] += d; boff[1] += d;
        asm volatile("" : "+v"(aoff[0]), "+v"(aoff[1]), "+v"(boff[0]), "+v"(boff[1]));
      }
      if (!LD && kt <= 19 - SD) {
        int lane = threadIdx.x & 63;
        asm volatile("" : "+v"(lane));
        auto clock = [&]() { u64 now; asm volatile("s_memrealtime %0\n\ts_waitcnt lgkmcnt(0)" : "=s"(now) :: "memory"); return now; };
        WAIT_V(0);
        if (kt <= 16 - SD) {
          if (kt >= 1 + KS0) { accum(DMP + wid * 1024, DMP + 4096); WAIT_L(0); }
          if (kt <= 15 - SD) {
            if (kt >= KS0) share(kt + SD, DMP + wid * 1024, DMP + 4096);
          } else {
            GLDS_SC1(rstat + m0 + wid * 64 + (lane & 32) + 2 * (lane & 15) + ((lane >> 4) & 1), shm_base + AUX + wid * 1024);
          }
          if (kt == 16 - SD && (lane & 15) < 4) {
            const __amdgpu_buffer_rsrc_t rc = __builtin_amdgcn_make_buffer_rsrc((void*)(csum + ntile * 5 * 512 + mtile * 64), 0, 5 * 2048, 0x00020000);
            const int co = (lane & 15) * 64 + (lane >> 4) * 16;
            __builtin_amdgcn_raw_buffer_store_b128(__builtin_bit_cast(u32x4, ca), rc, co + wid * 2048, 0, 16);
            if (wid == 0) __builtin_amdgcn_raw_buffer_store_b128(__builtin_bit_cast(u32x4, cb), rc, co + 4 * 2048, 0, 16);
          }
          if (kt < SD + KS0) {
            if (kt <= SD + KS0 - 4) share(kt + 3, DMP + 7168, DMP + 7168);
            const int sp = cur * G1_STAGE + 32768 + mtile * 1024;
            accum(sp + wid * 8192, sp + 4 * 8192);
            if (kt == 1 || kt == 2) {
              const bf16x8 ones = {0x3f80, 0x3f80, 0x3f80, 0x3f80, 0x3f80, 0x3f80, 0x3f80, 0x3f80};
              const char* const xr = shm + AUX + wid * 2048 + (lane & 8) * 128 + (lane >> 4) * 128 + (lane & 7) * 16;
              const bf16x8 r0 = *(const bf16x8*)xr, r1 = *(const bf16x8*)(xr + 512);
              const f32x4 z = {0.f, 0.f, 0.f, 0.f};
              const f32x4 s1 = __builtin_amdgcn_mfma_f32_16x16x32_bf16(ones, r1, __builtin_amdgcn_mfma_f32_16x16x32_bf16(ones, r0, z, 0, 0, 0), 0, 0, 0);
              const f32x4 s2 = __builtin_amdgcn_mfma_f32_16x16x32_bf16(r1, r1, __builtin_amdgcn_mfma_f32_16x16x32_bf16(r0, r0, z, 0, 0, 0), 0, 0, 0);
              float t0 = __uint_as_float((unsigned)tg) + s1[0];
              asm volatile("" : "+v"(t0));
              const float t1 = __uint_as_float((unsigned)(tg >> 32)) + ((lane & 2) ? ((lane & 1) ? s2[3] : s2[2]) : ((lane & 1) ? s2[1] : s2[0]));
              tg = ((u64)__float_as_uint(t1) << 32) | __float_as_uint(t0);
            }
            if (kt < 2) {
#pragma unroll
              for (int i = 0; i < 2; ++i) glds16_p(x + (size_t)(srow + i) * D_ + kt * 512, lane * 16, shm_base + AUX + wid * 2048 + i * 1024);
            }
            if (kt == PUB_K) {
              if (wid == 0 && lane < 8) *(unsigned*)(shm + CNT + lane * 4) = 0u;
              const bool dg = ((lane >> 2) & 3) == (lane >> 4);
              float sm = half_sum(dg ? __uint_as_float((unsigned)tg) : 0.f);
              asm volatile("" : "+v"(sm));
              const float sq = half_sum(dg ? __uint_as_float((unsigned)(tg >> 32)) : 0.f);
              const u64 now = clock();
              if ((lane & 31) == 0) {
                const __amdgpu_buffer_rsrc_t rs = __builtin_amdgcn_make_buffer_rsrc((void*)(rstat + srow), 0, 32, 0x00020000);
                __builtin_amdgcn_raw_buffer_store_b128(u32x4{__float_as_uint(sm), (unsigned)now, __float_as_uint(sq), (unsigned)now ^ (unsigned)(now >> 32)}, rs,
                                                       lane >> 1, 0, 16);
              }
            }
          }
          if (kt >= PF_K0 && kt < PF_K0 + 5) {
#pragma unroll
            for (int i = 0; i < 2; ++i) {
              const int c = (bid * 4 + wid) * 10 + (kt - PF_K0) * 2 + i;
              if (c < 9216) {
                const u16* src = (c < 7168 ? wd + (size_t)c * 512 : wo + (size_t)(c - 7168) * 512) + lane * 8;
                GLDS_NC(src, shm_base + DMP + 7168);
              }
            }
          }
          if (kt >= CP_K0 && kt <= CP_K1) {
            if (kt < CP_K1 - 1) {
              const int r1 = kt - CP_K0 - wid, ja = bid * 4 + wid, jb = (wid == 0 && bid < 128) ? 1024 + bid : 2048;
              const int q1 = r1 == 0 ? ja : (r1 == 1 ? jb : 2048), q0 = r1 == 1 ? ja : (r1 == 2 ? jb : 2048);
              if (q0 < 1152) {
                const __amdgpu_buffer_rsrc_t rc = __builtin_amdgcn_make_buffer_rsrc((void*)(wcopy + (q0 & 1) * 1179648 + (q0 >> 1) * 2048), 0, 2048, 0x00020000);
                __builtin_amdgcn_raw_buffer_store_b128(*(const u32x4*)(shm + AUX + wid * 2048 + lane * 16), rc, (lane >> 3) * 256 + (lane & 7) * 16, 0, 16);
                WAIT_L(0);
              }
              if (q1 < 1152) {
                const char* src = (const char*)((q1 >> 1) < 448 ? wd + (size_t)(q1 >> 1) * 8 * D_ : wo + (size_t)((q1 >> 1) - 448) * 8 * D_);
                src += ((3 - ((unsigned)(uintptr_t)src >> 7)) & 7) * 128 + (q1 & 1) * 1024;
                GLDS(src + (lane >> 3) * 2048 + (lane & 7) * 16, shm_base + AUX + wid * 2048);
              }
            } else if (kt == CP_K1) {
              if (wid == 0) {
                const u64 now = clock();
                if (lane == 0) __hip_atomic_store(&donec[bid], now, RLX_AGENT);
              }
              if (wid == 2) GLDS_SC1((const char*)(done2 + bid * 128) + lane * 16, shm_base + DMP + 7168);
            }
          }
        } else if (kt == 17 - SD) {
          const u64 now = clock();
          if (lane == 0) __hip_atomic_store(&cstamp[(ntile * 4 + wid) * 8 + mtile], now, RLX_AGENT);
          if (wid >= 2) GLDS_SC1((const char*)donec + (wid - 2) * 1024 + lane * 16, shm_base + DMP + wid * 2048);
          {
            const char* const wp = (const char*)wd + (size_t)(xcd < 4 ? 28 + 5 * (xcd & 1) : 5 - 5 * (xcd & 1)) * 131072 + (idx * 4 + wid) * 1024;
#pragma unroll
            for (int i = 0; i < 4; ++i) glds16_p(wp + (long)((i & 1) + (i >> 1) * 14) * 131072, lane * 16, shm_base + DMP + wid * 2048 + 1024);
          }
          u32x4 gr = *(const u32x4*)(shm + AUX + wid * 1024 + lane * 16);
          bool late = false;
          if (__builtin_expect(!__all(fresh(((u64)(gr[1] ^ gr[3]) << 32) | gr[1], t_own)), 0)) {
            const __amdgpu_buffer_rsrc_t rs = __builtin_amdgcn_make_buffer_rsrc((void*)(rstat + m0), 0, 4096, 0x00020000);
            const int prow = wid * 64 + (lane & 32) + 2 * (lane & 15) + ((lane >> 4) & 1);
            for (;;) {
              gr = __builtin_bit_cast(u32x4, __builtin_amdgcn_raw_buffer_load_b128(rs, prow * 16, 0, 16));
              if (__all(fresh(((u64)(gr[1] ^ gr[3]) << 32) | gr[1], t_own))) break;
              if (__builtin_amdgcn_s_memrealtime() - t_own > TIMEOUT_) { gr[1] = row_stats_serial(x + (size_t)(m0 + prow) * D_); late = true; break; }
              __builtin_amdgcn_s_sleep(2);
            }
          }
          float mean = __uint_as_float(gr[0]) * (1.0f / D_);
          asm volatile("" : "+v"(mean));
          const unsigned ps = pack_stats(mean, fmaxf(__builtin_fmaf(-mean, mean, __uint_as_float(gr[2]) * (1.0f / D_)), 0.f));
          const unsigned g0 = late ? gr[1] : ps;
          const float nm = -lo_f(g0), dn = hi_f(g0), r = __builtin_amdgcn_rcpf(dn);
          float k1 = -2.3022081986f * r, d2 = dn * dn;
          asm volatile("" : "+v"(k1));
          float k2 = -0.10294324f * r;
          asm volatile("" : "+v"(d2), "+v"(k2));
          const f32x4 fc = {nm, k1, k2 * r * r, d2};
          *(f32x4*)(shm + AUX + wid * 1024 + lane * 16) = fc;
        } else if (kt == 18 - SD) {
          const u32x4 c4 = *(const u32x4*)(shm + DMP + wid * 2048 + lane * 16);
          asm volatile("" :: "v"(c4) : "memory");
          cgather();
          const u64 win = tg - t_own;
          auto frs = [&](unsigned lo, unsigned hi) { return (((u64)hi << 32) | lo) - t_own - 100ull < win; };
          if (wid >= 2) *(unsigned*)(shm + CNT - 4 + wid * 4) = __all(frs(c4[0], c4[1]) & frs(c4[2], c4[3])) ? 1u : 0u;
        } else {
          const char* const sc = shm + (lane < 32 ? DMP + wid * 2048 + lane * 8 : QS + wid * 640 + (lane & 7) * 8);
          const int st = lane < 32 ? 256 : 64;
          const f32x4 fc = *(const f32x4*)(shm + AUX + wid * 1024 + lane * 16);
          f32x2 w[8];
          bool late = false;
          for (;;) {
            const u64 sv = *(const u64*)(shm + QS + wid * 640 + 512 + (lane & 15) * 8);
#pragma unroll
            for (int r = 0; r < 8; ++r) w[r] = *(const f32x2*)(sc + r * st);
            if (__builtin_expect(__all(sv - t_own - 100ull < tg - t_own - 200ull), 1)) break;
            late = __builtin_amdgcn_s_memrealtime() - t_own > TIMEOUT_;
            if (late) break;
            __builtin_amdgcn_s_sleep(2);
            WAIT_L(0);
            cgather();
            WAIT_V(0);
          }
          float t0 = w[0][0], t1 = w[0][1];
#pragma unroll
          for (int r = 1; r < 8; ++r)
            asm("v_add_f32 %0, %0, %2\n\tv_add_f32 %1, %1, %3" : "+v"(t0), "+v"(t1) : "v"(w[r][0]), "v"(w[r][1]));
          if (__builtin_expect(late, 0)) {
            const int gq = (lane >> 3) & 3;
            const char* pc = (lane < 32 ? pbase + ((psh & gq) ? dgu : 0) + (gq >> psh) * 32 : gb + dgu + wid * 32) + (lane & 7) * 4;
            t0 = 0.f; t1 = 0.f;
#pragma unroll 1
            for (int k = 0; k < D_; ++k) { const unsigned v = *(const unsigned*)(pc + (size_t)k * (lane < 32 ? pstr : F_ * 2)); t0 += lo_f(v); t1 += hi_f(v); }
          }
          if (lane < 40)
            *(f32x2*)((float*)(shm + CF) + (lane < 32 ? (lane >> 4) * 160 + wid * 32 + (lane & 15) * 2 : (wid >> 1) * 160 + 128 + (wid & 1) * 16 + (lane & 7) * 2)) = f32x2{t0, t1};
          float* const tw = (float*)(shm + DMP + wid * 2048) + lane;
#pragma unroll
          for (int i = 0; i < 4; ++i) tw[i * 64] = fc[i];
          ((float*)(shm + QS + wid * 640))[lane] = fc[1] * (-1.0f / 2.3022081986f);
        }
      }
      SB0();
    }
#pragma unroll
    for (int u = 0; u < 8; ++u) mm(u >> 2, 8 + (u >> 2), 1, u & 3);
  TS1(1);
  __builtin_amdgcn_sched_barrier(0);
  int tz = threadIdx.x;
  asm volatile("s_nop 7" : "+v"(tz));
  __builtin_amdgcn_sched_barrier(0);
  const int li = tz & 15, lq = (tz >> 4) & 3, ln = tz & 63;
  const __amdgpu_buffer_rsrc_t ra = __builtin_amdgcn_make_buffer_rsrc((void*)abuf, 0, 16 * 72 * 16384, 0x00020000);
  float* const svr = (float*)(shm + QS + wm * 640);
  float* const tb = (float*)(shm + DMP + wm * 2048);
  typedef __attribute__((address_space(3))) unsigned lds_u32;
  lds_u32* const cnt = (lds_u32*)(shm3 + CNT);
  unsigned vcnt = shm_base + CNT;
  asm volatile("" : "+v"(vcnt));
  auto signal = [&](unsigned v) {
    unsigned t;
    asm volatile("v_mov_b32 %0, %2\n\ts_mov_b64 exec, 1\n\tds_add_u32 %1, %0\n\ts_mov_b64 exec, -1" : "=&v"(t) : "v"(vcnt), "s"(v) : "memory");
  };
  auto await = [&](unsigned mask, unsigned val) {
    unsigned t, u;
    asm volatile("1:\n\tds_read_b32 %0, %2\n\ts_waitcnt lgkmcnt(0)\n\tv_readfirstlane_b32 %1, %0\n\ts_and_b32 %1, %1, %3\n\ts_cmp_eq_u32 %1, %4\n\t"
                 "s_cbranch_scc1 2f\n\ts_sleep 1\n\ts_branch 1b\n\t2:" : "=&v"(t), "=&s"(u) : "v"(vcnt), "s"(mask), "s"(val) : "memory", "scc");
  };
  if (grp) __builtin_amdgcn_s_setprio(1);
  signal(1u << (8 * grp));
  f32x4 rf[4];
#pragma unroll
  for (int mf = 0; mf < 4; ++mf) rf[mf] = *(const f32x4*)(shm + AUX + (wm * 64 + mf * 16 + li) * 16);
  const int sbase = ((mtile * 2 + (wm >> 1)) * 72 * 8 + (wm & 1) * 4) * 2048;
  auto putf = [&](int kg, int mf, u32x4 v) {
    __builtin_amdgcn_raw_buffer_store_b128(v, ra, ln * 16 + sbase + ((kg >> 1) * 8 + mf) * 2048 + (kg & 1) * 1024, 0, 16);
  };
  auto gelp = [&](f32x2 tg, f32x2 tu, f32x2 k12, f32x2 k22, f32x2 d2) {
    const f32x2 arg = tg * __builtin_elementwise_fma(tg * tg, k22, k12);
    const f32x2 dn = __builtin_elementwise_fma(f32x2{__builtin_amdgcn_exp2f(arg[0]), __builtin_amdgcn_exp2f(arg[1])}, d2, d2);
    const f32x2 r = (tg * tu) * f32x2{__builtin_amdgcn_rcpf(dn[0]), __builtin_amdgcn_rcpf(dn[1])};
    return pack2(r[0], r[1]);
  };
  auto gels = [&](const f32x4& g, const f32x4& u, const f32x4& k) {
    float t0, t1, t2, t3, n0, n1, n2, n3;
    asm("v_mul_f32 %0, %8, %8\n\tv_mul_f32 %1, %9, %9\n\tv_mul_f32 %2, %10, %10\n\tv_mul_f32 %3, %11, %11\n\t"
        "v_fma_f32 %0, %0, %17, %16\n\tv_fma_f32 %1, %1, %17, %16\n\tv_fma_f32 %2, %2, %17, %16\n\tv_fma_f32 %3, %3, %17, %16\n\t"
        "v_mul_f32 %0, %8, %0\n\tv_mul_f32 %1, %9, %1\n\tv_mul_f32 %2, %10, %2\n\tv_mul_f32 %3, %11, %3\n\t"
        "v_exp_f32 %0, %0\n\tv_mul_f32 %4, %8, %12\n\tv_exp_f32 %1, %1\n\tv_mul_f32 %5, %9, %13\n\t"
        "v_exp_f32 %2, %2\n\tv_mul_f32 %6, %10, %14\n\tv_exp_f32 %3, %3\n\tv_mul_f32 %7, %11, %15\n\t"
        "v_fma_f32 %0, %0, %18, %18\n\tv_fma_f32 %1, %1, %18, %18\n\tv_rcp_f32 %0, %0\n\tv_fma_f32 %2, %2, %18, %18\n\t"
        "v_rcp_f32 %1, %1\n\tv_fma_f32 %3, %3, %18, %18\n\tv_rcp_f32 %2, %2\n\tv_mul_f32 %0, %4, %0\n\t"
        "v_rcp_f32 %3, %3\n\tv_mul_f32 %1, %5, %1\n\tv_mul_f32 %2, %6, %2\n\tv_mul_f32 %3, %7, %3\n\t"
        "v_cvt_pk_bf16_f32 %0, %0, %1\n\tv_cvt_pk_bf16_f32 %1, %2, %3"
        : "=&v"(t0), "=&v"(t1), "=&v"(t2), "=&v"(t3), "=&v"(n0), "=&v"(n1), "=&v"(n2), "=&v"(n3)
        : "v"(g[0]), "v"(g[1]), "v"(g[2]), "v"(g[3]), "v"(u[0]), "v"(u[1]), "v"(u[2]), "v"(u[3]), "v"(k[1]), "v"(k[2]), "v"(k[3]));
    return u32x2{__float_as_uint(t0), __float_as_uint(t1)};
  };
  auto gel2 = [&](int p, int kg, int mf0 = 0, int mf1 = 4, int nsc = 0, bool opq = false) {
#pragma unroll
    for (int mf = mf0; mf < mf1; ++mf) {
      const f32x2 k12 = {rf[mf][1], rf[mf][1]}, k22 = {rf[mf][2], rf[mf][2]}, d2 = {rf[mf][3], rf[mf][3]};
      u32x4 o;
      if (opq) asm volatile("" : "+v"(acc[mf][p * 2]), "+v"(acc[mf][p * 2 + 1]));
      if (mf < nsc) {
        const u32x2 a = gels(acc[mf][p * 2], acc[mf][p * 2 + 2], rf[mf]), b = gels(acc[mf][p * 2 + 1], acc[mf][p * 2 + 3], rf[mf]);
        o = u32x4{a[0], a[1], b[0], b[1]};
      } else {
#pragma unroll
        for (int i = 0; i < 4; ++i) {
          const int j = i >> 1, e = (i & 1) * 2;
          const f32x4 g = acc[mf][p * 2 + j], u = acc[mf][p * 2 + 2 + j];
          o[i] = gelp(f32x2{g[e], g[e + 1]}, f32x2{u[e], u[e + 1]}, k12, k22, d2);
        }
      }
      putf(kg, mf, o);
      if (mf < nsc) SB0();
    }
  };
  auto client = [&](int l) { return (((l & 7) * 4 + (mtile & 3)) * 8) + (((mtile >> 2) * 2 + ((l >> 4) & 1)) ^ ((l & 8) ? 5 : 0)); };
  auto notify = [&](u64 lanes) {
    int l = threadIdx.x & 63;
    asm volatile("" : "+v"(l));
    const unsigned off = (client(l) * 128 + (l & 32) + ntile) * 8;
    u64 now, vn;
    asm volatile("s_memrealtime %0\n\ts_nop 0\n\ts_waitcnt lgkmcnt(0)\n\tv_mov_b64 %1, %0\n\ts_mov_b64 exec, %4\n\tglobal_store_dwordx2 %2, %1, %3 sc1\n\ts_mov_b64 exec, -1"
                 : "=&s"(now), "=&v"(vn) : "v"(off), "s"(done1), "s"(lanes) : "memory");
  };
  const u64 myh = 0xffffull << ((wm >> 1) * 16);
  const u64 mrole = (xcd & 4) ? 0x00ff00ffull : 0xff00ff00ull;
  const u64 mall = (tya || (xcd & 3) == 3) ? 0xffffffffull : mrole;
  auto arrive = [&](auto pc) {
    constexpr int part = decltype(pc)::value;
    unsigned t;
    asm volatile("v_mov_b32 %0, 1\n\ts_mov_b64 exec, 1\n\tds_add_rtn_u32 %0, %1, %0 offset:%2\n\ts_mov_b64 exec, -1\n\ts_waitcnt lgkmcnt(0)"
                 : "=&v"(t) : "v"(vcnt), "n"(12 + 4 * part) : "memory");
    if (__builtin_amdgcn_readfirstlane(t) == 3u) notify(part ? (myh & mall) << 32 : myh & mrole);
  };
  const int role = 1 - (xcd >> 2);
  const int xb = role ? xcd : xcd ^ 5, par = xb & 1;
  const int mt2 = ((xb >> 1) * 4 + (idx & 3)) * 2 + par, nt2 = idx >> 2;
  auto rsrc = [](u64 b, unsigned n) { return u32x4{(unsigned)b, (unsigned)(b >> 32) & 0xffffu, n, 0x00020000u}; };
  u32x4 rw[2];
  int wsh[2];
  unsigned vb[2][2], cokw = 0;
  auto wsetup = [&](const u16* w, int rows, int crow0, int l) {
    const unsigned vr = (wid >> 1) * 32 + (wid & 1) * 8 + ((l >> 5) & 1) * 16 + ((l >> 3) & 3), vc = (((l >> 1) & 3) ^ ((l >> 4) & 1)) * 32 + (l & 1) * 16;
    u64 bs = (u64)(uintptr_t)(wcopy + (nt2 >> 2) * 1179648 + crow0 * 256 - 1024), bf = (u64)(uintptr_t)(w + nt2 * 128) - 1024;
    asm volatile("" : "+s"(bs), "+s"(bf));
#pragma unroll
    for (int i = 0; i < 2; ++i) {
      const bool slow = ((((unsigned)bf >> 7) + i) & 7) == (cokw ? 3u : 8u);
      const int sh = slow ? 8 : 11;
      rw[i] = rsrc(slow ? bs : bf + i * 128, ((rows - 1) << sh) + 128 + 1024);
      wsh[i] = 6 + sh;
      vb[i][0] = (vr << sh) + vc + 1024;
      vb[i][1] = ((vr + 4) << sh) + (vc ^ 64);
    }
  };
#define G2_W "s_add_u32 m0, %[l], %[ow]\n\ts_lshl_b32 %[t], %[k], %[s0]\n\tbuffer_load_dwordx4 %[v0], %[w0], %[t] offen sc1 lds\n\t" \
             "buffer_load_dwordx4 %[v1], %[w0], %[t] offen offset:1024 sc1 lds\n\t" \
             "s_add_u32 m0, m0, 8192\n\ts_lshl_b32 %[t], %[k], %[s1]\n\tbuffer_load_dwordx4 %[v2], %[w1], %[t] offen sc1 lds\n\t" \
             "buffer_load_dwordx4 %[v3], %[w1], %[t] offen offset:1024 sc1 lds"
#define G2_WOP [ow] "s"(16384 + wid * 2048), [k] "s"(kt), [s0] "s"(wsh[0]), [s1] "s"(wsh[1]), [w0] "s"(rw[0]), [w1] "s"(rw[1]), \
               [v0] "v"(vb[0][0]), [v1] "v"(vb[0][1]), [v2] "v"(vb[1][0]), [v3] "v"(vb[1][1])
#define G2_A "s_add_u32 m0, %[l], %[oa]\n\ts_lshl_b32 %[t], %[k], 14\n\tbuffer_load_dwordx4 %[va], %[a], %[t] offen sc1 lds\n\t" \
             "buffer_load_dwordx4 %[va], %[a], %[t] offen offset:1024 sc1 lds\n\t"
#define G2_A2 "buffer_load_dwordx4 %[va], %[a], %[t] offen offset:2048 sc1 lds\n\tbuffer_load_dwordx4 %[va], %[a], %[t] offen offset:3072 sc1 lds\n\t"
  constexpr int NY = 28;
  const int ny = (xcd & 1) ? NY - DLT : NY + DLT;
  const u32x4 ra2 = rsrc((u64)(uintptr_t)abuf + mt2 * (72 * 16384), 72 * 16384);
  unsigned va, tab, nlo, nhi, sl = shm_base + 4 * G2_STAGE;
  const int kfirst = 28 * role + 5 * par;
  int q = 3, kt = kfirst + 15;
  u64 okb = 0;
  auto adv = [&]() { ++q; kt = __builtin_amdgcn_readlane(tab, q); sl = sl == shm_base + (G2_NSTAGE - 1) * G2_STAGE ? shm_base : sl + G2_STAGE; };
  auto stage_y = [&]() {
    unsigned t;
    asm volatile(G2_A G2_A2 G2_W : [t] "=&s"(t) : [l] "s"(sl), [oa] "s"((wid ^ role) * 4096), [a] "s"(ra2), [va] "v"(va), G2_WOP : "memory", "scc", "m0");
    adv();
  };
  auto digest = [&](u64 v, u64 valid = ~0ull) {
    const u64 r = __ballot(fresh(v, t_own)) & valid;
    const unsigned hi = (unsigned)(r >> 32), lo = (unsigned)r | hi;
    okb |= __ballot(((nlo & ~lo) | (nhi & ~hi)) == 0u);
  };
  auto aload = [&](const u64* base, unsigned off) {
    asm volatile("s_nop 4\n\tglobal_load_dwordx2 v[254:255], %0, %1 sc1" :: "v"(off), "s"(base) : "memory", "v254", "v255");
    SB0();
  };
  auto aget = [&]() { u64 v; asm volatile("v_mov_b64 %0, v[254:255]" : "=v"(v) :: "memory"); return v; };
  auto snapshot = [&]() { aload(done1 + bid * 128, ln * 8); };
  auto peek = [&]() { digest(aget()); };
  auto snapshot1 = [&]() { aload(done1 + bid * 128 + role * 16, (ln & 15) * 8); };
  auto peek1 = [&]() { digest(aget(), 0xffffull << (16 * role)); };
  auto wpair = [&](int g, int i) {
    unsigned t;
    asm volatile("s_mov_b32 m0, %1\n\ts_lshl_b32 %0, %2, %3\n\tbuffer_load_dwordx4 %5, %4, %0 offen sc1 lds\n\tbuffer_load_dwordx4 %6, %4, %0 offen offset:1024 sc1 lds"
                 : "=&s"(t) : "s"(shm_base + (g + 1) * G2_STAGE + 16384 + i * 8192 + wid * 2048), "s"(kfirst + (g == 2 ? 14 : g)), "s"(wsh[i]), "s"(rw[i]),
                   "v"(vb[i][0]), "v"(vb[i][1]) : "memory", "scc", "m0");
    SB0();
  };
  auto aquad = [&](int g) {
    unsigned t;
    asm volatile(G2_A G2_A2 : [t] "=&s"(t) : [l] "s"(shm_base + (g + 1) * G2_STAGE), [oa] "s"((wid ^ role) * 4096), [a] "s"(ra2), [va] "v"(va),
                 [k] "s"(((okb >> g) & 1) ? kfirst + (g == 2 ? 14 : g) : 100) : "memory", "scc", "m0");
    SB0();
  };
  auto mid = [&]() {
    cokw = __builtin_amdgcn_readfirstlane(cnt[1] & cnt[2]);
    wsetup(wd, F_, 0, ln);
    va = ln * 16 + wid * 4096;
    {
      const int ext = ln >= NY ? 1 : 0, qq = ln - ext * DLT, so = ln - ny;
      int j0 = 5 * par, j1 = 5 - 5 * par;
      asm volatile("" : "+s"(j1));
      const int j = (qq < 8 ? j0 : (qq < 16 ? j1 : 2)) + ((unsigned)(0x92492444444444ull >> (2 * qq)) & 3);
      const int xs = ((role ^ ext) << 2) | ((unsigned)(0xfd5a80f5a0f5a0ull >> (2 * qq)) & 3);
      unsigned bits = ((0x41a8282u >> (4 * j)) & 15u) << (4 * xs), ob = (so < 16 ? 1u : 0u) << (2 * so), ty = 7 * xs + j;
      asm volatile("" : "+v"(bits), "+v"(ob), "+v"(ty));
      tab = so < 0 ? ty : so;
      nlo = ln < 8 ? bits : 0u;
      nhi = so < 0 ? bits - nlo : ob;
    }
    asm volatile("" :: "s"(rw[0]), "s"(rw[1]), "s"(wsh[0]), "s"(wsh[1]), "s"(ra2));
    asm volatile("" : "+v"(vb[0][0]), "+v"(vb[0][1]), "+v"(vb[1][0]), "+v"(vb[1][1]), "+v"(va), "+v"(tab), "+v"(nlo), "+v"(nhi));
    await(0xffffu, 0x404u);
    wpair(0, 0);
    wpair(0, 1);
    WAIT_V(4);
    arrive(std::integral_constant<int, 0>{});
    __builtin_amdgcn_s_setprio(0);
  };
  int tsel = ntile & 1;
  asm volatile("" : "+s"(tsel));
  if (tsel != 0) {
    if (grp == 0) gel2(0, 2 * (kt0 + he) + wc, 0, NSC, NSC);
    else gel2(0, 2 * (kt0 + he) + wc, 0, NSC);
    gel2(0, 2 * (kt0 + he) + wc, NSC, 4);
    if (grp == 0) mid();
#pragma unroll
    for (int mf = 0; mf < 4; ++mf) {
      f32x4 t[4];
#pragma unroll
      for (int i = 1; i < 4; ++i) t[i] = *(const f32x4*)(tb + i * 64 + mf * 16 + lq * 4);
      const f32x4 g = acc[mf][4], u = acc[mf][5];
      u32x2 o;
#pragma unroll
      for (int e = 0; e < 4; e += 2)
        o[e >> 1] = gelp(f32x2{g[e], g[e + 1]}, f32x2{u[e], u[e + 1]}, f32x2{t[1][e], t[1][e + 1]}, f32x2{t[2][e], t[2][e + 1]}, f32x2{t[3][e], t[3][e + 1]});
      *(u32x2*)(shm + wm * 4096 + mf * 1024 + wc * 512 + li * 32 + lq * 8) = o;
      if (mf == 1 || grp == 0) SB0();
      if (grp == 0) {
        wpair(1 + (mf >> 1), mf & 1);
        if (mf == 3) snapshot1();
      }
    }
    signal(0x10000u << vid);
    if (grp) {
      gel2(3, 2 * (kt0 + 2 + he) + wc, 0, 4);
    } else {
#pragma unroll
      for (int mf = 0; mf < 4; ++mf) {
        gel2(3, 2 * (kt0 + 2 + he) + wc, mf, mf + 1);
        SB0();
        if (mf == 0) { WAIT_V(1); peek1(); }
        if (mf < 3) aquad(mf);
        if (mf == 2) snapshot1();
      }
    }
    await(0x110000u << wm, 0x110000u << wm);
#pragma unroll
    for (int i = 0; i < 2; ++i) putf(2 * (kt0 + 4) + he, 2 * wc + i, __builtin_bit_cast(u32x4, tr_frag(shm3 + wm * 4096 + wc * 2048 + i * 1024 + ln * 8)));
    if (grp == 0) WAIT_V(9);
  } else {
    if (grp) __builtin_amdgcn_s_setprio(3);
    float rd[4];
#pragma unroll
    for (int mf = 0; mf < 4; ++mf) rd[mf] = svr[mf * 16 + li];
    auto cvs = [&](const f32x4& a, const f32x4& b, const f32x4& r) {
      float t0, t1, t2, t3, t4, t5, t6, t7;
      asm("v_mul_f32 %0, %8, %16\n\tv_mul_f32 %4, %9, %17\n\tv_mul_f32 %1, %10, %18\n\tv_mul_f32 %5, %11, %19\n\t"
          "v_mul_f32 %2, %12, %16\n\tv_mul_f32 %6, %13, %17\n\tv_mul_f32 %3, %14, %18\n\tv_mul_f32 %7, %15, %19\n\t"
          "v_cvt_pk_bf16_f32 %0, %0, %4\n\tv_cvt_pk_bf16_f32 %1, %1, %5\n\tv_cvt_pk_bf16_f32 %2, %2, %6\n\tv_cvt_pk_bf16_f32 %3, %3, %7"
          : "=&v"(t0), "=&v"(t1), "=&v"(t2), "=&v"(t3), "=&v"(t4), "=&v"(t5), "=&v"(t6), "=&v"(t7)
          : "v"(a[0]), "v"(a[1]), "v"(a[2]), "v"(a[3]), "v"(b[0]), "v"(b[1]), "v"(b[2]), "v"(b[3]), "v"(r[0]), "v"(r[1]), "v"(r[2]), "v"(r[3]));
      return u32x4{__float_as_uint(t0), __float_as_uint(t1), __float_as_uint(t2), __float_as_uint(t3)};
    };
    auto cvp = [&](const f32x4& a, const f32x4& b) { return u32x4{pack2(a[0], a[1]), pack2(a[2], a[3]), pack2(b[0], b[1]), pack2(b[2], b[3])}; };
    auto cvk = [&](int mf, bool sc) {
      const f32x4 r4 = {rd[mf], rd[mf], rd[mf], rd[mf]};
      return sc ? cvs(acc[mf][2], acc[mf][3], r4) : cvp(acc[mf][2] * r4, acc[mf][3] * r4);
    };
    auto cvv = [&](int mf, bool sc) {
      const f32x4 r4 = *(const f32x4*)(svr + mf * 16 + lq * 4);
      return sc ? cvs(acc[mf][4], acc[mf][5], r4) : cvp(acc[mf][4] * r4, acc[mf][5] * r4);
    };
    u32x4 kx[4], vx[4], qx[4], hx[4];
    const bf16x8 ones = {0x3f80, 0x3f80, 0x3f80, 0x3f80, 0x3f80, 0x3f80, 0x3f80, 0x3f80};
    kx[2] = cvk(2, true); kx[3] = cvk(3, true);
    vx[2] = cvv(2, true); vx[3] = cvv(3, true);
    const bool giveh = !grp && wm == 3 && mtile < 7, needh = grp && wm == 0 && mtile > 0;
    char* const hptr = (char*)halo + (size_t)((needh ? mtile - 1 : mtile) * 32 + ntile + wc) * 16384;
    if (grp || wm < 3) {
      char* const ex = shm + (vid - wc) * 4096 + ln * 16;
      *(u32x4*)ex = kx[2]; *(u32x4*)(ex + 1024) = kx[3]; *(u32x4*)(ex + 2048) = vx[2]; *(u32x4*)(ex + 3072) = vx[3];
      signal(0x10000u << vid);
    } else if (giveh) {
      const __amdgpu_buffer_rsrc_t rh = __builtin_amdgcn_make_buffer_rsrc((void*)hptr, 0, 9216, 0x00020000);
      const u64 now = __builtin_amdgcn_s_memrealtime();
      const unsigned tag = (unsigned)now;
#pragma unroll
      for (int r = 0; r < 8; ++r) {
        const u32x4 w = r < 4 ? kx[2 + (r >> 1)] : vx[r >> 1];
        __builtin_amdgcn_raw_buffer_store_b128(u32x4{w[2 * (r & 1)], tag, w[2 * (r & 1) + 1], tag}, rh, ln * 16 + r * 1024, 0, 16);
      }
      __builtin_amdgcn_raw_buffer_store_b128(u32x4{tag, (unsigned)(now >> 32), tag, (unsigned)(now >> 32)}, rh, ln * 16 + 8192, 0, 16);
    }
    if (grp == 0) { gel2(3, 2 * (kt0 + 5 + he) + wc, 0, 4, NSC - 1, true); mid(); }
    kx[0] = cvk(0, false); kx[1] = cvk(1, false);
    vx[0] = cvv(0, false); vx[1] = cvv(1, false);
#pragma unroll
    for (int mf = 0; mf < 4; ++mf) qx[mf] = cvp(acc[mf][0], acc[mf][1]);
    f32x4 mk0, mk1;
#pragma unroll
    for (int j = 0; j < 4; ++j) {
      const int k0 = 4 * lq + j, k1 = 16 + 4 * lq + j;
      mk0[j] = (k0 >= li + 9 && k0 <= li + 16) ? 0.f : -1.0e30f;
      mk1[j] = (k1 >= li + 9 && k1 <= li + 16) ? 0.f : -1.0e30f;
    }
    auto max3 = [](float a, float b, float c) { float r; asm("v_max3_f32 %0, %1, %2, %3" : "=v"(r) : "v"(a), "v"(b), "v"(c)); return r; };
    auto max2 = [](float a, float b) { float r; asm("v_max_f32 %0, %1, %2" : "=v"(r) : "v"(a), "v"(b)); return r; };
    auto tiles = [&](int t0, const u32x4* kp, const u32x4* vp, bool first_rows) {
      const f32x4 z = {0.f, 0.f, 0.f, 0.f};
      const f32x4 mz = {-1.0e30f, -1.0e30f, -1.0e30f, -1.0e30f};
      f32x4 s0[2], s1[2];
#pragma unroll
      for (int n = 0; n < 2; ++n) {
        const bf16x8 qf = __builtin_bit_cast(bf16x8, qx[t0 + n]);
        s0[n] = __builtin_amdgcn_mfma_f32_16x16x32_bf16(__builtin_bit_cast(bf16x8, kp[n]), qf, first_rows ? mz : mk0, 0, 0, 0);
        s1[n] = __builtin_amdgcn_mfma_f32_16x16x32_bf16(__builtin_bit_cast(bf16x8, kx[t0 + n]), qf, mk1, 0, 0, 0);
      }
      SB0();
      if (grp == 0) { if (t0) wpair(1, 1); else aquad(1); }
      asm volatile("s_nop 7\n\ts_nop 3" : "+v"(s0[0]), "+v"(s1[0]), "+v"(s0[1]), "+v"(s1[1]));
      float mx[2];
#pragma unroll
      for (int n = 0; n < 2; ++n) mx[n] = max3(max3(s0[n][0], s0[n][1], s0[n][2]), max3(s0[n][3], s1[n][0], s1[n][1]), max2(s1[n][2], s1[n][3]));
#pragma unroll
      for (int n = 0; n < 2; ++n) { auto r = __builtin_amdgcn_permlane16_swap(__float_as_uint(mx[n]), __float_as_uint(mx[n]), false, false);
        mx[n] = max2(__uint_as_float(r[0]), __uint_as_float(r[1])); }
#pragma unroll
      for (int n = 0; n < 2; ++n) { auto r = __builtin_amdgcn_permlane32_swap(__float_as_uint(mx[n]), __float_as_uint(mx[n]), false, false);
        mx[n] = max2(__uint_as_float(r[0]), __uint_as_float(r[1])); }
      SB0();
      if (grp == 0) {
        if (t0) wpair(2, 0); else aquad(2);
        snapshot1();
      }
      constexpr float cs2 = 0.17677669529663687f * 1.4426950408889634f;
      f32x4 os[2], o0[2], o1[2];
#pragma unroll
      for (int n = 0; n < 2; ++n) {
        const float c = cs2 * rd[t0 + n], nm = -mx[n] * c;
        float e8[8];
#pragma unroll
        for (int j = 0; j < 4; ++j) {
          e8[j] = __builtin_amdgcn_exp2f(__builtin_fmaf(s0[n][j], c, nm));
          e8[4 + j] = __builtin_amdgcn_exp2f(__builtin_fmaf(s1[n][j], c, nm));
        }
        u32x4 pk;
#pragma unroll
        for (int j = 0; j < 4; ++j) pk[j] = pack2(e8[2 * j], e8[2 * j + 1]);
        const bf16x8 pf = __builtin_bit_cast(bf16x8, pk);
        const u32x4 va = {vp[n][0], vp[n][1], vx[t0 + n][0], vx[t0 + n][1]}, vb = {vp[n][2], vp[n][3], vx[t0 + n][2], vx[t0 + n][3]};
        os[n] = __builtin_amdgcn_mfma_f32_16x16x32_bf16(ones, pf, z, 0, 0, 0);
        o0[n] = __builtin_amdgcn_mfma_f32_16x16x32_bf16(__builtin_bit_cast(bf16x8, va), pf, z, 0, 0, 0);
        o1[n] = __builtin_amdgcn_mfma_f32_16x16x32_bf16(__builtin_bit_cast(bf16x8, vb), pf, z, 0, 0, 0);
      }
      if (grp == 0 && t0) { SB0(); wpair(2, 1); }
#pragma unroll
      for (int n = 0; n < 2; ++n) {
        const float inv = __builtin_amdgcn_rcpf(os[n][0]);
        const f32x4 n0 = o0[n] * inv, n1 = o1[n] * inv;
        putf(112 + ntile + wc, t0 + n, u32x4{pack2(n0[0], n0[1]), pack2(n0[2], n0[3]), pack2(n1[0], n1[1]), pack2(n1[2], n1[3])});
      }
    };
    if (grp == 0) wpair(1, 0);
    tiles(2, &kx[0], &vx[0], false);
    constexpr int HL = 131072, HS = DMP + 1024;
    auto hload = [&]() {
      asm volatile("s_mov_b32 m0, %2\n\ts_nop 0\n\tglobal_load_lds_dwordx4 %0, %1 offset:-4096 sc1\n\tglobal_load_lds_dwordx4 %0, %1 offset:-3072 sc1\n\t"
                   "global_load_lds_dwordx4 %0, %1 offset:-2048 sc1\n\tglobal_load_lds_dwordx4 %0, %1 offset:-1024 sc1\n\tglobal_load_lds_dwordx4 %0, %1 sc1\n\t"
                   "global_load_lds_dwordx4 %0, %1 offset:1024 sc1\n\tglobal_load_lds_dwordx4 %0, %1 offset:2048 sc1\n\tglobal_load_lds_dwordx4 %0, %1 offset:3072 sc1\n\t"
                   "s_mov_b32 m0, %4\n\ts_nop 0\n\tglobal_load_lds_dwordx4 %0, %3 sc1"
                   :: "v"(ln * 16), "s"(hptr + 4096), "s"(shm_base + HL + wc * 8192 + 4096), "s"(hptr + 8192), "s"(shm_base + HS + wc * 2048) : "memory", "m0");
    };
    if (needh) { await(0xffffu, 0x404u); hload(); }
    if (grp) { __builtin_amdgcn_s_setprio(1); gel2(3, 2 * (kt0 + 5 + he) + wc, 0, 2, 0, true); __builtin_amdgcn_s_setprio(3); }
    else {
      WAIT_V(4);
      peek1();
      aquad(0);
    }
    if (!grp || wm > 0) {
      await(0x8000u << vid, 0x8000u << vid);
#pragma unroll
      for (int i = 0; i < 4; ++i) hx[i] = *(const u32x4*)(shm + (vid - wc - 1) * 4096 + i * 1024 + ln * 16);
    } else if (!needh) {
#pragma unroll
      for (int i = 0; i < 4; ++i) hx[i] = u32x4{0u, 0u, 0u, 0u};
    } else {
      bool late = false;
      WAIT_V(2);
      for (;;) {
        const char* const hl = shm + HL + wc * 8192 + ln * 16;
        const u32x4 sp = *(const u32x4*)(shm + HS + wc * 2048 + ln * 16);
        unsigned df = 0;
#pragma unroll
        for (int i = 0; i < 4; ++i) {
          const u32x4 g0 = *(const u32x4*)(hl + i * 2048), g1 = *(const u32x4*)(hl + i * 2048 + 1024);
          df |= (g0[1] ^ sp[0]) | (g0[3] ^ sp[0]) | (g1[1] ^ sp[0]) | (g1[3] ^ sp[0]);
          hx[i] = u32x4{g0[0], g0[2], g1[0], g1[2]};
        }
        if (__builtin_expect(__all((df == 0) & fresh(((u64)sp[1] << 32) | sp[0], t_own)), 1)) break;
        late = __builtin_amdgcn_s_memrealtime() - t_own > TIMEOUT_;
        if (late) break;
        __builtin_amdgcn_s_sleep(1);
        WAIT_L(0);
        hload();
        WAIT_V(0);
      }
      if (__builtin_expect(late, 0)) {
        char* const fb = shm + HL + wc * 8192 + ln * 16;
#pragma unroll 1
        for (int i = 0; i < 32; ++i) {
          const int rec = i >> 3, e = i & 7;
          const int row = m0 - 32 + 2 * (rec < 2 ? li : lq * 4 + (e & 3)) + (rec & 1);
          const int col = (rec < 2 ? D_ + lq * 4 + (e & 3) : 2 * D_ + li) + (ntile + wc) * 32 + (e >> 2) * 16;
          const u16* xr = x + (size_t)row * D_;
          const unsigned stt = row_stats_serial(xr);
          const float mb = lo_f(stt);
          float a = 0.f;
#pragma unroll 1
          for (int k = 0; k < D_; ++k) a += (bf2f(xr[k]) - mb) * bf2f(wqkv[(size_t)k * 3072 + col]);
          *(u16*)(fb + rec * 1024 + e * 2) = f2bf(a / hi_f(stt));
        }
#pragma unroll
        for (int i = 0; i < 4; ++i) hx[i] = *(const u32x4*)(fb + i * 1024);
      }
    }
    tiles(0, &hx[0], &hx[2], grp && mtile == 0 && wm == 0);
    if (grp) { __builtin_amdgcn_s_setprio(1); gel2(3, 2 * (kt0 + 5 + he) + wc, 2, 4, 0, true); }
    else WAIT_V(7);
  }
  TS1(2);
  const bool cok = cokw != 0u;
  {
  __builtin_amdgcn_sched_barrier(0);
  int tid = threadIdx.x & 255;
  asm volatile("" : "+v"(tid));
  __builtin_amdgcn_sched_barrier(0);
  const int lane = tid & 63;
  const int wm = (wid >> 1) & 1, wn = wid & 1;
  const int mtile = mt2, ntile = nt2;
  const int m0 = mtile * 128, n0 = ntile * 128;
  TS(0);
  constexpr int EP = 528;
  const int img0 = ((ny + 13) % G2_NSTAGE) * G2_STAGE, img1 = ((ny + 14) % G2_NSTAGE) * G2_STAGE;
  if constexpr (!LD) {
    const int p = wid;
    const u64* const mbox = &done1[bid * 128 + lane];
    auto stage_s = [&]() {
      unsigned t;
      asm volatile(G2_A G2_W : [t] "=&s"(t) : [l] "s"(sl), [oa] "s"((p >> 1) * 8192 + (p & 1) * 2048), [a] "s"(ra2), [va] "v"(va), G2_WOP : "memory", "scc", "m0");
      adv();
    };
    u64 ren = 0;
    auto need = [&]() {
      while (__builtin_expect(!((okb >> q) & 1), 0)) {
        digest(__hip_atomic_load(mbox, RLX_AGENT));
        if (!((okb >> q) & 1)) {
          __builtin_amdgcn_s_sleep(2);
          if (__builtin_expect(!cok, 0)) notify(ren);
          if (GIVEUP()) break;
        }
      }
      asm volatile("" ::: "memory");
    };
    __builtin_amdgcn_s_setprio(0);
    if ((okb & 7) != 7) {
      const unsigned done = (unsigned)okb;
#pragma unroll 1
      for (q = 0; q < 3; ++q)
        if (!((done >> q) & 1)) {
          need();
          aquad(q);
        }
      if (done & 4) WAIT_V(0);
      WAIT_V(4);
    }
    TS(4);
    BARL();
    need();
    stage_y();
    WAIT_V(8);
    peek1();
    if (p == 0) snapshot();
    need();
    stage_y();
    if (p) okb = ~0ull;
    auto check = [&]() {
      if (__builtin_expect(okb != ~0ull && !(q & 1), 0)) {
        peek();
        if (okb != ~0ull) snapshot();
      }
    };
#define G2_RUN(N, A23) "s_branch 2f\n1:\n\tbuffer_load_dwordx4 %[v3], %[w1], %[t2] offen offset:1024 sc1 lds\n2:\n\t" \
      "s_add_u32 m0, %[l], %[oa]\n\ts_lshl_b32 %[t], %[k], 14\n\ts_waitcnt vmcnt(" N ")\n\ts_barrier\n\t" \
      "buffer_load_dwordx4 %[va], %[a], %[t] offen sc1 lds\n\ts_add_u32 %[q], %[q], 1\n\ts_lshl_b32 %[t2], %[k], %[s1]\n\t" \
      "buffer_load_dwordx4 %[va], %[a], %[t] offen offset:1024 sc1 lds\n\t" A23 \
      "s_add_u32 m0, %[l], %[ow]\n\ts_lshl_b32 %[t], %[k], %[s0]\n\t" \
      "buffer_load_dwordx4 %[v0], %[w0], %[t] offen sc1 lds\n\tv_readlane_b32 %[k], %[tab], %[q]\n\ts_add_u32 %[l], %[l], 0x8000\n\t" \
      "buffer_load_dwordx4 %[v1], %[w0], %[t] offen offset:1024 sc1 lds\n\ts_add_u32 m0, m0, 8192\n\ts_cmp_eq_u32 %[l], %[le]\n\ts_cselect_b32 %[l], %[lb], %[l]\n\t" \
      "buffer_load_dwordx4 %[v2], %[w1], %[t2] offen sc1 lds\n\ts_cmp_lt_u32 %[q], %[qe]\n\ts_cbranch_scc1 1b\n\t" \
      "buffer_load_dwordx4 %[v3], %[w1], %[t2] offen offset:1024 sc1 lds"
#define G2_RUNOP : [t] "=&s"(t), [t2] "=&s"(t2), [q] "+&s"(q), [k] "+&s"(kt), [l] "+&s"(sl) \
      : [oa] "s"(oa), [a] "s"(ra2), [va] "v"(va), [ow] "s"(16384 + wid * 2048), [s0] "s"(wsh[0]), [s1] "s"(wsh[1]), [w0] "s"(rw[0]), [w1] "s"(rw[1]), \
        [v0] "v"(vb[0][0]), [v1] "v"(vb[0][1]), [v2] "v"(vb[1][0]), [v3] "v"(vb[1][1]), [tab] "v"(tab), [qe] "s"(qe), \
        [lb] "s"(shm_base), [le] "s"(shm_base + G2_NSTAGE * G2_STAGE) : "memory", "scc", "m0"
    auto sync = [&](bool y) {
      if (y) WAIT_V(16); else WAIT_V(12);
      need();
      BAR();
      check();
    };
    auto upto = [&](bool y, int qe) {
#pragma unroll 1
      while (__builtin_expect(okb != ~0ull && q < qe, 0)) {
        sync(y);
        if (y) stage_y(); else stage_s();
      }
      if (q < qe) {
        unsigned t, t2;
        const int oa = y ? (p ^ role) * 4096 : (p >> 1) * 8192 + (p & 1) * 2048;
        if (y) asm volatile(G2_RUN("16", G2_A2) G2_RUNOP);
        else asm volatile(G2_RUN("12", "") G2_RUNOP);
      }
    };
    sync(true);
    ren = 0xffffffff00000000ull;
    if (p == 1) notify((myh & mall) << 32);
    stage_y();
    upto(true, 16 + 2 * p);
    sync(true);
    const u32x4 rwd[2] = {rw[0], rw[1]};
    const int wshd[2] = {wsh[0], wsh[1]};
    const unsigned vbd[4] = {vb[0][0], vb[0][1], vb[1][0], vb[1][1]};
    wsetup(wo, D_, F_, lane);
    const u32x4 rwo[2] = {rw[0], rw[1]};
    const int wsho[2] = {wsh[0], wsh[1]};
    unsigned vbo[5] = {vb[0][0], vb[0][1], vb[1][0], vb[1][1], (unsigned)(lane * 16 + (p >> 1) * 8192 + role * 4096 + (p & 1) * 2048 + (56 << 14))};
    asm volatile("" :: "s"(rwo[0]), "s"(rwo[1]), "s"(wsho[0]), "s"(wsho[1]));
    asm volatile("" : "+v"(vbo[0]), "+v"(vbo[1]), "+v"(vbo[2]), "+v"(vbo[3]), "+v"(vbo[4]));
    rw[0] = rwd[0]; rw[1] = rwd[1]; wsh[0] = wshd[0]; wsh[1] = wshd[1];
    vb[0][0] = vbd[0]; vb[0][1] = vbd[1]; vb[1][0] = vbd[2]; vb[1][1] = vbd[3];
    const char* ps = (const char*)slab + (size_t)(bid ^ 5) * 16384 + p * 4096;
    asm volatile("" : "+s"(ps));
    const u16* xp = x + (size_t)(m0 + role * 32) * D_ + n0;
    u16* op = out + (size_t)(m0 + role * 32) * D_ + n0;
    unsigned vo = ((tid >> 4) * 2 * D_ + (tid & 15) * 8) * 2, vi0 = img0 + (tid >> 4) * EP + (tid & 15) * 32, vi1 = vi0 + (img1 - img0);
    unsigned vo2 = vo + 64 * D_ * 2;
    asm volatile("" : "+s"(xp));
    asm volatile("" : "+s"(op));
    asm volatile("" : "+v"(vo), "+v"(vo2), "+v"(vi0), "+v"(vi1));
    stage_y();
    upto(true, ny);
    va = vbo[4];
    rw[0] = rwo[0]; rw[1] = rwo[1]; wsh[0] = wsho[0]; wsh[1] = wsho[1];
    vb[0][0] = vbo[0]; vb[0][1] = vbo[1]; vb[1][0] = vbo[2]; vb[1][1] = vbo[3];
    bool pok = true;
    upto(false, ny + (p == 0 ? 13 : (p == 1 ? 10 : 16)));
    if (p < 2) {
      sync(false);
      if (p == 1) {
        if (lane == 0) __hip_atomic_store(done2 + bid * 128, __builtin_amdgcn_s_memrealtime(), RLX_AGENT);
      } else {
        q += 2;
        need();
        --q;
        need();
        --q;
        aload(done2 + (bid ^ 5) * 128, 0);
      }
      stage_s();
      upto(false, ny + 15);
      WAIT_V(12);
      BAR();
      if (p == 0) pok = __all(fresh(aget(), t_own));
      stage_s();
    }
    WAIT_V(12);
    while (__builtin_expect(!pok, 0)) {
      aload(done2 + (bid ^ 5) * 128, 0);
      WAIT_V(0);
      pok = __all(fresh(aget(), t_own)) || GIVEUP();
      if (__builtin_expect(!cok, 0)) { notify(ren); if (lane == 0) __hip_atomic_store(done2 + bid * 128, __builtin_amdgcn_s_memrealtime(), RLX_AGENT); }
    }
    SB0();
    BAR();
    asm volatile("s_mov_b32 m0, %2\n\ts_nop 0\n\tglobal_load_lds_dwordx4 %0, %1 sc1\n\tglobal_load_lds_dwordx4 %0, %1 offset:1024 sc1\n\t"
                 "global_load_lds_dwordx4 %0, %1 offset:2048 sc1\n\tglobal_load_lds_dwordx4 %0, %1 offset:3072 sc1"
                 :: "v"(lane * 16), "s"(ps), "s"(sl + p * 4096) : "memory", "m0");
    SB0();
    WAIT_V(10);
    BAR();
    const __amdgpu_buffer_rsrc_t rx = __builtin_amdgcn_make_buffer_rsrc((void*)xp, 0, 96 * D_ * 2, 0x00020000);
    const __amdgpu_buffer_rsrc_t ro = __builtin_amdgcn_make_buffer_rsrc((void*)op, 0, 96 * D_ * 2, 0x00020000);
    u32x4 xv[4];
#pragma unroll
    for (int i = 0; i < 4; ++i) xv[i] = __builtin_bit_cast(u32x4, __builtin_amdgcn_raw_buffer_load_b128(rx, ((i & 1) ? vo2 : vo) + (i >> 1) * D_ * 2, 0, 0));
    WAIT_V(8);
    BAR();
    WAIT_V(4);
    BAR();
    f32x4 xf[8];
#pragma unroll
    for (int i = 0; i < 8; ++i) {
      const unsigned a = xv[i >> 1][(i & 1) * 2], b = xv[i >> 1][(i & 1) * 2 + 1];
      xf[i] = f32x4{lo_f(a), hi_f(a), lo_f(b), hi_f(b)};
      asm volatile("" : "+v"(xf[i]));
    }
#pragma unroll
    for (int h = 0; h < 2; ++h) {
      BAR();
      f32x4 t[4];
#pragma unroll
      for (int i = 0; i < 4; ++i) t[i] = *(const f32x4*)(shm + (h ? vi1 : vi0) + (i >> 1) * 16 * EP + (i & 1) * 16) + xf[h * 4 + i];
#pragma unroll
      for (int i = 0; i < 2; ++i)
        __builtin_amdgcn_raw_buffer_store_b128(u32x4{pack2(t[2 * i][0], t[2 * i][1]), pack2(t[2 * i][2], t[2 * i][3]), pack2(t[2 * i + 1][0], t[2 * i + 1][1]),
                                                     pack2(t[2 * i + 1][2], t[2 * i + 1][3])}, ro, (i ? vo2 : vo) + h * D_ * 2, 0, 2);
    }
    TS(5);
    if (tid == 0) __hip_atomic_store(&acks[bid], __builtin_amdgcn_s_memrealtime(), RLX_AGENT);
    if (__builtin_expect(!cok, 0) && p == 0) {
      const bool mine = lane <= 32;
      const u64* const pa = &acks[lane < 32 ? client(lane) : bid ^ 5];
      while (!__all(!mine || fresh(__hip_atomic_load(pa, RLX_AGENT), t_own))) {
        notify(ren);
        if (lane == 0) __hip_atomic_store(done2 + bid * 128, __builtin_amdgcn_s_memrealtime(), RLX_AGENT);
        __builtin_amdgcn_s_sleep(32);
        if (GIVEUP()) break;
      }
    }
  } else {
    TS(6);
    f32x4 acc[4][4], sacc[2][4];
    const int fr = lane & 15, fq = lane >> 4, q4 = fr >> 2, pp = lane & 3;
    const int ff = ((q4 >> 1) & 1) | ((fq & 1) << 1);
    int aof = G2_STAGE + wm * 8192 + lane * 16;
    int boff[4];
#pragma unroll
    for (int j = 0; j < 4; ++j) boff[j] = G2_STAGE + 16384 + wn * 8192 + (8 * fq + q4) * 128 + ((j ^ ff) * 32) + pp * 8;
#pragma unroll
    for (int b = 0; b < 4; ++b) {
#pragma unroll
      for (int a = 0; a < 4; ++a) acc[a][b] = f32x4{0.f, 0.f, 0.f, 0.f};
      sacc[0][b] = f32x4{0.f, 0.f, 0.f, 0.f}; sacc[1][b] = f32x4{0.f, 0.f, 0.f, 0.f};
    }
#define SB() __builtin_amdgcn_sched_barrier(0)
    struct Fr { bf16x8 a[4]; bf16x4 bl[4], bh[4]; };
    Fr f0[2], f1[2];
    auto rd = [&](Fr& f, lds_cptr sb, int ks, int t, bool sp) {
      constexpr int ord[12] = {0, 1, 8, 2, 3, 4, 5, 6, 7, 9, 10, 11};
      const int o = ord[t];
      if (o >= 8) f.a[o - 8] = *(const __attribute__((address_space(3))) bf16x8*)(sb + aof + (o - 8) * 2048 + ks * 1024);
      else if (o & 1) f.bh[o >> 1] = tr_read(sb + boff[o >> 1] + ks * 4096 + 512);
      else f.bl[o >> 1] = tr_read(sb + boff[o >> 1] + ks * 4096);
    };
    auto bfr = [&](const Fr& c, int j) { return __builtin_shufflevector(c.bl[j], c.bh[j], 0, 1, 2, 3, 4, 5, 6, 7); };
    int so = G2_STAGE;
    auto ystg = [&](const Fr* c, Fr* n, bool last) {
      int d = 0;
      sfor<32>([&](auto uc) __attribute__((always_inline)) {
        constexpr int u = decltype(uc)::value, t = u >> 1, mf = t >> 2, j = (t & 4) ? 3 - (t & 3) : t & 3, ks = (u ^ t) & 1;
        SB();
        asm volatile("v_mfma_f32_16x16x32_bf16 %0, %1, %2, %0" : "+v"(acc[mf][j]) : "v"(bfr(c[ks], j)), "v"(c[ks].a[mf]));
        SB();
        const int h = last ? 10 : 12;
        if (u < 2 * h) rd(n[u / h], shm3, u / h, u % h, true);
        if (u == 24) { d = so == (G2_NSTAGE - 1) * G2_STAGE ? -(G2_NSTAGE - 1) * G2_STAGE : G2_STAGE; asm volatile("" : "+s"(d)); }
        if (u == 25) { so += d; aof += d; asm volatile("" : "+v"(aof)); asm volatile("" : "+s"(so)); }
        if constexpr (u >= 26 && u < 30) { boff[u - 26] += d; asm volatile("" : "+v"(boff[u - 26])); }
      });
      SB();
      __builtin_amdgcn_s_waitcnt(0xC07F);
    };
    auto stg = [&](const Fr* c, Fr* n) {
      int d = 0;
      sfor<16>([&](auto uc) __attribute__((always_inline)) {
        constexpr int u = decltype(uc)::value, t = u >> 1, mf = t >> 2, j = (t & 4) ? 3 - (t & 3) : t & 3, ks = (u ^ t) & 1;
        SB();
        asm volatile("v_mfma_f32_16x16x32_bf16 %0, %1, %2, %0" : "+v"(sacc[mf][j]) : "v"(bfr(c[ks], j)), "v"(c[ks].a[mf]));
        SB();
        if (u < 10) { rd(n[0], shm3, 0, u, true); rd(n[1], shm3, 1, u, true); }
        if (u == 10) { d = so == (G2_NSTAGE - 1) * G2_STAGE ? -(G2_NSTAGE - 1) * G2_STAGE : G2_STAGE; asm volatile("" : "+s"(d)); }
        if (u == 11) { so += d; aof += d; asm volatile("" : "+v"(aof)); asm volatile("" : "+s"(so)); }
        if (u == 12) { boff[0] += d; boff[1] += d; asm volatile("" : "+v"(boff[0]), "+v"(boff[1])); }
        if (u == 13) { boff[2] += d; boff[3] += d; asm volatile("" : "+v"(boff[2]), "+v"(boff[3])); }
      });
      SB();
      __builtin_amdgcn_s_waitcnt(0xC07F);
    };
    asm volatile("" : "+v"(aof), "+v"(boff[0]), "+v"(boff[1]), "+v"(boff[2]), "+v"(boff[3]));
    asm volatile("" : "+v"(sacc[0][0]), "+v"(sacc[0][1]), "+v"(sacc[0][2]), "+v"(sacc[0][3]), "+v"(sacc[1][0]), "+v"(sacc[1][1]), "+v"(sacc[1][2]), "+v"(sacc[1][3]));
#pragma unroll
    for (int a = 0; a < 4; ++a) asm volatile("" : "+v"(acc[a][0]), "+v"(acc[a][1]), "+v"(acc[a][2]), "+v"(acc[a][3]));
    WAIT_V(0);
    arrive(std::integral_constant<int, 1>{});
    BARL();
    TS(7);
    __builtin_amdgcn_s_waitcnt(0xC07F);
    SB();
#pragma unroll
    for (int t = 0; t < 24; ++t) rd(f0[t & 1], shm3, t & 1, t >> 1, false);
    auto nxt = [&]() {
      const int d = so == (G2_NSTAGE - 1) * G2_STAGE ? -(G2_NSTAGE - 1) * G2_STAGE : G2_STAGE;
      so += d; aof += d; boff[0] += d; boff[1] += d; boff[2] += d; boff[3] += d;
      asm volatile("" : "+v"(aof), "+v"(boff[0]), "+v"(boff[1]), "+v"(boff[2]), "+v"(boff[3]));
    };
    nxt();
    __builtin_amdgcn_s_waitcnt(0xC07F);
#pragma unroll 1
    for (int g = 0; g < ny - 2; g += 2) {
      ystg(f0, f1, false);
      BAR();
      ystg(f1, f0, false);
      BAR();
    }
    if (!(DLT & 1)) { ystg(f0, f1, false); BAR(); }
    Fr sa[2], sb2[2];
    ystg((DLT & 1) ? f0 : f1, sa, true);
    asm volatile("" : "+v"(acc[0][0]), "+v"(acc[0][1]), "+v"(acc[0][2]), "+v"(acc[0][3]), "+v"(acc[1][0]), "+v"(acc[1][1]), "+v"(acc[1][2]), "+v"(acc[1][3]));
    TS(2);
    {
      const __amdgpu_buffer_rsrc_t rs = __builtin_amdgcn_make_buffer_rsrc((void*)((char*)slab + (size_t)bid * 16384), 0, 16384, 0x00020000);
#pragma unroll
      for (int f = 0; f < 4; ++f) {
        BAR();
        if (f & 1) stg(sb2, sa); else stg(sa, sb2);
        asm volatile("" : "+v"(acc[2 + (f >> 1)][(f & 1) * 2]), "+v"(acc[2 + (f >> 1)][(f & 1) * 2 + 1]));
        const f32x4 a = acc[2 + (f >> 1)][(f & 1) * 2], b = acc[2 + (f >> 1)][(f & 1) * 2 + 1];
        __builtin_amdgcn_raw_buffer_store_b128(u32x4{pack2(a[0], a[1]), pack2(a[2], a[3]), pack2(b[0], b[1]), pack2(b[2], b[3])}, rs,
                                               (f * 256 + tid) * 16, 0, 16);
        SB();
      }
    }
    int vi0 = img0 + (wm * 16 + fr) * EP + wn * 256 + fq * 16, vi1 = vi0 + (img1 - img0);
    asm volatile("" : "+v"(vi0), "+v"(vi1));
#pragma unroll 1
    for (int i = 2; i < 7; ++i) {
      BAR();
      stg(sa, sb2);
      BAR();
      stg(sb2, sa);
      WAIT_V(0);
    }
    BAR();
    stg(sa, sb2);
    BAR();
    u32x4 pv[4];
    {
      lds_cptr sb = shm3 + so + tid * 16;
#pragma unroll
      for (int f = 0; f < 4; ++f) pv[f] = *(const __attribute__((address_space(3))) u32x4*)(sb + f * 4096);
    }
    f32x4 pf[8];
    sfor<16>([&](auto uc) __attribute__((always_inline)) {
      constexpr int u = decltype(uc)::value, t = u >> 1, mf = t >> 2, j = (t & 4) ? 3 - (t & 3) : t & 3, ks = (u ^ t) & 1;
      SB();
      asm volatile("v_mfma_f32_16x16x32_bf16 %0, %1, %2, %0" : "+v"(sacc[mf][j]) : "v"(bfr(sb2[ks], j)), "v"(sb2[ks].a[mf]));
      SB();
      if constexpr (u >= 8) {
        const unsigned w0 = pv[(u - 8) >> 1][(u & 1) * 2], w1 = pv[(u - 8) >> 1][(u & 1) * 2 + 1];
        pf[u - 8] = f32x4{lo_f(w0), hi_f(w0), lo_f(w1), hi_f(w1)};
        asm volatile("" : "+v"(pf[u - 8]));
      }
    });
    SB();
    TS(1);
#pragma unroll
    for (int mf = 0; mf < 2; ++mf) {
      f32x4 ya[4], e[4];
#pragma unroll
      for (int j = 0; j < 4; ++j) { ya[j] = acc[mf][j] + pf[mf * 4 + j]; e[j] = ya[j] * -1.4426950408889634f; }
      SB();
#pragma unroll
      for (int j = 0; j < 4; ++j) {
        e[j] = f32x4{__builtin_amdgcn_exp2f(e[j][0]), __builtin_amdgcn_exp2f(e[j][1]), __builtin_amdgcn_exp2f(e[j][2]), __builtin_amdgcn_exp2f(e[j][3])};
        ya[j] *= sacc[mf][j];
      }
      SB();
#pragma unroll
      for (int j = 0; j < 4; ++j) e[j] += 1.0f;
      SB();
#pragma unroll
      for (int j = 0; j < 4; ++j)
        *(f32x4*)(shm + (mf ? vi1 : vi0) + j * 64) =
            ya[j] * f32x4{__builtin_amdgcn_rcpf(e[j][0]), __builtin_amdgcn_rcpf(e[j][1]), __builtin_amdgcn_rcpf(e[j][2]), __builtin_amdgcn_rcpf(e[j][3])};
      SB();
      BARL();
      SB();
    }
    TS(3);
  }
  TSFLUSH();
  }
  };
  if (wid < 4) kloop(std::integral_constant<int, 0>{});
  else kloop(std::integral_constant<int, 1>{});
  asm volatile(".Lcode_end:");
}

extern "C" void kernel_launch(void* const* d_in, const int* in_sizes, int n_in, void* d_out, int out_size, void* d_ws,
                              size_t ws_size, hipStream_t stream) {
  const u16* A_x = (const u16*)d_in[0];
  const u16* A_wqkv = (const u16*)d_in[1];
  const u16* A_wo = (const u16*)d_in[2];
  const u16* B_wg = (const u16*)d_in[4];
  const u16* B_wu = (const u16*)d_in[5];
  const u16* B_wd = (const u16*)d_in[6];
  u16* out = (u16*)d_out;
  fused_kernel<<<256, 512, 0, stream>>>(A_x, A_wqkv, B_wg, B_wu, B_wd, A_wo, out, (char*)d_ws);
}
```
